```python
import torch

def _softmax(x, axis=-1):
    return torch.softmax(x, dim=axis)
def _gelu(x):
    return torch.nn.functional.gelu(x, approximate="tanh")
def _silu(x):
    return torch.nn.functional.silu(x)
def _sigmoid(x):
    return torch.sigmoid(x)
def _layernorm(x, axis=-1, eps=1e-5):
    m = x.mean(dim=axis, keepdim=True)
    v = x.var(dim=axis, keepdim=True, unbiased=False)
    return (x - m) / torch.sqrt(v + eps)
def _scatter_add(tgt, idx, src, axis=0):
    return tgt.clone().index_add_(axis, idx, src)
def _segment_sum(src, seg, n):
    out = torch.zeros((n,) + src.shape[1:], dtype=src.dtype)
    return out.index_add_(0, seg, src)
def _pad(x, pw):
    flat = tuple(v for pair in reversed(pw) for v in pair)
    return torch.nn.functional.pad(x, flat)

kernel_name = 'gated_mix__sliding_block__mixer_channel_mlp__prompt'

def setup_inputs():

    _w = (lambda a: (lambda t: t.to(torch.bfloat16) if t.is_floating_point() else t)(torch.as_tensor(a)))
    def _A_setup():
        B, S, D = 1, 2048, 576
        return {
            "x": _w((lambda *s: torch.randn(*s, dtype=torch.bfloat16) * 0.05)(B, S, D)),
            "wqkv": _w((lambda *s: torch.randn(*s, dtype=torch.bfloat16) * 0.8)(D, 3 * D)),
            "wo": _w((lambda *s: torch.randn(*s, dtype=torch.bfloat16) * 0.05)(D, D)),
        }
    _A_in = _A_setup()

    def _B_setup():
        B, S, D, F = 1, 2048, 576, 4608
        return {
            "x": _w((lambda *s: torch.randn(*s, dtype=torch.bfloat16) * 0.05)(B, S, D)),
            "w1": _w((lambda *s: torch.randn(*s, dtype=torch.bfloat16) * 3.2)(D, F) * (D ** -0.5)),
            "b1": _w((lambda *s: torch.randn(*s, dtype=torch.bfloat16) * 1.6)(F)),
            "w2": _w((lambda *s: torch.randn(*s, dtype=torch.bfloat16) * 0.1)(F, D) * (F ** -0.5)),
            "b2": _w((lambda *s: torch.randn(*s, dtype=torch.bfloat16) * 0.2)(D)),
        }
    _B_in = _B_setup()

    return {
        "A_x": _A_in["x"],
        "A_wqkv": _A_in["wqkv"],
        "A_wo": _A_in["wo"],
        "B_x": _B_in["x"],
        "B_w1": _B_in["w1"],
        "B_b1": _B_in["b1"],
        "B_w2": _B_in["w2"],
        "B_b2": _B_in["b2"],
        "gate_w": _w((lambda *s: torch.randn(*s, dtype=torch.bfloat16) * 0.05)(576) * 16.0),
    }

def reference(A_x, A_wqkv, A_wo, B_x, B_w1, B_b1, B_w2, B_b2, gate_w):
    def _A_ref(x, wqkv, wo):
        B, S, D = x.shape
        H = 32; Dh = D // H; W = 256
        qkv = (lambda x, *d: x.permute(*d))((x @ wqkv).reshape(B, S, 3, H, Dh), 2, 0, 3, 1, 4)
        q, k, v = qkv[0], qkv[1], qkv[2]
        scores = torch.einsum("bhqd,bhkd->bhqk", q, k) * (Dh ** -0.5)
        idx = torch.arange(S)
        mask = (idx[:, None] - idx[None, :] >= 0) & (idx[:, None] - idx[None, :] < W)
        scores = torch.where(mask[None, None], scores, -1e9)
        p = _softmax(scores, axis=-1)
        o = torch.einsum("bhqk,bhkd->bhqd", p, v)
        return (lambda x, *d: x.permute(*d))(o, 0, 2, 1, 3).reshape(B, S, D) @ wo

    def _B_ref(x, w1, b1, w2, b2):
        y = _layernorm(x, axis=-1)
        h = _gelu(torch.einsum("bsd,df->bsf", y, w1) + b1)
        return torch.einsum("bsf,fd->bsd", h, w2) + b2

    x = A_x
    g = _sigmoid((x * gate_w).sum(-1, keepdims=True))
    a = _A_ref(x, A_wqkv, A_wo)
    b = _B_ref(x, B_w1, B_b1, B_w2, B_b2)
    return g * a + (1 - g) * b
```

```cpp
#include <hip/hip_runtime.h>
#include <hip/hip_bf16.h>
typedef unsigned short u16;
__device__ inline float bf2f(u16 b){ return __uint_as_float(((unsigned)b)<<16); }
__device__ inline u16 f2bf(float f){ unsigned u=__float_as_uint(f); u += 0x7FFFu + ((u>>16)&1u); return (u16)(u>>16); }
constexpr int S=2048, D=576, H=32, DH=18, W=256, F=4608;

__global__ void gemm_naive(const u16* A, const u16* B, const u16* bias, u16* C, float* Cf, int M, int N, int K, int mode){
  __shared__ float sA[16][17], sB[16][17];
  int tx=threadIdx.x, ty=threadIdx.y;
  int m=blockIdx.y*16+ty, n=blockIdx.x*16+tx;
  float acc=0.f;
  for(int k0=0;k0<K;k0+=16){
    sA[ty][tx]=bf2f(A[(size_t)m*K+k0+tx]);
    sB[ty][tx]=bf2f(B[(size_t)(k0+ty)*N+n]);
    __syncthreads();
    for(int k=0;k<16;k++) acc+=sA[ty][k]*sB[k][tx];
    __syncthreads();
  }
  if(mode==0){ C[(size_t)m*N+n]=f2bf(acc); }
  else if(mode==1){ float x=acc+bf2f(bias[n]); float u=0.7978845608028654f*(x+0.044715f*x*x*x); float y=0.5f*x*(1.f+tanhf(u)); C[(size_t)m*N+n]=f2bf(y);}
  else { Cf[(size_t)m*N+n]=acc; }
}
__global__ void ln_gate(const u16* Bx, const u16* Ax, const u16* gw, u16* y, float* g){
  int row=blockIdx.x; int t=threadIdx.x;
  float s=0,s2=0,gs=0;
  for(int i=t;i<D;i+=64){ float v=bf2f(Bx[row*D+i]); s+=v; gs+=bf2f(Ax[row*D+i])*bf2f(gw[i]); }
  for(int o=32;o>=1;o>>=1){ s+=__shfl_xor(s,o); gs+=__shfl_xor(gs,o);}
  float m=s/D;
  for(int i=t;i<D;i+=64){ float v=bf2f(Bx[row*D+i])-m; s2+=v*v; }
  for(int o=32;o>=1;o>>=1) s2+=__shfl_xor(s2,o);
  float r=1.f/sqrtf(s2/D+1e-5f);
  for(int i=t;i<D;i+=64){ y[row*D+i]=f2bf((bf2f(Bx[row*D+i])-m)*r); }
  if(t==0) g[row]=1.f/(1.f+__expf(-gs));
}
__global__ void attn_naive(const u16* qkv, u16* o){
  int q=blockIdx.x, h=blockIdx.y; int t=threadIdx.x;
  __shared__ float p[256]; __shared__ float red[256];
  int key=q-t; float s=-1e30f;
  if(key>=0){ s=0; for(int d=0;d<DH;d++) s+=bf2f(qkv[(size_t)q*3*D+h*DH+d])*bf2f(qkv[(size_t)key*3*D+D+h*DH+d]); s*=0.23570226039551584f; }
  red[t]=s; __syncthreads();
  for(int o2=128;o2>=1;o2>>=1){ if(t<o2) red[t]=fmaxf(red[t],red[t+o2]); __syncthreads(); }
  float mx=red[0]; __syncthreads();
  float e= key>=0? __expf(s-mx):0.f; p[t]=e; red[t]=e; __syncthreads();
  for(int o2=128;o2>=1;o2>>=1){ if(t<o2) red[t]+=red[t+o2]; __syncthreads(); }
  float l=red[0];
  if(t<DH){ float a=0; for(int j=0;j<256;j++){ int kk=q-j; if(kk>=0) a+=p[j]*bf2f(qkv[(size_t)kk*3*D+2*D+h*DH+t]); } o[(size_t)q*D+h*DH+t]=f2bf(a/l); }
}
__global__ void final_k(const float* a, const float* b, const u16* b2, const float* g, u16* out){
  int i=blockIdx.x*256+threadIdx.x; int row=i/D, c=i%D; float gg=g[row];
  out[i]=f2bf(gg*a[i]+(1.f-gg)*(b[i]+bf2f(b2[c])));
}
extern "C" void kernel_launch(void* const* d_in, const int* in_sizes, int n_in, void* d_out, int out_size, void* d_ws, size_t ws_size, hipStream_t stream){
  const u16* Ax=(const u16*)d_in[0]; const u16* wqkv=(const u16*)d_in[1]; const u16* wo=(const u16*)d_in[2];
  const u16* Bx=(const u16*)d_in[3]; const u16* w1=(const u16*)d_in[4]; const u16* b1=(const u16*)d_in[5];
  const u16* w2=(const u16*)d_in[6]; const u16* b2=(const u16*)d_in[7]; const u16* gw=(const u16*)d_in[8];
  char* ws=(char*)d_ws;
  u16* qkv=(u16*)ws; ws+= (size_t)S*3*D*2;
  u16* y=(u16*)ws; ws+=(size_t)S*D*2;
  u16* o=(u16*)ws; ws+=(size_t)S*D*2;
  u16* h=(u16*)ws; ws+=(size_t)S*F*2;
  float* a=(float*)ws; ws+=(size_t)S*D*4;
  float* b=(float*)ws; ws+=(size_t)S*D*4;
  float* g=(float*)ws;
  dim3 blk(16,16);
  gemm_naive<<<dim3(3*D/16,S/16),blk,0,stream>>>(Ax,wqkv,nullptr,qkv,nullptr,S,3*D,D,0);
  ln_gate<<<S,64,0,stream>>>(Ax,Ax,gw,y,g);
  attn_naive<<<dim3(S,H),256,0,stream>>>(qkv,o);
  gemm_naive<<<dim3(F/16,S/16),blk,0,stream>>>(y,w1,b1,h,nullptr,S,F,D,1);
  gemm_naive<<<dim3(D/16,S/16),blk,0,stream>>>(o,wo,nullptr,nullptr,a,S,D,D,2);
  gemm_naive<<<dim3(D/16,S/16),blk,0,stream>>>(h,w2,nullptr,nullptr,b,S,D,F,2);
  final_k<<<S*D/256,256,0,stream>>>(a,b,b2,g,(u16*)d_out);
}
```

```cpp
#include <hip/hip_runtime.h>
#include <hip/hip_bf16.h>
typedef unsigned short u16;
typedef unsigned int u32;
#define LAS __attribute__((address_space(3)))
typedef short bf16x8 __attribute__((ext_vector_type(8)));
typedef short s16x4 __attribute__((ext_vector_type(4)));
typedef float f32x16 __attribute__((ext_vector_type(16)));
typedef float f32x4 __attribute__((ext_vector_type(4)));
typedef u32 u32x4 __attribute__((ext_vector_type(4)));
typedef u32 u32x2 __attribute__((ext_vector_type(2)));
typedef LAS char* lptr;
typedef LAS const char* lcptr;

constexpr int S_ = 2048, D_ = 576, H_ = 32, DH_ = 18, F_ = 4608, NQKV_ = 1728;

__device__ __forceinline__ float bf2f(u16 b) { return __uint_as_float(((u32)b) << 16); }
__device__ __forceinline__ float bflo(u32 w) { return __uint_as_float(w << 16); }
__device__ __forceinline__ float bfhi(u32 w) { return __uint_as_float(w & 0xffff0000u); }
typedef __bf16 bf16x2_t __attribute__((ext_vector_type(2)));
typedef float f32x2 __attribute__((ext_vector_type(2)));
__device__ __forceinline__ u32 cvtpk_c(float lo, float hi) { return __builtin_bit_cast(u32, __builtin_convertvector((f32x2){lo, hi}, bf16x2_t)); }
__device__ __forceinline__ u32 cvtpk(float lo, float hi) { u32 r; asm("v_cvt_pk_bf16_f32 %0, %1, %2" : "=v"(r) : "v"(lo), "v"(hi)); return r; }
__device__ __forceinline__ u16 f2bf(float f) { return (u16)(cvtpk(f, f) & 0xffffu); }
__device__ __forceinline__ void glds16(const void* g, u32 lds_base) {
    u32 sv;
    asm volatile("s_mov_b32 %0, m0\n\ts_mov_b32 m0, %2\n\ts_nop 0\n\tglobal_load_lds_dwordx4 %1, off\n\ts_mov_b32 m0, %0" : "=&s"(sv) : "v"(g), "s"(lds_base) : "memory");
}
__device__ __forceinline__ s16x4 trd(lcptr p) {
    typedef short v4i16_t __attribute__((ext_vector_type(4)));
    return __builtin_bit_cast(s16x4, __builtin_amdgcn_ds_read_tr16_b64_v4i16((LAS v4i16_t*)p));
}
__device__ __forceinline__ bf16x8 cat8(s16x4 a, s16x4 b) { return (bf16x8){a[0], a[1], a[2], a[3], b[0], b[1], b[2], b[3]}; }
#define MFMA32(a, b, c) __builtin_amdgcn_mfma_f32_32x32x16_bf16(a, b, c, 0, 0, 0)
#define MFMA16(a, b, c) __builtin_amdgcn_mfma_f32_16x16x32_bf16(a, b, c, 0, 0, 0)
#define SBAR() __builtin_amdgcn_sched_barrier(0)
#define RFL(x) __builtin_amdgcn_readfirstlane(x)
__global__ __launch_bounds__(512) void k0_stats(const u16* __restrict__ x, const u16* __restrict__ gw, const u16* __restrict__ w1, float* __restrict__ st, float* __restrict__ cs) {
    const int tid = threadIdx.x, lane = tid & 63, wid = tid >> 6;
    if (blockIdx.x < 256) {
        const int row = blockIdx.x * 8 + wid;
        const u32* xr = (const u32*)(x + row * D_);
        const u32* gr = (const u32*)gw;
        float v[9]; float s = 0.f, gs = 0.f;
        u32 w[5];
#pragma unroll
        for (int i = 0; i < 5; ++i) { const int j = lane + 64 * i; w[i] = (j < 288) ? xr[j] : 0u; }
#pragma unroll
        for (int i = 0; i < 5; ++i) { const int j = lane + 64 * i; const u32 g = (j < 288) ? gr[j] : 0u; s += bflo(w[i]) + bfhi(w[i]); gs += bflo(w[i]) * bflo(g) + bfhi(w[i]) * bfhi(g); }
#pragma unroll
        for (int o = 32; o >= 1; o >>= 1) { s += __shfl_xor(s, o); gs += __shfl_xor(gs, o); }
        const float m = s * (1.f / 576.f);
        float q = 0.f;
#pragma unroll
        for (int i = 0; i < 5; ++i) { const int j = lane + 64 * i; if (j < 288) { const float a = bflo(w[i]) - m, b = bfhi(w[i]) - m; q += a * a + b * b; } }
#pragma unroll
        for (int o = 32; o >= 1; o >>= 1) q += __shfl_xor(q, o);
        const float r = 1.f / sqrtf(q * (1.f / 576.f) + 1e-5f);
        if (lane == 0) { st[row] = r; st[S_ + row] = m * r; st[2 * S_ + row] = 1.f / (1.f + __expf(-gs)); }
        (void)v;
    } else {
        __shared__ float red[16][64];
        const int cb = (blockIdx.x - 256) * 64;
        const int cw = tid & 31, rr = tid >> 5;
        float s0 = 0.f, s1 = 0.f;
        for (int k = rr; k < D_; k += 16) { const u32 w = *(const u32*)(w1 + (size_t)k * F_ + cb + 2 * cw); s0 += bflo(w); s1 += bfhi(w); }
        red[rr][2 * cw] = s0; red[rr][2 * cw + 1] = s1;
        __syncthreads();
        if (tid < 64) { float s = 0.f; for (int i = 0; i < 16; ++i) s += red[i][tid]; cs[cb + tid] = s; }
    }
}
constexpr int P1_SLOT = 24576, P1_TAB = 6 * P1_SLOT, P1_LDS = P1_TAB + 2 * 288 * 4;
constexpr float QSCALE = 0.23570226039551584f * 1.4426950408889634f;
constexpr float GC0 = -2.f * 0.7978845608028654f * 1.4426950408889634f, GC1 = GC0 * 0.044715f;

__device__ __forceinline__ int p1_group(int c, int i, int ch) {
    const int p0 = 54 + 9 * c + (c & 1), ws = 54 + ((c & 1) ? 9 * c : 9 * c + 8);
    if (c < 6) {
        if (i < 2) return 4 * c + 2 * i + ch;
        if (i < 6) return p0 + 2 * (i - 2) + ch;
        return ch ? -1 : ws;
    } else {
        const int q0 = 24 + 3 * (c - 6), odd = (c - 6) & 1;
        if (i == 0) return q0 + odd + ch;
        if (i == 1) return ch ? ws : (odd ? q0 : q0 + 2);
        return p0 + 2 * (i - 2) + ch;
    }
}
struct P1Ctx {
    const u16* wqkv; const u16* w1; u16* qkvb; u16* hb;
    int c, rb32, wid, lane, ch, nst;
    u32 lds0;
    const char* lp; u32 lpitch;
    lcptr trbase;
    float rstd, mu;
};
__device__ __forceinline__ int p1_slot(int t) { return t < 3 ? (t & 1) : ((0x032541 >> (4 * ((t - 3) % 6))) & 15); }
__device__ __forceinline__ void p1_cursor(P1Ctx& X, int i) {
    const int r = X.lane >> 3, cp = X.lane & 7, cs = cp ^ (((r >> 1) & 1) << 2), half = cs >> 2, sub = cs & 3;
    int g0 = p1_group(X.c, i, 0), g1 = p1_group(X.c, i, 1); if (g1 < 0) g1 = g0;
    const char* b0 = g0 < 54 ? (const char*)(X.wqkv + 32 * g0) : (const char*)(X.w1 + 32 * (g0 - 54));
    const char* b1 = g1 < 54 ? (const char*)(X.wqkv + 32 * g1) : (const char*)(X.w1 + 32 * (g1 - 54));
    const u32 p0 = g0 < 54 ? NQKV_ * 2 : F_ * 2, p1 = g1 < 54 ? NQKV_ * 2 : F_ * 2;
    const u32 pitch = half ? p1 : p0;
    X.lp = (half ? b1 : b0) + (size_t)((u32)(X.wid * 8 + r) * pitch) + sub * 16;
    X.lpitch = pitch * 64;
}
__device__ __forceinline__ void p1_load_stage(P1Ctx& X, int slot) {
    const u32 dst = X.lds0 + slot * P1_SLOT + X.wid * 1024;
#pragma unroll
    for (int i = 0; i < 3; ++i) { glds16(X.lp, (u32)RFL(dst + i * 8192)); X.lp += X.lpitch; }
}
__device__ __forceinline__ void p1_load_x(const u16* x, int rb, int wid, int lane, int piece, u32 dst) {
    const int r = lane >> 3, cp = lane & 7;
#pragma unroll
    for (int i = 0; i < 2; ++i) {
        const int row = 16 * wid + 8 * i + r, cs = cp ^ ((row >> 1) & 7);
        glds16((const char*)(x + (size_t)(rb * 128 + row) * D_ + piece * 64) + cs * 16, (u32)RFL(dst + (16 * wid + 8 * i) * 128));
    }
}
#define P1_WAITBAR(N) asm volatile("s_waitcnt vmcnt(" #N ") lgkmcnt(0)\n\ts_barrier" ::: "memory")
__device__ __forceinline__ void p1_wait_stage(int rem  ) {
    if (rem >= 4) P1_WAITBAR(12); else if (rem == 3) P1_WAITBAR(9); else if (rem == 2) P1_WAITBAR(6); else if (rem == 1) P1_WAITBAR(3); else P1_WAITBAR(0);
}
__device__ __forceinline__ float gelu_f(float x) {
    const float t = x * x, p = __builtin_fmaf(t, GC1, GC0), e = __builtin_amdgcn_exp2f(x * p);
    return x * __builtin_amdgcn_rcpf(1.f + e);
}
template <int PREV, int R0, int R1>
__device__ __forceinline__ void p1_epi(const P1Ctx& X, const f32x16& pa, u32 (&pw)[8], lcptr tab, float sc) {
    if (PREV == 0) {
#pragma unroll
        for (int r = R0; r < R1; r += 2) pw[r >> 1] = cvtpk(pa[r] * sc, pa[r + 1] * sc);
    } else {
#pragma unroll
        for (int r = R0; r < R1; r += 2) {
            const int q = r >> 2, e = r & 3;
            const float c0 = *(const LAS float*)(tab + q * 32 + e * 4), c1 = *(const LAS float*)(tab + q * 32 + e * 4 + 4);
            const float b0 = *(const LAS float*)(tab + 1152 + q * 32 + e * 4), b1 = *(const LAS float*)(tab + 1152 + q * 32 + e * 4 + 4);
            const float x0 = __builtin_fmaf(pa[r], X.rstd, __builtin_fmaf(-X.mu, c0, b0));
            const float x1 = __builtin_fmaf(pa[r + 1], X.rstd, __builtin_fmaf(-X.mu, c1, b1));
            pw[r >> 1] = cvtpk(gelu_f(x0), gelu_f(x1));
        }
    }
}
__device__ __forceinline__ void p1_store(u16* blk, int lane, const u32 (&pw)[8]) {
    *(u32x4*)((char*)blk + lane * 16) = (u32x4){pw[0], pw[1], pw[2], pw[3]};
    *(u32x4*)((char*)blk + 1024 + lane * 16) = (u32x4){pw[4], pw[5], pw[6], pw[7]};
}
__device__ __forceinline__ u16* p1_outblk(const P1Ctx& X, int g) { return g < 54 ? X.qkvb + (size_t)(X.rb32 * 54 + g) * 1024 : X.hb + (size_t)(X.rb32 * 144 + (g - 54)) * 1024; }

template <int CUR  , int J>
__device__ __forceinline__ void p1_mma_stage(const P1Ctx& X, int slot, const bf16x8 (&xf)[36], f32x16& acc) {
    if (CUR == 2) return;
    lcptr p = X.trbase + slot * P1_SLOT;
#pragma unroll
    for (int ks = 0; ks < 12; ++ks) {
        const bf16x8 wf = cat8(trd(p + ks * 2048), trd(p + ks * 2048 + 512));
        if (J == 0 && ks == 0) { f32x16 z = {}; acc = CUR == 0 ? MFMA32(wf, xf[0], z) : MFMA32(xf[0], wf, z); }
        else acc = CUR == 0 ? MFMA32(wf, xf[J * 12 + ks], acc) : MFMA32(xf[J * 12 + ks], wf, acc);
    }
}
template <int CUR, int PREV>
__device__ __forceinline__ void p1_chunk(P1Ctx& X, int i, const bf16x8 (&xf)[36], f32x16& acc, const f32x16& pacc, int pg  ) {
    const int t0 = 3 * i;
    u32 pw[8];
    lcptr tab = (lcptr)(size_t)(X.lds0 + P1_TAB) + (PREV == 1 ? (pg - 54 - 9 * X.c) * 128 : 0) + (X.lane >> 5) * 16;
    const float sc = pg < 18 ? QSCALE : 1.f;
    if (i == 1) { P1_WAITBAR(6); p1_cursor(X, 2); p1_load_stage(X, p1_slot(6)); p1_load_stage(X, p1_slot(7)); p1_load_stage(X, p1_slot(8)); }
    else { p1_wait_stage(X.nst - 1 - t0); if (t0 + 5 < X.nst) p1_load_stage(X, p1_slot(t0 + 5)); }
    p1_mma_stage<CUR, 0>(X, p1_slot(t0), xf, acc);
    p1_epi<PREV, 0, 6>(X, pacc, pw, tab, sc);
    p1_wait_stage(X.nst - 2 - t0);
    if (t0 + 6 < X.nst) { p1_cursor(X, i + 2); p1_load_stage(X, p1_slot(t0 + 6)); }
    p1_mma_stage<CUR, 1>(X, p1_slot(t0 + 1), xf, acc);
    p1_epi<PREV, 6, 12>(X, pacc, pw, tab, sc);
    p1_wait_stage(X.nst - 3 - t0);
    if (t0 + 7 < X.nst) p1_load_stage(X, p1_slot(t0 + 7));
    p1_mma_stage<CUR, 2>(X, p1_slot(t0 + 2), xf, acc);
    p1_epi<PREV, 12, 16>(X, pacc, pw, tab, sc);
    p1_store(p1_outblk(X, pg), X.lane, pw);
}
template <int PAR>
__device__ __forceinline__ void p1_dispatch(P1Ctx& X, int i, const bf16x8 (&xf)[36], f32x16& acc, const f32x16& pacc) {
    const int g = p1_group(X.c, i, X.ch), pg = p1_group(X.c, i - 1, X.ch);
    if (pg < 54) { if (g >= 36 && g < 54) p1_chunk<1, 0>(X, i, xf, acc, pacc, pg); else p1_chunk<0, 0>(X, i, xf, acc, pacc, pg); }
    else { if (g < 0) p1_chunk<2, 1>(X, i, xf, acc, pacc, pg); else p1_chunk<0, 1>(X, i, xf, acc, pacc, pg); }
}

__global__ __launch_bounds__(512, 2) void p1_kernel(const u16* __restrict__ x, const u16* __restrict__ wqkv, const u16* __restrict__ w1, const u16* __restrict__ b1,
                                                     const float* __restrict__ st, const float* __restrict__ cs, u16* __restrict__ qkvb, u16* __restrict__ hb) {
    extern __shared__ __attribute__((aligned(16))) char smem[];
    const int tid = threadIdx.x, lane = tid & 63, wid = RFL(tid >> 6);
    const int xcd = blockIdx.x & 7, idx = blockIdx.x >> 3;
    const int rb = (xcd & 1) * 8 + (idx & 7), c = (xcd >> 1) * 4 + (idx >> 3);
    const int rg = wid & 3, ch = wid >> 2;
    P1Ctx X; X.wqkv = wqkv; X.w1 = w1; X.qkvb = qkvb; X.hb = hb; X.c = c; X.rb32 = rb * 4 + rg; X.wid = wid; X.lane = lane; X.ch = ch;
    const int nch = c < 6 ? 7 : 6; X.nst = 3 * nch;
    X.lds0 = (u32)(uintptr_t)smem;
    { const int h = lane >> 5, s16 = (lane >> 4) & 1, q = (lane & 15) >> 2, p = lane & 3;
      X.trbase = (lcptr)(size_t)X.lds0 + (8 * h + q) * 128 + (((4 * ch + 2 * s16 + (p >> 1)) ^ (((q >> 1) & 1) << 2)) * 16) + (p & 1) * 8; }
    const u32 XR = X.lds0 + 2 * P1_SLOT;
    p1_cursor(X, 0);
    p1_load_stage(X, 0);
    p1_load_x(x, rb, wid, lane, 0, XR); p1_load_x(x, rb, wid, lane, 1, XR + 16384); p1_load_x(x, rb, wid, lane, 2, XR + 32768);
    p1_load_stage(X, 1);
    p1_load_x(x, rb, wid, lane, 3, XR + 49152); p1_load_x(x, rb, wid, lane, 4, XR + 65536); p1_load_x(x, rb, wid, lane, 5, XR + 81920);
    { const int row = rb * 128 + rg * 32 + (lane & 31); X.rstd = st[row]; X.mu = st[S_ + row];
      if (tid < 288) { ((LAS float*)(size_t)(X.lds0 + P1_TAB))[tid] = cs[288 * c + tid]; ((LAS float*)(size_t)(X.lds0 + P1_TAB + 1152))[tid] = bf2f(b1[288 * c + tid]); } }
    bf16x8 xf[36];
    f32x16 accA, accB;
    const int g0 = p1_group(c, 0, ch); const bool sw0 = g0 >= 36;
    const int m = lane & 31, h = lane >> 5, xrow = rg * 32 + m, xz = (xrow >> 1) & 7;
    lcptr xb = (lcptr)(size_t)XR + xrow * 128;
#define P1_XF(s0, pz0) _Pragma("unroll") for (int s = 0; s < 12; ++s) xf[s0 + s] = *(const LAS bf16x8*)(xb + ((pz0) + (s >> 2)) * 16384 + (((2 * (s & 3) + h) ^ xz) << 4))
    P1_WAITBAR(0);
    P1_XF(0, 0);
    if (sw0) p1_mma_stage<1, 0>(X, 0, xf, accA); else p1_mma_stage<0, 0>(X, 0, xf, accA);
    P1_WAITBAR(0);
    p1_load_x(x, rb, wid, lane, 6, XR); p1_load_x(x, rb, wid, lane, 7, XR + 16384); p1_load_x(x, rb, wid, lane, 8, XR + 32768);
    p1_load_stage(X, 0);
    P1_XF(12, 3);
    if (sw0) p1_mma_stage<1, 1>(X, 1, xf, accA); else p1_mma_stage<0, 1>(X, 1, xf, accA);
    P1_WAITBAR(0);
    p1_cursor(X, 1);
    p1_load_stage(X, p1_slot(3)); p1_load_stage(X, p1_slot(4)); p1_load_stage(X, p1_slot(5));
    P1_XF(24, 0);
    if (sw0) p1_mma_stage<1, 2>(X, 0, xf, accA); else p1_mma_stage<0, 2>(X, 0, xf, accA);
    for (int i = 1; i < nch; ++i) {
        if (i & 1) p1_dispatch<1>(X, i, xf, accB, accA); else p1_dispatch<0>(X, i, xf, accA, accB);
    }
    { const int pg = p1_group(c, nch - 1, ch);
      if (pg >= 0) { u32 pw[8]; lcptr tab = (lcptr)(size_t)(X.lds0 + P1_TAB) + (pg - 54 - 9 * c) * 128 + (lane >> 5) * 16;
        if ((nch - 1) & 1) p1_epi<1, 0, 16>(X, accB, pw, tab, 1.f); else p1_epi<1, 0, 16>(X, accA, pw, tab, 1.f);
        p1_store(p1_outblk(X, pg), lane, pw); } }
}
constexpr int AT_K = 0, AT_V = 12 * 4096, AT_Q = 24 * 4096, AT_LDS = AT_Q + 4 * 4096;
__device__ __forceinline__ int qk_off(int c) { const int cc = c & 31; return (c >> 5) * 2048 + (2 * (cc >> 4) + ((cc >> 2) & 1)) * 512 + (((cc >> 3) & 1) * 4 + (cc & 3)) * 2; }

__device__ __forceinline__ float xmax16(float v) { auto r = __builtin_amdgcn_permlane16_swap(__float_as_uint(v), __float_as_uint(v), false, false); return fmaxf(__uint_as_float(r[0]), __uint_as_float(r[1])); }
__device__ __forceinline__ float xmax32(float v) { auto r = __builtin_amdgcn_permlane32_swap(__float_as_uint(v), __float_as_uint(v), false, false); return fmaxf(__uint_as_float(r[0]), __uint_as_float(r[1])); }

__device__ __forceinline__ void attn_unit(lcptr L, int c0  , int cones, int w, int lane, int Q0, bool early, float gate, u16* ob, int head) {
    const int i16 = lane & 15, g = lane >> 4;
    const int lb0 = w >> 1;
    bf16x8 qf;
    { const int tok = (w & 1) * 16 + i16; lcptr qp = L + AT_Q + (w >> 1) * 4096 + tok * 16;
      const u32 a = *(const LAS u32*)(qp + qk_off(c0 + 2 * g)), b = *(const LAS u32*)(qp + qk_off(c0 + 2 * g + 8));
      u32 c = *(const LAS u32*)(qp + qk_off(c0 + 16)); if (g != 0) c = 0u;
      qf = __builtin_bit_cast(bf16x8, (u32x4){a, b, c, 0u}); }
    lcptr k0 = L + AT_K + lb0 * 4096 + i16 * 16 + qk_off(c0 + 2 * g), k1 = L + AT_K + lb0 * 4096 + i16 * 16 + qk_off(c0 + 2 * g + 8), k2 = L + AT_K + lb0 * 4096 + i16 * 16 + qk_off(c0 + 16);
    f32x4 s[18];
#pragma unroll
    for (int t = 0; t < 18; ++t) {
        const int o = (t >> 1) * 4096 + (t & 1) * 256;
        const u32x4 kw = {*(const LAS u32*)(k0 + o), *(const LAS u32*)(k1 + o), *(const LAS u32*)(k2 + o), 0u};
        s[t] = MFMA16(__builtin_bit_cast(bf16x8, kw), qf, ((f32x4){0.f, 0.f, 0.f, 0.f}));
    }
    const int q = Q0 + 16 * w + i16, kb = Q0 - 256 + lb0 * 32 + 4 * g;
    const int dq = q - kb;
#pragma unroll
    for (int t = 0; t < 18; ++t) {
        if (t < 2 || t >= 16 || early) {
#pragma unroll
            for (int r = 0; r < 4; ++r) { const int d = dq - 16 * t - r; const bool ok = (u32)d <= 255u && (kb + 16 * t + r) >= 0; s[t][r] = ok ? s[t][r] : -INFINITY; }
        }
    }
    float m = fmaxf(fmaxf(s[0][0], s[0][1]), fmaxf(s[0][2], s[0][3]));
#pragma unroll
    for (int t = 1; t < 18; ++t) m = fmaxf(fmaxf(m, fmaxf(s[t][0], s[t][1])), fmaxf(s[t][2], s[t][3]));
    m = xmax32(xmax16(m));
    const int ca = c0 + i16, cb = (i16 < 2) ? c0 + 16 + i16 : (i16 == 2 ? cones : c0);
    lcptr va = L + AT_V + lb0 * 4096 + (ca >> 5) * 2048 + (g & 1) * 512 + (ca & 31) * 16 + (g >> 1) * 8;
    lcptr vb = L + AT_V + lb0 * 4096 + (cb >> 5) * 2048 + (g & 1) * 512 + (cb & 31) * 16 + (g >> 1) * 8;
    f32x4 oa = {0.f, 0.f, 0.f, 0.f}, obb = {0.f, 0.f, 0.f, 0.f};
#pragma unroll
    for (int b = 0; b < 9; ++b) {
        u32x4 pw;
        pw[0] = cvtpk_c(__builtin_amdgcn_exp2f(s[2 * b][0] - m), __builtin_amdgcn_exp2f(s[2 * b][1] - m));
        pw[1] = cvtpk_c(__builtin_amdgcn_exp2f(s[2 * b][2] - m), __builtin_amdgcn_exp2f(s[2 * b][3] - m));
        pw[2] = cvtpk_c(__builtin_amdgcn_exp2f(s[2 * b + 1][0] - m), __builtin_amdgcn_exp2f(s[2 * b + 1][1] - m));
        pw[3] = cvtpk_c(__builtin_amdgcn_exp2f(s[2 * b + 1][2] - m), __builtin_amdgcn_exp2f(s[2 * b + 1][3] - m));
        const bf16x8 pf = __builtin_bit_cast(bf16x8, pw);
        const u32x2 a0 = *(const LAS u32x2*)(va + b * 4096), a1 = *(const LAS u32x2*)(va + b * 4096 + 1024);
        const u32x2 b0 = *(const LAS u32x2*)(vb + b * 4096), b1 = *(const LAS u32x2*)(vb + b * 4096 + 1024);
        oa = MFMA16(__builtin_bit_cast(bf16x8, ((u32x4){a0[0], a0[1], a1[0], a1[1]})), pf, oa);
        obb = MFMA16(__builtin_bit_cast(bf16x8, ((u32x4){b0[0], b0[1], b1[0], b1[1]})), pf, obb);
    }
    const float l = __shfl(obb[2], i16);
    const float sc = gate * __builtin_amdgcn_rcpf(l);
    char* base = (char*)ob + (size_t)(q >> 5) * 18 * 2048 + (q & 31) * 16;
    const int col = 18 * head + 4 * g;
    auto off = [](int c) { const int cc = c & 31; return (c >> 5) * 2048 + (2 * (cc >> 4) + ((cc >> 2) & 1)) * 512 + (((cc >> 3) & 1) * 4 + (cc & 3)) * 2; };
    *(u32*)(base + off(col)) = cvtpk(oa[0] * sc, oa[1] * sc);
    *(u32*)(base + off(col + 2)) = cvtpk(oa[2] * sc, oa[3] * sc);
    if (g == 0) *(u32*)(base + off(18 * head + 16)) = cvtpk(obb[0] * sc, obb[1] * sc);
}

__global__ __launch_bounds__(512, 2) void attn_kernel(const u16* __restrict__ qkvb, const float* __restrict__ st, u16* __restrict__ ob) {
    extern __shared__ __attribute__((aligned(16))) char smem[];
    const int tid = threadIdx.x, lane = tid & 63, wid = RFL(tid >> 6);
    const int qb = blockIdx.x >> 4, hp = blockIdx.x & 15;
    const int Q0 = qb * 128, cg0 = (36 * hp) >> 5, c0 = 36 * hp - 32 * cg0;
    const u32 lds0 = (u32)(uintptr_t)smem;
    for (int p = wid; p < 56; p += 8) {
        const int u = p >> 1, gsel = p & 1;
        int rb, cg; u32 dst;
        if (u < 12) { rb = qb * 4 - 8 + u; cg = 18 + cg0 + gsel; dst = AT_K + u * 4096 + gsel * 2048; }
        else if (u < 24) { rb = qb * 4 - 8 + (u - 12); cg = 36 + cg0 + gsel; dst = AT_V + (u - 12) * 4096 + gsel * 2048; }
        else { rb = qb * 4 + (u - 24); cg = cg0 + gsel; dst = AT_Q + (u - 24) * 4096 + gsel * 2048; }
        if (rb < 0) rb = 0;
        const char* src = (const char*)(qkvb + (size_t)(rb * 54 + cg) * 1024) + lane * 16;
        glds16(src, (u32)RFL(lds0 + dst)); glds16(src + 1024, (u32)RFL(lds0 + dst + 1024));
    }
    const int q = Q0 + 16 * wid + (lane & 15);
    const float gate = st[2 * S_ + q];
    asm volatile("s_waitcnt vmcnt(0)\n\ts_barrier" ::: "memory");
    const int cones = (c0 + 36) & 63;
    if (tid < 48) { const int kb = tid >> 2, pc = tid & 3; *(LAS u32x4*)((lptr)(size_t)lds0 + AT_V + kb * 4096 + (cones >> 5) * 2048 + pc * 512 + (cones & 31) * 16) = (u32x4){0x3f803f80u, 0x3f803f80u, 0x3f803f80u, 0x3f803f80u}; }
    asm volatile("s_waitcnt lgkmcnt(0)\n\ts_barrier" ::: "memory");
    const bool early = qb < 2;
    attn_unit((lcptr)(size_t)lds0, c0, cones, wid, lane, Q0, early, gate, ob, 2 * hp);
    attn_unit((lcptr)(size_t)lds0, c0 + 18, cones, wid, lane, Q0, early, gate, ob, 2 * hp + 1);
}
constexpr int P3_A = 16384, P3_B = 64 * 288, P3_SLOT = P3_A + P3_B, P3_NS = 4, P3_LDS = P3_NS * P3_SLOT;
struct P3Ctx { const u16* hb; const u16* ob; const u16* w2; const u16* wo; int rb, nt, s, wid, lane; int nh  , o0  , nst; u32 lds0; };
__device__ __forceinline__ void p3_load(const P3Ctx& X, int t) {
    const u32 slot = X.lds0 + (t & 3) * P3_SLOT;
    { const int r4 = X.wid >> 1, gs = X.wid & 1;
      const char* src = t < X.nh ? (const char*)(X.hb + (size_t)((X.rb * 4 + r4) * 144 + 36 * X.s + 2 * t + gs) * 1024)
                                 : (const char*)(X.ob + (size_t)((X.rb * 4 + r4) * 18 + 2 * (X.o0 + t - X.nh) + gs) * 1024);
      src += X.lane * 16;
      glds16(src, (u32)RFL(slot + X.wid * 2048)); glds16(src + 1024, (u32)RFL(slot + X.wid * 2048 + 1024)); }
    const char* wb = t < X.nh ? (const char*)(X.w2 + (size_t)(1152 * X.s + 64 * t) * D_ + 144 * X.nt) : (const char*)(X.wo + (size_t)(64 * (X.o0 + t - X.nh)) * D_ + 144 * X.nt);
#pragma unroll
    for (int i = 0; i < 3; ++i) {
        const int p = X.wid + 8 * i;
        if (p < 18) { const int e = p * 64 + X.lane, row = e / 18, c = e - row * 18; glds16(wb + row * 1152 + c * 16, (u32)RFL(slot + P3_A + p * 1024)); }
    }
}
template <int NT>
__device__ __forceinline__ void p3_stage(const P3Ctx& X, int t, lcptr abase, lcptr bbase, f32x4 (&acc)[4][5]) {
    const int so = (t & 3) * P3_SLOT;
    bf16x8 hf[4];
#pragma unroll
    for (int m = 0; m < 4; ++m) hf[m] = *(const LAS bf16x8*)(abase + so + (m >> 1) * 4096 + (m & 1) * 256);
#pragma unroll
    for (int n = 0; n < NT; ++n) {
        const bf16x8 wf = cat8(trd(bbase + so + n * 32), trd(bbase + so + n * 32 + 8 * 288));
#pragma unroll
        for (int m = 0; m < 4; ++m) acc[m][n] = MFMA16(wf, hf[m], acc[m][n]);
    }
}
__global__ __launch_bounds__(512, 2) void p3_kernel(const u16* __restrict__ hb, const u16* __restrict__ ob, const u16* __restrict__ w2, const u16* __restrict__ wo,
                                                     const float* __restrict__ st, float* __restrict__ part) {
    extern __shared__ __attribute__((aligned(16))) char smem[];
    const int tid = threadIdx.x, lane = tid & 63, wid = RFL(tid >> 6);
    const int xcd = blockIdx.x & 7, idx = blockIdx.x >> 3;
    P3Ctx X; X.hb = hb; X.ob = ob; X.w2 = w2; X.wo = wo; X.wid = wid; X.lane = lane;
    X.rb = (xcd & 1) * 8 + (idx & 7); X.s = xcd >> 1; X.nt = idx >> 3;
    X.nh = 18; X.o0 = X.s == 0 ? 0 : 1 + 2 * X.s; X.nst = X.s == 0 ? 21 : 20;
    X.lds0 = (u32)(uintptr_t)smem;
    const int kh = wid >> 2, mh = (wid >> 1) & 1, nh = (wid & 1) ^ kh;
    const int i16 = lane & 15, kq = lane >> 4;
    lcptr abase = (lcptr)(size_t)X.lds0 + (mh * 2 * 2 + kh) * 2048 + kq * 512 + i16 * 16;
    lcptr bbase = (lcptr)(size_t)X.lds0 + P3_A + (32 * kh + 16 * (kq >> 1) + 4 * (kq & 1) + (i16 >> 2)) * 288 + (nh ? 5 * 32 : 0) + (i16 & 3) * 8;
    float og[4];
#pragma unroll
    for (int m = 0; m < 4; ++m) og[m] = 1.f - st[2 * S_ + X.rb * 128 + mh * 64 + m * 16 + i16];
    asm volatile("s_waitcnt vmcnt(0)" ::: "memory");
    f32x4 acc[4][5];
#pragma unroll
    for (int m = 0; m < 4; ++m)
#pragma unroll
        for (int n = 0; n < 5; ++n) acc[m][n] = (f32x4){0.f, 0.f, 0.f, 0.f};
    p3_load(X, 0); p3_load(X, 1); p3_load(X, 2);
    const bool big = wid < 2;
    for (int t = 0; t < X.nst; ++t) {
        const int rem = X.nst - 1 - t;
        if (big) { if (rem >= 2) asm volatile("s_waitcnt vmcnt(10) lgkmcnt(0)\n\ts_barrier" ::: "memory"); else if (rem == 1) asm volatile("s_waitcnt vmcnt(5) lgkmcnt(0)\n\ts_barrier" ::: "memory"); else asm volatile("s_waitcnt vmcnt(0) lgkmcnt(0)\n\ts_barrier" ::: "memory"); }
        else { if (rem >= 2) asm volatile("s_waitcnt vmcnt(8) lgkmcnt(0)\n\ts_barrier" ::: "memory"); else if (rem == 1) asm volatile("s_waitcnt vmcnt(4) lgkmcnt(0)\n\ts_barrier" ::: "memory"); else asm volatile("s_waitcnt vmcnt(0) lgkmcnt(0)\n\ts_barrier" ::: "memory"); }
        if (t + 3 < X.nst) p3_load(X, t + 3);
        if (t == X.nh) {
#pragma unroll
            for (int m = 0; m < 4; ++m)
#pragma unroll
                for (int n = 0; n < 5; ++n) acc[m][n] *= og[m];
        }
        if (nh == 0) p3_stage<5>(X, t, abase, bbase, acc); else p3_stage<4>(X, t, abase, bbase, acc);
    }
    asm volatile("s_waitcnt lgkmcnt(0)\n\ts_barrier" ::: "memory");
    LAS f32x4* red = (LAS f32x4*)(size_t)X.lds0;
    if (kh == 1) {
#pragma unroll
        for (int m = 0; m < 4; ++m)
#pragma unroll
            for (int n = 0; n < 5; ++n) red[((mh * 2 + nh) * 20 + m * 5 + n) * 64 + lane] = acc[m][n];
    }
    asm volatile("s_waitcnt lgkmcnt(0)\n\ts_barrier" ::: "memory");
    if (kh == 0) {
        float* pb = part + ((size_t)((X.s * 16 + X.rb) * 4 + X.nt) * 72) * 256;
        const int NTN = nh ? 4 : 5;
#pragma unroll
        for (int m = 0; m < 4; ++m)
#pragma unroll
            for (int n = 0; n < 5; ++n)
                if (n < NTN) { const f32x4 v = acc[m][n] + red[((mh * 2 + nh) * 20 + m * 5 + n) * 64 + lane];
                    *(f32x4*)(pb + ((mh * 4 + m) * 9 + (nh ? 5 : 0) + n) * 256 + lane * 4) = v; }
    }
}
__global__ __launch_bounds__(256) void k4_reduce(const float* __restrict__ part, const float* __restrict__ st, const u16* __restrict__ b2, u16* __restrict__ out) {
    const int gid = blockIdx.x * 256 + threadIdx.x;
    const int lane = gid & 63, f = gid >> 6;
    const int n = f % 9, mt = (f / 9) & 7, nt = (f / 72) & 3, rb = f / 288;
    f32x4 v = *(const f32x4*)(part + (size_t)gid * 4);
#pragma unroll
    for (int s = 1; s < 4; ++s) v += *(const f32x4*)(part + (size_t)s * (16 * 4 * 72 * 256) + (size_t)gid * 4);
    const int row = rb * 128 + mt * 16 + (lane & 15), col = nt * 144 + n * 16 + (lane >> 4) * 4;
    const float og = 1.f - st[2 * S_ + row];
    const u32x2 bb = *(const u32x2*)(b2 + col);
    u32x2 o; o[0] = cvtpk(v[0] + og * bflo(bb[0]), v[1] + og * bfhi(bb[0])); o[1] = cvtpk(v[2] + og * bflo(bb[1]), v[3] + og * bfhi(bb[1]));
    *(u32x2*)(out + (size_t)row * D_ + col) = o;
}
extern "C" void kernel_launch(void* const* d_in, const int* in_sizes, int n_in, void* d_out, int out_size, void* d_ws, size_t ws_size, hipStream_t stream) {
    const u16* x = (const u16*)d_in[0]; const u16* wqkv = (const u16*)d_in[1]; const u16* wo = (const u16*)d_in[2];
    const u16* w1 = (const u16*)d_in[4]; const u16* b1 = (const u16*)d_in[5]; const u16* w2 = (const u16*)d_in[6]; const u16* b2 = (const u16*)d_in[7]; const u16* gw = (const u16*)d_in[8];
    char* ws = (char*)d_ws;
    float* st = (float*)ws;
    float* cs = (float*)(ws + 32768);
    u16* qkvb = (u16*)(ws + 65536);
    u16* hb = (u16*)(ws + 65536 + 7077888);
    u16* ob = (u16*)(ws + 65536 + 7077888 + 18874368);
    float* part = (float*)(ws + 65536 + 7077888 + 18874368 + 2359296);
    (void)hipFuncSetAttribute((const void*)p1_kernel, hipFuncAttributeMaxDynamicSharedMemorySize, P1_LDS);
    (void)hipFuncSetAttribute((const void*)attn_kernel, hipFuncAttributeMaxDynamicSharedMemorySize, AT_LDS);
    (void)hipFuncSetAttribute((const void*)p3_kernel, hipFuncAttributeMaxDynamicSharedMemorySize, P3_LDS);
    k0_stats<<<256 + 72, 512, 0, stream>>>(x, gw, w1, st, cs);
    p1_kernel<<<256, 512, P1_LDS, stream>>>(x, wqkv, w1, b1, st, cs, qkvb, hb);
    attn_kernel<<<256, 512, AT_LDS, stream>>>(qkvb, st, ob);
    p3_kernel<<<256, 512, P3_LDS, stream>>>(hb, ob, w2, wo, st, part);
    k4_reduce<<<1152, 256, 0, stream>>>(part, st, b2, (u16*)d_out);
}
```

```cpp
#include <hip/hip_runtime.h>
#include <hip/hip_bf16.h>
typedef unsigned short u16;
typedef unsigned int u32;
#define LAS __attribute__((address_space(3)))
typedef short bf16x8 __attribute__((ext_vector_type(8)));
typedef short s16x4 __attribute__((ext_vector_type(4)));
typedef float f32x16 __attribute__((ext_vector_type(16)));
typedef float f32x4 __attribute__((ext_vector_type(4)));
typedef u32 u32x4 __attribute__((ext_vector_type(4)));
typedef u32 u32x2 __attribute__((ext_vector_type(2)));
typedef LAS char* lptr;
typedef LAS const char* lcptr;

constexpr int S_ = 2048, D_ = 576, H_ = 32, DH_ = 18, F_ = 4608, NQKV_ = 1728;

__device__ __forceinline__ float bf2f(u16 b) { return __uint_as_float(((u32)b) << 16); }
__device__ __forceinline__ float bflo(u32 w) { return __uint_as_float(w << 16); }
__device__ __forceinline__ float bfhi(u32 w) { return __uint_as_float(w & 0xffff0000u); }
typedef __bf16 bf16x2_t __attribute__((ext_vector_type(2)));
typedef float f32x2 __attribute__((ext_vector_type(2)));
__device__ __forceinline__ u32 cvtpk_c(float lo, float hi) { return __builtin_bit_cast(u32, __builtin_convertvector((f32x2){lo, hi}, bf16x2_t)); }
__device__ __forceinline__ u32 cvtpk(float lo, float hi) { u32 r; asm("v_cvt_pk_bf16_f32 %0, %1, %2" : "=v"(r) : "v"(lo), "v"(hi)); return r; }
__device__ __forceinline__ u16 f2bf(float f) { return (u16)(cvtpk(f, f) & 0xffffu); }
__device__ __forceinline__ void glds16(const void* g, u32 lds_base) {
    u32 sv;
    asm volatile("s_mov_b32 %0, m0\n\ts_mov_b32 m0, %2\n\ts_nop 0\n\tglobal_load_lds_dwordx4 %1, off\n\ts_mov_b32 m0, %0" : "=&s"(sv) : "v"(g), "s"(lds_base) : "memory");
}
__device__ __forceinline__ void glds16s(const void* sbase, u32 voff, u32 lds_base) {
    u32 sv;
    asm volatile("s_mov_b32 %0, m0\n\ts_mov_b32 m0, %3\n\ts_nop 0\n\tglobal_load_lds_dwordx4 %1, %2\n\ts_mov_b32 m0, %0" : "=&s"(sv) : "v"(voff), "s"(sbase), "s"(lds_base) : "memory");
}
__device__ __forceinline__ void glds16s_off1k(const void* sbase, u32 voff, u32 lds_base) {
    u32 sv;
    asm volatile("s_mov_b32 %0, m0\n\ts_mov_b32 m0, %3\n\ts_nop 0\n\tglobal_load_lds_dwordx4 %1, %2\n\tglobal_load_lds_dwordx4 %1, %2 offset:1024\n\ts_mov_b32 m0, %0" : "=&s"(sv) : "v"(voff), "s"(sbase), "s"(lds_base) : "memory");
}
__device__ __forceinline__ s16x4 trd(lcptr p) {
    typedef short v4i16_t __attribute__((ext_vector_type(4)));
    return __builtin_bit_cast(s16x4, __builtin_amdgcn_ds_read_tr16_b64_v4i16((LAS v4i16_t*)p));
}
__device__ __forceinline__ bf16x8 cat8(s16x4 a, s16x4 b) { return (bf16x8){a[0], a[1], a[2], a[3], b[0], b[1], b[2], b[3]}; }
#define MFMA32(a, b, c) __builtin_amdgcn_mfma_f32_32x32x16_bf16(a, b, c, 0, 0, 0)
#define MFMA16(a, b, c) __builtin_amdgcn_mfma_f32_16x16x32_bf16(a, b, c, 0, 0, 0)
#define SBAR() __builtin_amdgcn_sched_barrier(0)
#define PIN(x) asm volatile("" : "+v"(x))
#define RFL(x) __builtin_amdgcn_readfirstlane(x)

#define STAMP_DECL
#define STAMP(i)
#define STAMP_OUT(buf, n)
constexpr float GC0 = -2.f * 0.7978845608028654f * 1.4426950408889634f, GC1 = GC0 * 0.044715f;
__device__ __forceinline__ u32 bfbits(float f) { return cvtpk(f, 0.f) & 0xffffu; }
__global__ __launch_bounds__(512) void k0_stats(const u16* __restrict__ x, const u16* __restrict__ gw, const u16* __restrict__ w1, const u16* __restrict__ b1, u32x4* __restrict__ rowrec, u32x4* __restrict__ wext) {
    const int tid = threadIdx.x, lane = tid & 63, wid = tid >> 6;
    if (blockIdx.x < 256) {
        const int row = blockIdx.x * 8 + wid;
        const u32* xr = (const u32*)(x + row * D_);
        const u32* gr = (const u32*)gw;
        float s = 0.f, gs = 0.f;
        u32 w[5], g[5];
#pragma unroll
        for (int i = 0; i < 5; ++i) { const int j = lane + 64 * i; w[i] = (j < 288) ? xr[j] : 0u; g[i] = (j < 288) ? gr[j] : 0u; }
#pragma unroll
        for (int i = 0; i < 5; ++i) { s += bflo(w[i]) + bfhi(w[i]); gs += bflo(w[i]) * bflo(g[i]) + bfhi(w[i]) * bfhi(g[i]); }
#pragma unroll
        for (int o = 32; o >= 1; o >>= 1) { s += __shfl_xor(s, o); gs += __shfl_xor(gs, o); }
        const float m = s * (1.f / 576.f);
        float q = 0.f;
#pragma unroll
        for (int i = 0; i < 5; ++i) { const int j = lane + 64 * i; if (j < 288) { const float a = bflo(w[i]) - m, b = bfhi(w[i]) - m; q += a * a + b * b; } }
#pragma unroll
        for (int o = 32; o >= 1; o >>= 1) q += __shfl_xor(q, o);
        const float sd = sqrtf(q * (1.f / 576.f) + 1e-5f), r = 1.f / sd;
        if (lane == 0) {
            const u32 mh = bfbits(-m); const u32 ml = bfbits(-m - __uint_as_float(mh << 16));
            const u32 sh = bfbits(sd); const u32 sl = bfbits(sd - __uint_as_float(sh << 16));
            rowrec[2 * row] = (u32x4){mh | (mh << 16), ml | (sh << 16), sl, 0u};
            const float gate = 1.f / (1.f + __expf(-gs)), og = 1.f / (1.f + __expf(gs));
            rowrec[2 * row + 1] = (u32x4){__float_as_uint(GC0 * r), __float_as_uint(GC1 * r * r * r), __float_as_uint(sd / og), __float_as_uint(gate)};
        }
    } else {
        __shared__ float red[32][128];
        const int cb = (blockIdx.x - 256) * 128;
        const int cw = tid & 15, rr = tid >> 4;
        u32x4 v[18];
#pragma unroll
        for (int i = 0; i < 18; ++i) v[i] = *(const u32x4*)(w1 + (size_t)(rr + 32 * i) * F_ + cb + 8 * cw);
        float s[8] = {0.f, 0.f, 0.f, 0.f, 0.f, 0.f, 0.f, 0.f};
#pragma unroll
        for (int i = 0; i < 18; ++i)
#pragma unroll
            for (int j = 0; j < 4; ++j) { s[2 * j] += bflo(v[i][j]); s[2 * j + 1] += bfhi(v[i][j]); }
#pragma unroll
        for (int j = 0; j < 8; ++j) red[rr][8 * cw + j] = s[j];
        __syncthreads();
        if (tid < 128) { float t = 0.f; for (int i = 0; i < 32; ++i) t += red[i][tid];
            const u32 ch = bfbits(t), cl = bfbits(t - __uint_as_float(ch << 16)), bb = b1[cb + tid];
            wext[cb + tid] = (u32x4){ch | (cl << 16), ch | (bb << 16), bb, 0u}; }
    }
}
constexpr int P1_SLOT = 24576, P1_RING = 6 * P1_SLOT, P1_WEXT = P1_RING, P1_ROW = P1_WEXT + 288 * 16 + 64  , P1_LDS = P1_ROW + 128 * 32;
constexpr float QSCALE = 0.23570226039551584f * 1.4426950408889634f;
#define P1_WAITBAR(N) asm volatile("s_waitcnt vmcnt(" #N ") lgkmcnt(0)\n\ts_barrier" ::: "memory")

struct P1Ctx {
    const char* w1; __amdgpu_buffer_rsrc_t hrs;
    int c, nch, g1first; u32 l16;
    const char* lb; const char* la;
    u32 ld, ldst0;
    u32 voffW[3];
    lcptr trbase, wexb; u32 wexs; u32 rs;
    float c0, c1, sd; lcptr xeb;
};
constexpr u32 P1_PART = 192u * F_ * 2u;
__device__ __forceinline__ int p1_w1g(const P1Ctx& X, int i) { return i < 6 ? X.g1first + 2 * (i - 2) : ((i == 6 && X.nch == 7) ? ((X.c & 1) ? 9 * X.c : 9 * X.c + 8) : X.g1first); }
__device__ __forceinline__ void p1_piece_a(const P1Ctx& X, int k) { asm volatile("s_add_u32 m0, %0, %1" :: "s"(X.ld), "n"(k * 8192) : "memory", "scc"); }
__device__ __forceinline__ void p1_piece_b(const P1Ctx& X, int k) {
    asm volatile("global_load_lds_dwordx4 %0, %1" :: "v"(X.voffW[k]), "s"(X.la) : "memory");
}
__device__ __forceinline__ void p1_piece(const P1Ctx& X, int k) { p1_piece_a(X, k); asm volatile("s_nop 0"); p1_piece_b(X, k); }
__device__ __forceinline__ void p1_top(P1Ctx& X, int i, int j) {
    if (j == 0) asm volatile("s_cmp_eq_u32 %0, 1\n\ts_cbranch_scc1 3f\n\ts_waitcnt vmcnt(12) lgkmcnt(0)\n\ts_barrier\n3:" :: "s"(i) : "memory", "scc");
    else asm volatile("s_waitcnt vmcnt(12) lgkmcnt(0)\n\ts_barrier" ::: "memory");
    X.ld = X.ldst0 + X.rs;
    X.rs = X.rs == 5 * P1_SLOT ? 0 : X.rs + P1_SLOT;
    if (j == 0) X.la = X.lb + 2 * P1_PART;
    if (j == 1) { X.lb = X.w1 + p1_w1g(X, i + 2) * 64; X.la = X.lb; }
    if (j == 2) X.la = X.lb + P1_PART;
}
__device__ __forceinline__ void p1_top3(P1Ctx& X) {
    P1_WAITBAR(6);
    X.lb = X.w1 + p1_w1g(X, 2) * 64;
    X.la = X.lb; X.ld = X.ldst0; p1_piece(X, 0); p1_piece(X, 1); p1_piece(X, 2);
    X.la = X.lb + P1_PART; X.ld = X.ldst0 + P1_SLOT; p1_piece(X, 0); p1_piece(X, 1); p1_piece(X, 2);
    X.rs = 2 * P1_SLOT;
}
struct Epi { float a0, a1, u0, u1; u32 pw[4]; };
__device__ __forceinline__ void p1_gelu_slot(int s, const P1Ctx& X, const f32x16& pa, Epi& E, u32 vo) {
    const int k = s >> 2, ph = s & 3;
    if (ph == 0) { E.a0 = pa[2 * k]; E.a1 = pa[2 * k + 1]; E.u0 = __builtin_fmaf(E.a0 * E.a0, X.c1, X.c0); E.u1 = __builtin_fmaf(E.a1 * E.a1, X.c1, X.c0); PIN(E.u0); PIN(E.u1); }
    if (ph == 1) { E.u0 = __builtin_amdgcn_exp2f(E.a0 * E.u0); E.u1 = __builtin_amdgcn_exp2f(E.a1 * E.u1); PIN(E.u0); PIN(E.u1); }
    if (ph == 2) { E.u0 = __builtin_amdgcn_rcpf(__builtin_fmaf(E.u0, X.sd, X.sd)); E.u1 = __builtin_amdgcn_rcpf(__builtin_fmaf(E.u1, X.sd, X.sd)); PIN(E.u0); PIN(E.u1); }
    if (ph == 3) { E.pw[k & 3] = cvtpk(E.a0 * E.u0, E.a1 * E.u1);
        if ((k & 3) == 3) __builtin_amdgcn_raw_buffer_store_b128((u32x4){E.pw[0], E.pw[1], E.pw[2], E.pw[3]}, X.hrs, vo + (k >> 2) * 1024, 0, 0); }
}
__device__ __forceinline__ void p1_w1_chunk(P1Ctx& X, int i, int g, int pg, bool st, const bf16x8 (&xf)[36], bf16x8 (&wf)[4], f32x16& acc, f32x16& pacc) {
    const u32 vo = st ? X.l16 + (u32)pg * 2048 : 0x40000000u;
    Epi E; bf16x8 we, xe;
    lcptr p = X.trbase;
    SBAR();
#pragma unroll
    for (int s = 0; s < 36; ++s) {
        if (s == 0 || s == 12 || s == 24) { p1_top(X, i, s / 12); p = X.trbase + X.rs; PIN(p); }
        if (s < 3) { pacc = MFMA32(wf[(s + 1) & 3], xf[33 + s], pacc); PIN(pacc); }
        if (s == 3) { f32x16 z = {}; acc = MFMA32(we, xe, z); }
        if (s >= 3) { acc = MFMA32(wf[(s - 3) & 3], xf[s - 3], acc); PIN(acc); }
        if (s % 4 == 1) p1_piece_a(X, (s % 12) >> 2);
        if (s == 0) we = *(const LAS bf16x8*)(X.wexb + (g - 9 * X.c) * X.wexs);
        if (s == 1) xe = *(const LAS bf16x8*)X.xeb;
        wf[s & 3] = cat8(trd(p + (s % 12) * 2048), trd(p + (s % 12) * 2048 + 512));
        if (s % 4 == 1) p1_piece_b(X, (s % 12) >> 2);
        if (s >= 4) p1_gelu_slot(s - 4, X, pacc, E, vo);
        SBAR();
    }
}
template <bool SWAP, int J, bool LD>
__device__ __forceinline__ void p1_qkv_stage(P1Ctx& X, lcptr p, const bf16x8 (&xf)[36], f32x16& acc) {
    bf16x8 wf[4];
    PIN(p);
#pragma unroll
    for (int s = 0; s < 12 + 3; ++s) {
        if (s < 12) wf[s & 3] = cat8(trd(p + s * 2048), trd(p + s * 2048 + 512));
        if (s >= 3) { const int u = s - 3;
            if (J == 0 && u == 0) { f32x16 z = {}; acc = SWAP ? MFMA32(xf[0], wf[0], z) : MFMA32(wf[0], xf[0], z); }
            else acc = SWAP ? MFMA32(xf[J * 12 + u], wf[u & 3], acc) : MFMA32(wf[u & 3], xf[J * 12 + u], acc); }
        if (LD && (s == 4 || s == 8 || s == 12)) p1_piece(X, (s >> 2) - 1);
        SBAR();
    }
}
__device__ __forceinline__ void p1_qkv_store(char* out, int lane, const f32x16& a, float sc) {
    u32 w[8];
#pragma unroll
    for (int k = 0; k < 8; ++k) w[k] = cvtpk(a[2 * k] * sc, a[2 * k + 1] * sc);
    *(u32x4*)(out + lane * 16) = (u32x4){w[0], w[1], w[2], w[3]};
    *(u32x4*)(out + 1024 + lane * 16) = (u32x4){w[4], w[5], w[6], w[7]};
}

__global__ __launch_bounds__(512, 2) void p1_kernel(const u16* __restrict__ x, const u16* __restrict__ wqkv, const u16* __restrict__ w1,
                                                     const u32x4* __restrict__ rowrec, const u32x4* __restrict__ wext, u16* __restrict__ qkvb, u16* __restrict__ hb, unsigned long long* stamps) {
    STAMP_DECL STAMP(0);
    extern __shared__ __attribute__((aligned(16))) char smem[];
    const int tid = threadIdx.x, lane = tid & 63, wid = RFL(tid >> 6);
    const int xcd = blockIdx.x & 7, idx = blockIdx.x >> 3;
    const int rb = idx & 15, c = 2 * xcd + (idx >> 4);
    const u32 lds0 = (u32)(uintptr_t)smem;
    const int r8 = lane >> 3, cp = lane & 7;
    const int csw = cp ^ (((r8 >> 1) & 1) << 2);
    int gq0, gq1, gs;
    if (c < 6) { gq0 = 4 * c; gq1 = 4 * c + 2; gs = 0; } else { const int q0 = 24 + 3 * (c - 6), odd = (c - 6) & 1; gq0 = q0 + odd; gq1 = 0; gs = odd ? q0 : q0 + 2; }
    const u32 voffQ = (u32)(wid * 8 + r8) * (NQKV_ * 2) + csw * 16;
    const char* wq0 = (const char*)wqkv + gq0 * 64;
    const char* xs = (const char*)x + (size_t)rb * 128 * (D_ * 2);
    u32 voffX[2];
#pragma unroll
    for (int i = 0; i < 2; ++i) { const int row = 16 * wid + 8 * i + r8; voffX[i] = (u32)row * (D_ * 2) + ((cp ^ ((row >> 1) & 7)) << 4); }
    const u32 XA = lds0 + P1_SLOT, XB = lds0 + 4 * P1_SLOT;
#define P1_LDQ(sb, j, slot) _Pragma("unroll") for (int k = 0; k < 3; ++k) glds16s((sb) + (size_t)((j) * 192 + k * 64) * (NQKV_ * 2), voffQ, lds0 + (slot) * P1_SLOT + wid * 1024 + k * 8192)
#define P1_LDX(piece, dst) _Pragma("unroll") for (int i = 0; i < 2; ++i) glds16s(xs + (piece) * 128, voffX[i], (dst) + (16 * wid + 8 * i) * 128)
    P1_LDQ(wq0, 0, 0);
    P1_LDX(0, XA); P1_LDX(1, XA + 16384); P1_LDX(2, XA + 32768);
    P1_LDQ(wq0, 1, 3);
    P1_LDX(3, XB); P1_LDX(4, XB + 16384); P1_LDX(5, XB + 32768);
    if (wid * 64 + lane < 288) glds16(wext + 288 * c + wid * 64 + lane, lds0 + P1_WEXT + wid * 1024);
    if (wid >= 4) glds16(rowrec + rb * 256 + (wid - 4) * 64 + lane, lds0 + P1_ROW + (wid - 4) * 1024);
    if (tid < 4) *(LAS u32x4*)((lptr)(size_t)lds0 + P1_WEXT + 288 * 16 + tid * 16) = (u32x4){0u, 0u, 0u, 0u};
    const int rg = wid & 3, ch = wid >> 2;
    P1Ctx X; X.w1 = (const char*)w1; X.hrs = __builtin_amdgcn_make_buffer_rsrc((char*)hb + (size_t)(rb * 4 + rg) * 144 * 2048, 0, 144 * 2048, 0x00020000); X.c = c; X.l16 = lane * 16;
    const int nch = c < 6 ? 7 : 6; X.nch = nch; X.g1first = 9 * c + (c & 1);
    const int m = lane & 31, h = lane >> 5;
#pragma unroll
    for (int k = 0; k < 3; ++k) X.voffW[k] = (u32)(wid * 8 + r8 + 64 * k) * (F_ * 2) + csw * 16;
    X.ldst0 = lds0 + wid * 1024; X.rs = 0; X.ld = X.ldst0; X.lb = X.w1; X.la = X.w1;
    { const int s16 = (lane >> 4) & 1, q = (lane & 15) >> 2, p = lane & 3;
      X.trbase = (lcptr)(size_t)lds0 + (8 * h + q) * 128 + (((4 * ch + 2 * s16 + (p >> 1)) ^ (((q >> 1) & 1) << 2)) * 16) + (p & 1) * 8;
      X.wexb = (lcptr)(size_t)lds0 + (h ? P1_WEXT + 288 * 16 : P1_WEXT + m * 16); X.wexs = h ? 0 : 512; }
    const int wsingle = (c & 1) ? 9 * c : 9 * c + 8;
    bf16x8 xf[36];
    f32x16 accA, accB;
    const bool swp = c >= 10;
    const int xrow = rg * 32 + m, xz = (xrow >> 1) & 7;
    lcptr xa = (lcptr)(size_t)XA + xrow * 128, xb = (lcptr)(size_t)XB + xrow * 128;
#define P1_XF(s0, base) _Pragma("unroll") for (int s = 0; s < 12; ++s) xf[s0 + s] = *(const LAS bf16x8*)((base) + (s >> 2) * 16384 + (((2 * (s & 3) + h) ^ xz) << 4))
#define P1_QS(J, LD, P) do { if (swp) p1_qkv_stage<true, J, LD>(X, P, xf, accA); else p1_qkv_stage<false, J, LD>(X, P, xf, accA); } while (0)
    STAMP(1);
    P1_WAITBAR(9);
    STAMP(2);
    P1_XF(0, xa);
    P1_WAITBAR(9);
    P1_LDX(6, XA); P1_LDX(7, XA + 16384); P1_LDX(8, XA + 32768);
    P1_QS(0, false, X.trbase);
    STAMP(3);
    P1_WAITBAR(6);
    STAMP(4);
    P1_LDQ(wq0, 2, 0);
    { lcptr rr = (lcptr)(size_t)lds0 + P1_ROW + xrow * 32;
      const u32x4 b = *(const LAS u32x4*)(rr + 16);
      X.xeb = h ? (lcptr)(size_t)lds0 + P1_WEXT + 288 * 16 : rr;
      X.c0 = __uint_as_float(b[0]); X.c1 = __uint_as_float(b[1]); X.sd = __uint_as_float(b[2]); }
    P1_XF(12, xb);
    P1_QS(1, false, X.trbase + 3 * P1_SLOT);
    STAMP(5);
    P1_WAITBAR(3);
    STAMP(6);
    if (c < 6) { const char* wq1 = (const char*)wqkv + gq1 * 64; P1_LDQ(wq1, 0, 3); P1_LDQ(wq1, 1, 4); P1_LDQ(wq1, 2, 5); }
    else {
        const bool hi = csw >= 4; const u32 pitch = hi ? F_ * 2 : NQKV_ * 2;
        const char* lp = (hi ? (const char*)w1 + wsingle * 64 : (const char*)wqkv + gs * 64) + (size_t)((u32)(wid * 8 + r8) * pitch) + (csw & 3) * 16;
#pragma unroll
        for (int k = 0; k < 9; ++k) { glds16(lp, lds0 + 3 * P1_SLOT + wid * 1024 + k * 8192); lp += pitch * 64; }
    }
    P1_XF(24, xa);
    P1_WAITBAR(9);
    P1_QS(2, false, X.trbase);
    char* qout = (char*)qkvb + (size_t)(rb * 4 + rg) * 54 * 2048;
    p1_qkv_store(qout + (gq0 + ch) * 2048, lane, accA, gq0 + ch < 18 ? QSCALE : 1.f);
    STAMP(7);
    const bool c1q = c < 6 || ch == 0;
    p1_top3(X);
    if (c1q) {
        p1_top(X, 1, 0); P1_QS(0, true, X.trbase + X.rs);
        p1_top(X, 1, 1); P1_QS(1, true, X.trbase + X.rs);
        p1_top(X, 1, 2); P1_QS(2, true, X.trbase + X.rs);
        const int g = c < 6 ? gq1 + ch : gs;
        p1_qkv_store(qout + g * 2048, lane, accA, g < 18 ? QSCALE : 1.f);
    }
    STAMP(8);
    const int i1 = c1q ? 2 : 1;
    const int iend = (nch == 7 && ch == 1) ? 6 : nch;
#define P1_G(i) ((i) == 1 ? wsingle : p1_w1g(X, i) + ((i) < 6 ? ch : 0))
    bf16x8 wf[4];
#pragma unroll
    for (int k = 0; k < 4; ++k) wf[k] = xf[k];
#define P1_ST1
#define P1_ST2
    for (int i = i1; i < iend; i += 2) {
        p1_w1_chunk(X, i, P1_G(i), P1_G(i - 1), i > i1, xf, wf, accA, accB); P1_ST1
        if (i + 1 < iend) p1_w1_chunk(X, i + 1, P1_G(i + 1), P1_G(i), true, xf, wf, accB, accA); P1_ST2
    }
    int ie = iend; asm volatile("" : "+s"(ie));
    if (((ie - i1) & 1) != 0) accB = accA;
    if (ie < nch) P1_WAITBAR(0);
    { Epi E; const u32 vo = X.l16 + (u32)P1_G(ie - 1) * 2048;
#pragma unroll
      for (int s = 0; s < 3; ++s) accB = MFMA32(wf[(s + 1) & 3], xf[33 + s], accB);
#pragma unroll
      for (int s = 0; s < 32; ++s) p1_gelu_slot(s, X, accB, E, vo); }
    if (ie < nch) { P1_WAITBAR(0); P1_WAITBAR(0); }
    asm volatile("s_waitcnt vmcnt(0)" ::: "memory");
    STAMP(15);
    STAMP_OUT(stamps, 16);
}
constexpr int AT_K = 0, AT_V = 12 * 4096, AT_Q = 24 * 4096, AT_LDS = AT_Q + 4 * 4096;
__device__ __forceinline__ int qk_off(int c) { const int cc = c & 31; return (c >> 5) * 2048 + (2 * (cc >> 4) + ((cc >> 2) & 1)) * 512 + (((cc >> 3) & 1) * 4 + (cc & 3)) * 2; }
__device__ __forceinline__ float xmax16(float v) { auto r = __builtin_amdgcn_permlane16_swap(__float_as_uint(v), __float_as_uint(v), false, false); return fmaxf(__uint_as_float(r[0]), __uint_as_float(r[1])); }
__device__ __forceinline__ float xmax32(float v) { auto r = __builtin_amdgcn_permlane32_swap(__float_as_uint(v), __float_as_uint(v), false, false); return fmaxf(__uint_as_float(r[0]), __uint_as_float(r[1])); }
__device__ __forceinline__ float max3f(float a, float b, float c) { return __builtin_fmaxf(__builtin_fmaxf(a, b), c); }

template <bool EARLY>
__device__ __forceinline__ void attn_unit(lcptr L, int c0  , int cones, int w, int lane, int Q0, float gate, char* ob, int head) {
    const int i16 = lane & 15, g = lane >> 4;
    const int lb0 = w >> 1;
    const int o0 = qk_off(c0 + 2 * g), o1 = qk_off(c0 + 2 * g + 8), o2 = qk_off(c0 + 16);
    bf16x8 qf;
    { lcptr qp = L + AT_Q + (w >> 1) * 4096 + ((w & 1) * 16 + i16) * 16;
      const u32 a = *(const LAS u32*)(qp + o0), b = *(const LAS u32*)(qp + o1);
      u32 c = *(const LAS u32*)(qp + o2); if (g != 0) c = 0u;
      qf = __builtin_bit_cast(bf16x8, (u32x4){a, b, c, 0u}); }
    lcptr kb0 = L + AT_K + lb0 * 4096 + i16 * 16;
    lcptr k0 = kb0 + o0, k1 = kb0 + o1, k2 = kb0 + o2;
    PIN(k0); PIN(k1); PIN(k2);
    f32x4 s[18];
#pragma unroll
    for (int t = 0; t < 18; ++t) {
        const int o = (t >> 1) * 4096 + (t & 1) * 256;
        const u32x4 kw = {*(const LAS u32*)(k0 + o), *(const LAS u32*)(k1 + o), *(const LAS u32*)(k2 + o), 0u};
        s[t] = MFMA16(__builtin_bit_cast(bf16x8, kw), qf, ((f32x4){0.f, 0.f, 0.f, 0.f}));
    }
    const int q = Q0 + 16 * w + i16, kb = Q0 - 256 + lb0 * 32 + 4 * g;
    const int dq = q - kb;
#pragma unroll
    for (int t = 0; t < 18; ++t) {
        if (EARLY) {
#pragma unroll
            for (int r = 0; r < 4; ++r) { const int d = dq - 16 * t - r; const bool ok = (u32)d <= 255u && (kb + 16 * t + r) >= 0; s[t][r] = ok ? s[t][r] : -INFINITY; }
        } else if (t < 2) {
#pragma unroll
            for (int r = 0; r < 4; ++r) s[t][r] = (dq - 16 * t - r <= 255) ? s[t][r] : -INFINITY;
        } else if (t >= 16) {
#pragma unroll
            for (int r = 0; r < 4; ++r) s[t][r] = (dq - 16 * t - r >= 0) ? s[t][r] : -INFINITY;
        }
    }
    float m0 = max3f(s[0][0], s[0][1], s[0][2]), m1 = max3f(s[0][3], s[1][0], s[1][1]);
    m0 = max3f(m0, s[1][2], s[1][3]);
#pragma unroll
    for (int t = 2; t < 18; t += 2) { m0 = max3f(m0, s[t][0], s[t][1]); m1 = max3f(m1, s[t][2], s[t][3]); m0 = max3f(m0, s[t + 1][0], s[t + 1][1]); m1 = max3f(m1, s[t + 1][2], s[t + 1][3]); }
    const float m = xmax32(xmax16(fmaxf(m0, m1)));
    const int ca = c0 + i16, cb = (i16 < 2) ? c0 + 16 + i16 : (i16 == 2 ? cones : c0);
    lcptr va = L + AT_V + lb0 * 4096 + (ca >> 5) * 2048 + (g & 1) * 512 + (ca & 31) * 16 + (g >> 1) * 8;
    lcptr vb = L + AT_V + lb0 * 4096 + (cb >> 5) * 2048 + (g & 1) * 512 + (cb & 31) * 16 + (g >> 1) * 8;
    PIN(va); PIN(vb);
    f32x4 oa = {0.f, 0.f, 0.f, 0.f}, obb = {0.f, 0.f, 0.f, 0.f};
#pragma unroll
    for (int b = 0; b < 9; ++b) {
        const u32x2 a0 = *(const LAS u32x2*)(va + b * 4096), a1 = *(const LAS u32x2*)(va + b * 4096 + 1024);
        const u32x2 b0 = *(const LAS u32x2*)(vb + b * 4096), b1 = *(const LAS u32x2*)(vb + b * 4096 + 1024);
        u32x4 pw;
        pw[0] = cvtpk_c(__builtin_amdgcn_exp2f(s[2 * b][0] - m), __builtin_amdgcn_exp2f(s[2 * b][1] - m));
        pw[1] = cvtpk_c(__builtin_amdgcn_exp2f(s[2 * b][2] - m), __builtin_amdgcn_exp2f(s[2 * b][3] - m));
        pw[2] = cvtpk_c(__builtin_amdgcn_exp2f(s[2 * b + 1][0] - m), __builtin_amdgcn_exp2f(s[2 * b + 1][1] - m));
        pw[3] = cvtpk_c(__builtin_amdgcn_exp2f(s[2 * b + 1][2] - m), __builtin_amdgcn_exp2f(s[2 * b + 1][3] - m));
        const bf16x8 pf = __builtin_bit_cast(bf16x8, pw);
        oa = MFMA16(__builtin_bit_cast(bf16x8, ((u32x4){a0[0], a0[1], a1[0], a1[1]})), pf, oa);
        obb = MFMA16(__builtin_bit_cast(bf16x8, ((u32x4){b0[0], b0[1], b1[0], b1[1]})), pf, obb);
    }
    const float l = __shfl(obb[2], i16);
    const float sc = gate * __builtin_amdgcn_rcpf(l);
    char* base = ob + (size_t)(q >> 5) * 18 * 2048 + (q & 31) * 16;
    const int col = 18 * head + 4 * g;
    auto off = [](int c) { const int cc = c & 31; return (c >> 5) * 2048 + (2 * (cc >> 4) + ((cc >> 2) & 1)) * 512 + (((cc >> 3) & 1) * 4 + (cc & 3)) * 2; };
    *(u32*)(base + off(col)) = cvtpk(oa[0] * sc, oa[1] * sc);
    *(u32*)(base + off(col + 2)) = cvtpk(oa[2] * sc, oa[3] * sc);
    if (g == 0) *(u32*)(base + off(18 * head + 16)) = cvtpk(obb[0] * sc, obb[1] * sc);
}

__global__ __launch_bounds__(1024, 4) void attn_kernel(const u16* __restrict__ qkvb, const float* __restrict__ st, u16* __restrict__ ob, unsigned long long* stamps) {
    STAMP_DECL STAMP(0);
    extern __shared__ __attribute__((aligned(16))) char smem[];
    const int tid = threadIdx.x, lane = tid & 63, wid = RFL(tid >> 6);
    const int qb = blockIdx.x >> 4, hp = blockIdx.x & 15;
    const int Q0 = qb * 128, cg0 = (36 * hp) >> 5, c0 = 36 * hp - 32 * cg0;
    const u32 lds0 = (u32)(uintptr_t)smem;
#pragma unroll
    for (int i = 0; i < 4; ++i) {
        const int p = wid + 16 * i;
        if (p < 56) {
            const int u = p >> 1, gsel = p & 1;
            const int sec = u < 12 ? 1 : (u < 24 ? 2 : 0), ub = u < 12 ? u : (u < 24 ? u - 12 : u - 24);
            int rb = qb * 4 + ub - (sec ? 8 : 0); if (rb < 0) rb = 0;
            glds16s_off1k((const char*)qkvb + (size_t)(rb * 54 + sec * 18 + cg0 + gsel) * 2048, lane * 16, lds0 + u * 4096 + gsel * 2048);
        }
    }
    const int w = wid & 7, hsel = wid >> 3;
    const int q = Q0 + 16 * w + (lane & 15);
    const float gate = st[q * 8 + 7];
    STAMP(1);
    asm volatile("s_waitcnt vmcnt(0)\n\ts_barrier" ::: "memory");
    STAMP(2);
    const int cones = (c0 + 36) & 63;
    if (tid < 48) { const int kb = tid >> 2, pc = tid & 3; *(LAS u32x4*)((lptr)(size_t)lds0 + AT_V + kb * 4096 + (cones >> 5) * 2048 + pc * 512 + (cones & 31) * 16) = (u32x4){0x3f803f80u, 0x3f803f80u, 0x3f803f80u, 0x3f803f80u}; }
    asm volatile("s_waitcnt lgkmcnt(0)\n\ts_barrier" ::: "memory");
    STAMP(3);
    if (qb < 2) attn_unit<true>((lcptr)(size_t)lds0, c0 + 18 * hsel, cones, w, lane, Q0, gate, (char*)ob, 2 * hp + hsel);
    else attn_unit<false>((lcptr)(size_t)lds0, c0 + 18 * hsel, cones, w, lane, Q0, gate, (char*)ob, 2 * hp + hsel);
    STAMP(4);
    STAMP_OUT(stamps, 5);
}
constexpr int P3_A = 32768, P3_B = 64 * 288, P3_SLOT = P3_A + P3_B, P3_LDS = 3 * P3_SLOT;
struct P3Ctx {
    const char* pA; const char* pB;
    const char* oA; const char* oB;
    u32 aStride;
    int tl, nst; u32 sl;
    u32 dA, dB;
    u32 vA, vB0, vB1, vB2;
    lcptr ab, bb;
};
__device__ __forceinline__ void glds_c2(int tl, int nst, const void* sbase, u32 voff, u32 lds_base) {
    u32 sv;
    asm volatile("s_cmp_ge_i32 %4, %5\n\ts_cbranch_scc1 1f\n\ts_mov_b32 %0, m0\n\ts_mov_b32 m0, %3\n\ts_nop 0\n\tglobal_load_lds_dwordx4 %1, %2\n\tglobal_load_lds_dwordx4 %1, %2 offset:1024\n\ts_mov_b32 m0, %0\n1:"
                 : "=&s"(sv) : "v"(voff), "s"(sbase), "s"(lds_base), "s"(tl), "s"(nst) : "memory", "scc");
}
__device__ __forceinline__ void glds_c1(int tl, int nst, const void* sbase, u32 voff, u32 lds_base) {
    u32 sv;
    asm volatile("s_cmp_ge_i32 %4, %5\n\ts_cbranch_scc1 1f\n\ts_mov_b32 %0, m0\n\ts_mov_b32 m0, %3\n\ts_nop 0\n\tglobal_load_lds_dwordx4 %1, %2\n\ts_mov_b32 m0, %0\n1:"
                 : "=&s"(sv) : "v"(voff), "s"(sbase), "s"(lds_base), "s"(tl), "s"(nst) : "memory", "scc");
}
__device__ __forceinline__ void glds_cq(int tl, int nst, const void* sbase, u32 voff, u32 lds_base) {
    u32 sv;
    asm volatile("s_cmp_ge_i32 %4, %5\n\ts_cbranch_scc1 1f\n\ts_mov_b32 %0, m0\n\ts_mov_b32 m0, %3\n\ts_mov_b64 exec, 0xffff\n\tglobal_load_lds_dwordx4 %1, %2\n\ts_mov_b64 exec, -1\n\ts_mov_b32 m0, %0\n1:"
                 : "=&s"(sv) : "v"(voff), "s"(sbase), "s"(lds_base), "s"(tl), "s"(nst) : "memory", "scc");
}
__device__ __forceinline__ void p3_load_part(P3Ctx& X, int k) {
    if (k == 0) glds_c2(X.tl, X.nst, X.pA, X.vA, X.dA + X.sl);
    if (k == 1) glds_c2(X.tl, X.nst, X.pA + 2048, X.vA, X.dA + X.sl + 2048);
    if (k == 2) glds_c1(X.tl, X.nst, X.pB, X.vB0, X.dB + X.sl);
    if (k == 3) glds_c1(X.tl, X.nst, X.pB, X.vB1, X.dB + X.sl + 1024);
    if (k == 4) { glds_cq(X.tl, X.nst, X.pB, X.vB2, X.dB + X.sl + 2048);
        X.tl += 1; const bool sw = X.tl == 9; X.pA = sw ? X.oA : X.pA + 4096; X.pB = sw ? X.oB : X.pB + 64 * 1152; X.sl = X.sl == 2 * P3_SLOT ? 0 : X.sl + P3_SLOT; }
}
struct P3Frag { bf16x8 h[4]; bf16x8 w[5]; };
__device__ __forceinline__ void p3_waitbar(int rem) {
    asm volatile("s_cmp_lt_i32 %0, 1\n\ts_cbranch_scc1 2f\n1:\ts_waitcnt vmcnt(7) lgkmcnt(0)\n\ts_barrier\n\t.subsection 1\n2:\ts_waitcnt vmcnt(0)\n\ts_branch 1b\n\t.subsection 0" :: "s"(rem) : "memory", "scc");
}
template <int NT, int HF, bool RD, bool MM>
__device__ __forceinline__ void p3_half(P3Ctx& X, int t, u32 so, P3Frag& R, const P3Frag& P, f32x4 (&acc)[4][5]) {
    lcptr a = X.ab + so + HF * 2048, b = X.bb + so + HF * (32 * 288);
    if (RD) { if (HF == 0) p3_waitbar(X.nst - 1 - t); PIN(a); PIN(b); }
    s16x4 lo;
    SBAR();
#pragma unroll
    for (int i = 0; i < 4 * NT; ++i) {
        if (MM) acc[i & 3][i >> 2] = MFMA16(P.w[i >> 2], P.h[i & 3], acc[i & 3][i >> 2]);
        if (RD) {
            if (i < 4) R.h[i] = *(const LAS bf16x8*)(a + (i >> 1) * 4096 + (i & 1) * 256);
            else if (i < 4 + 2 * NT) { const int j = i - 4; if (j & 1) R.w[j >> 1] = cat8(lo, trd(b + (j >> 1) * 32 + 8 * 288)); else lo = trd(b + (j >> 1) * 32); }
            if (HF == 0) { if (i == (NT == 5 ? 2 : 6)) p3_load_part(X, 0); if (i == (NT == 5 ? 10 : 13)) p3_load_part(X, 1); }
            else { if (i == (NT == 5 ? 1 : 4)) p3_load_part(X, 2); if (i == (NT == 5 ? 8 : 10)) p3_load_part(X, 3); if (i == (NT == 5 ? 15 : 14)) p3_load_part(X, 4); }
        }
        SBAR();
    }
}
template <int NT>
__device__ __forceinline__ void p3_loop(P3Ctx& X, int nhalf, f32x4 (&acc)[4][5]) {
    P3Frag F0, F1;
    u32 so = 0;
    p3_half<NT, 0, true, false>(X, 0, so, F0, F1, acc);
    p3_half<NT, 1, true, true>(X, 0, so, F1, F0, acc);
    int t = 1;
    for (; 2 * t + 1 < nhalf; ++t) {
        so = so == 2 * P3_SLOT ? 0 : so + P3_SLOT;
        p3_half<NT, 0, true, true>(X, t, so, F0, F1, acc);
        p3_half<NT, 1, true, true>(X, t, so, F1, F0, acc);
    }
    if (2 * t < nhalf) {
        so = so == 2 * P3_SLOT ? 0 : so + P3_SLOT;
        p3_half<NT, 0, true, true>(X, t, so, F0, F1, acc);
        p3_half<NT, 1, false, true>(X, t, so, F1, F0, acc);
    } else p3_half<NT, 0, false, true>(X, t, so, F0, F1, acc);
}
__global__ __launch_bounds__(512, 2) void p3_kernel(const u16* __restrict__ hb, const u16* __restrict__ ob, const u16* __restrict__ w2, const u16* __restrict__ wo,
                                                     u16* __restrict__ part, unsigned long long* stamps) {
    STAMP_DECL STAMP(0);
    extern __shared__ __attribute__((aligned(16))) char smem[];
    const int tid = threadIdx.x, lane = tid & 63, wid = RFL(tid >> 6);
    const int sp = blockIdx.x & 7, idx = blockIdx.x >> 3;
    const int mt = idx & 7, nt = idx >> 3;
    const u32 lds0 = (u32)(uintptr_t)smem;
    const int mq = wid >> 1, nh = (wid & 1) ^ (wid >> 2);
    const int i16 = lane & 15, kq = lane >> 4;
    P3Ctx X;
    const int no = (sp & 3) == 0 ? 3 : 2, o0 = (sp >> 2) * 9 + ((sp & 3) == 0 ? 0 : 1 + 2 * (sp & 3));
    const int nhalf = 18 + no; X.nst = (nhalf + 1) >> 1; X.tl = 0; X.sl = 0;
    X.pA = (const char*)hb + (size_t)((mt * 8 + wid) * 144 + 18 * sp) * 2048;
    X.oA = (const char*)ob + (size_t)((mt * 8 + wid) * 18 + o0) * 2048;
    X.pB = (const char*)w2 + (size_t)(576 * sp) * 1152 + 288 * nt;
    X.oB = (const char*)wo + (size_t)(32 * o0) * 1152 + 288 * nt;
    X.dA = lds0 + wid * 4096; X.dB = lds0 + P3_A + wid * 144 * 16;
    X.vA = lane * 16;
    { const int e0 = wid * 144 + lane, e1 = e0 + 64, e2 = e0 + 128;
      X.vB0 = (e0 / 18) * 1152 + (e0 % 18) * 16; X.vB1 = (e1 / 18) * 1152 + (e1 % 18) * 16; X.vB2 = (e2 / 18) * 1152 + (e2 % 18) * 16; }
    X.ab = (lcptr)(size_t)lds0 + (mq * 2) * 4096 + kq * 512 + i16 * 16;
    X.bb = (lcptr)(size_t)lds0 + P3_A + (16 * (kq >> 1) + 4 * (kq & 1) + (i16 >> 2)) * 288 + (nh ? 5 * 32 : 0) + (i16 & 3) * 8;
#pragma unroll
    for (int t = 0; t < 2; ++t)
#pragma unroll
        for (int k = 0; k < 5; ++k) p3_load_part(X, k);
    f32x4 acc[4][5];
#pragma unroll
    for (int m = 0; m < 4; ++m)
#pragma unroll
        for (int n = 0; n < 5; ++n) acc[m][n] = (f32x4){0.f, 0.f, 0.f, 0.f};
    STAMP(1);
    if (nh == 0) p3_loop<5>(X, nhalf, acc); else p3_loop<4>(X, nhalf, acc);
    STAMP(2);
    u16* pb = part + ((size_t)((sp * 8 + mt) * 4 + nt) * 144) * 256;
    const int NTN = nh ? 4 : 5;
#pragma unroll
    for (int m = 0; m < 4; ++m)
#pragma unroll
        for (int n = 0; n < 5; ++n)
            if (n < NTN) *(u32x2*)(pb + ((mq * 4 + m) * 9 + (nh ? 5 : 0) + n) * 256 + lane * 4) = (u32x2){cvtpk_c(acc[m][n][0], acc[m][n][1]), cvtpk_c(acc[m][n][2], acc[m][n][3])};
    STAMP(3);
    STAMP_OUT(stamps, 4);
}
__global__ __launch_bounds__(256) void k4_reduce(const u16* __restrict__ part, const float* __restrict__ st, const u16* __restrict__ b2, u16* __restrict__ out) {
    const int gid = blockIdx.x * 256 + threadIdx.x;
    const int l2 = gid & 31, f = gid >> 5;
    const int n = f % 9, mtile = (f / 9) & 15, nt = (f / 144) & 3, mt = f / 576;
    float v[8] = {0.f, 0.f, 0.f, 0.f, 0.f, 0.f, 0.f, 0.f};
    u32x4 p[8];
#pragma unroll
    for (int s = 0; s < 8; ++s) p[s] = *(const u32x4*)(part + (size_t)s * (8 * 4 * 144 * 256) + (size_t)gid * 8);
#pragma unroll
    for (int s = 0; s < 8; ++s)
#pragma unroll
        for (int j = 0; j < 4; ++j) { v[2 * j] += bflo(p[s][j]); v[2 * j + 1] += bfhi(p[s][j]); }
#pragma unroll
    for (int e = 0; e < 2; ++e) {
        const int lane = 2 * l2 + e;
        const int row = mt * 256 + mtile * 16 + (lane & 15), col = nt * 144 + n * 16 + (lane >> 4) * 4;
        const float og = 1.f - st[row * 8 + 7];
        const u32x2 bb = *(const u32x2*)(b2 + col);
        u32x2 o; o[0] = cvtpk(v[4 * e] + og * bflo(bb[0]), v[4 * e + 1] + og * bfhi(bb[0])); o[1] = cvtpk(v[4 * e + 2] + og * bflo(bb[1]), v[4 * e + 3] + og * bfhi(bb[1]));
        *(u32x2*)(out + (size_t)row * D_ + col) = o;
    }
}
extern "C" void kernel_launch(void* const* d_in, const int* in_sizes, int n_in, void* d_out, int out_size, void* d_ws, size_t ws_size, hipStream_t stream) {
    const u16* x = (const u16*)d_in[0]; const u16* wqkv = (const u16*)d_in[1]; const u16* wo = (const u16*)d_in[2];
    const u16* w1 = (const u16*)d_in[4]; const u16* b1 = (const u16*)d_in[5]; const u16* w2 = (const u16*)d_in[6]; const u16* b2 = (const u16*)d_in[7]; const u16* gw = (const u16*)d_in[8];
    char* ws = (char*)d_ws;
    u32x4* rowrec = (u32x4*)ws;
    u32x4* wext = (u32x4*)(ws + 65536);
    char* big = ws + 65536 + 73728 + 8192;
    u16* qkvb = (u16*)big;
    u16* hb = (u16*)(big + 7077888);
    u16* ob = (u16*)(big + 7077888 + 18874368);
    u16* part = (u16*)(big + 7077888 + 18874368 + 2359296);
    (void)hipFuncSetAttribute((const void*)p1_kernel, hipFuncAttributeMaxDynamicSharedMemorySize, P1_LDS);
    (void)hipFuncSetAttribute((const void*)attn_kernel, hipFuncAttributeMaxDynamicSharedMemorySize, AT_LDS);
    (void)hipFuncSetAttribute((const void*)p3_kernel, hipFuncAttributeMaxDynamicSharedMemorySize, P3_LDS);
    k0_stats<<<256 + 36, 512, 0, stream>>>(x, gw, w1, b1, rowrec, wext);
    p1_kernel<<<256, 512, P1_LDS, stream>>>(x, wqkv, w1, rowrec, wext, qkvb, hb, (unsigned long long*)(ws + (200u << 20)));
    attn_kernel<<<256, 1024, AT_LDS, stream>>>(qkvb, (const float*)rowrec, ob, (unsigned long long*)(ws + (202u << 20)));
    p3_kernel<<<256, 512, P3_LDS, stream>>>(hb, ob, w2, wo, part, (unsigned long long*)(ws + (201u << 20)));
    k4_reduce<<<576, 256, 0, stream>>>(part, (const float*)rowrec, b2, (u16*)d_out);
}
```

```cpp
#include <hip/hip_runtime.h>
#include <hip/hip_bf16.h>
typedef unsigned short u16;
typedef unsigned int u32;
#define LAS __attribute__((address_space(3)))
typedef short bf16x8 __attribute__((ext_vector_type(8)));
typedef short s16x4 __attribute__((ext_vector_type(4)));
typedef float f32x16 __attribute__((ext_vector_type(16)));
typedef float f32x4 __attribute__((ext_vector_type(4)));
typedef u32 u32x4 __attribute__((ext_vector_type(4)));
typedef u32 u32x2 __attribute__((ext_vector_type(2)));
typedef LAS char* lptr;
typedef LAS const char* lcptr;

constexpr int S_ = 2048, D_ = 576, H_ = 32, DH_ = 18, F_ = 4608, NQKV_ = 1728;

__device__ __forceinline__ float bf2f(u16 b) { return __uint_as_float(((u32)b) << 16); }
__device__ __forceinline__ float bflo(u32 w) { return __uint_as_float(w << 16); }
__device__ __forceinline__ float bfhi(u32 w) { return __uint_as_float(w & 0xffff0000u); }
typedef __bf16 bf16x2_t __attribute__((ext_vector_type(2)));
typedef float f32x2 __attribute__((ext_vector_type(2)));
__device__ __forceinline__ u32 cvtpk_c(float lo, float hi) { return __builtin_bit_cast(u32, __builtin_convertvector((f32x2){lo, hi}, bf16x2_t)); }
__device__ __forceinline__ u32 cvtpk(float lo, float hi) { u32 r; asm("v_cvt_pk_bf16_f32 %0, %1, %2" : "=v"(r) : "v"(lo), "v"(hi)); return r; }
__device__ __forceinline__ u16 f2bf(float f) { return (u16)(cvtpk(f, f) & 0xffffu); }
__device__ __forceinline__ void glds16(const void* g, u32 lds_base) {
    u32 sv;
    asm volatile("s_mov_b32 %0, m0\n\ts_mov_b32 m0, %2\n\ts_nop 0\n\tglobal_load_lds_dwordx4 %1, off\n\ts_mov_b32 m0, %0" : "=&s"(sv) : "v"(g), "s"(lds_base) : "memory");
}
__device__ __forceinline__ void glds16s(const void* sbase, u32 voff, u32 lds_base) {
    u32 sv;
    asm volatile("s_mov_b32 %0, m0\n\ts_mov_b32 m0, %3\n\ts_nop 0\n\tglobal_load_lds_dwordx4 %1, %2\n\ts_mov_b32 m0, %0" : "=&s"(sv) : "v"(voff), "s"(sbase), "s"(lds_base) : "memory");
}
__device__ __forceinline__ void glds16s_off1k(const void* sbase, u32 voff, u32 lds_base) {
    u32 sv;
    asm volatile("s_mov_b32 %0, m0\n\ts_mov_b32 m0, %3\n\ts_nop 0\n\tglobal_load_lds_dwordx4 %1, %2\n\tglobal_load_lds_dwordx4 %1, %2 offset:1024\n\ts_mov_b32 m0, %0" : "=&s"(sv) : "v"(voff), "s"(sbase), "s"(lds_base) : "memory");
}
__device__ __forceinline__ s16x4 trd(lcptr p) {
    typedef short v4i16_t __attribute__((ext_vector_type(4)));
    return __builtin_bit_cast(s16x4, __builtin_amdgcn_ds_read_tr16_b64_v4i16((LAS v4i16_t*)p));
}
__device__ __forceinline__ bf16x8 cat8(s16x4 a, s16x4 b) { return (bf16x8){a[0], a[1], a[2], a[3], b[0], b[1], b[2], b[3]}; }
#define MFMA32(a, b, c) __builtin_amdgcn_mfma_f32_32x32x16_bf16(a, b, c, 0, 0, 0)
#define MFMA16(a, b, c) __builtin_amdgcn_mfma_f32_16x16x32_bf16(a, b, c, 0, 0, 0)
#define SBAR() __builtin_amdgcn_sched_barrier(0)
#define PIN(x) asm volatile("" : "+v"(x))
#define RFL(x) __builtin_amdgcn_readfirstlane(x)

#define STAMP_DECL
#define STAMP(i)
#define STAMP_OUT(buf, n)
constexpr float GC0 = -2.f * 0.7978845608028654f * 1.4426950408889634f, GC1 = GC0 * 0.044715f;
__device__ __forceinline__ u32 bfbits(float f) { return cvtpk(f, 0.f) & 0xffffu; }
__global__ __launch_bounds__(512) void k0_stats(const u16* __restrict__ x, const u16* __restrict__ gw, const u16* __restrict__ w1, const u16* __restrict__ b1, u32x4* __restrict__ rowrec, u32x4* __restrict__ wext) {
    const int tid = threadIdx.x, lane = tid & 63, wid = tid >> 6;
    if (blockIdx.x < 256) {
        const int row = blockIdx.x * 8 + wid;
        const u32* xr = (const u32*)(x + row * D_);
        const u32* gr = (const u32*)gw;
        float s = 0.f, gs = 0.f;
        u32 w[5], g[5];
#pragma unroll
        for (int i = 0; i < 5; ++i) { const int j = lane + 64 * i; w[i] = (j < 288) ? xr[j] : 0u; g[i] = (j < 288) ? gr[j] : 0u; }
#pragma unroll
        for (int i = 0; i < 5; ++i) { s += bflo(w[i]) + bfhi(w[i]); gs += bflo(w[i]) * bflo(g[i]) + bfhi(w[i]) * bfhi(g[i]); }
#pragma unroll
        for (int o = 32; o >= 1; o >>= 1) { s += __shfl_xor(s, o); gs += __shfl_xor(gs, o); }
        const float m = s * (1.f / 576.f);
        float q = 0.f;
#pragma unroll
        for (int i = 0; i < 5; ++i) { const int j = lane + 64 * i; if (j < 288) { const float a = bflo(w[i]) - m, b = bfhi(w[i]) - m; q += a * a + b * b; } }
#pragma unroll
        for (int o = 32; o >= 1; o >>= 1) q += __shfl_xor(q, o);
        const float sd = sqrtf(q * (1.f / 576.f) + 1e-5f), r = 1.f / sd;
        if (lane == 0) {
            const u32 mh = bfbits(-m); const u32 ml = bfbits(-m - __uint_as_float(mh << 16));
            const u32 sh = bfbits(sd); const u32 sl = bfbits(sd - __uint_as_float(sh << 16));
            rowrec[2 * row] = (u32x4){mh | (mh << 16), ml | (sh << 16), sl, 0u};
            const float gate = 1.f / (1.f + __expf(-gs)), og = 1.f / (1.f + __expf(gs));
            rowrec[2 * row + 1] = (u32x4){__float_as_uint(GC0 * r), __float_as_uint(GC1 * r * r * r), __float_as_uint(sd / og), __float_as_uint(gate)};
        }
    } else {
        __shared__ float red[32][128];
        const int cb = (blockIdx.x - 256) * 128;
        const int cw = tid & 15, rr = tid >> 4;
        u32x4 v[18];
#pragma unroll
        for (int i = 0; i < 18; ++i) v[i] = *(const u32x4*)(w1 + (size_t)(rr + 32 * i) * F_ + cb + 8 * cw);
        float s[8] = {0.f, 0.f, 0.f, 0.f, 0.f, 0.f, 0.f, 0.f};
#pragma unroll
        for (int i = 0; i < 18; ++i)
#pragma unroll
            for (int j = 0; j < 4; ++j) { s[2 * j] += bflo(v[i][j]); s[2 * j + 1] += bfhi(v[i][j]); }
#pragma unroll
        for (int j = 0; j < 8; ++j) red[rr][8 * cw + j] = s[j];
        __syncthreads();
        if (tid < 128) { float t = 0.f; for (int i = 0; i < 32; ++i) t += red[i][tid];
            const u32 ch = bfbits(t), cl = bfbits(t - __uint_as_float(ch << 16)), bb = b1[cb + tid];
            wext[cb + tid] = (u32x4){ch | (cl << 16), ch | (bb << 16), bb, 0u}; }
    }
}
constexpr int P1_SLOT = 24576, P1_RING = 6 * P1_SLOT, P1_WEXT = P1_RING, P1_ROW = P1_WEXT + 288 * 16 + 64  , P1_LDS = P1_ROW + 128 * 32;
constexpr float QSCALE = 0.23570226039551584f * 1.4426950408889634f;
#define P1_WAITBAR(N) asm volatile("s_waitcnt vmcnt(" #N ") lgkmcnt(0)\n\ts_barrier" ::: "memory")

struct P1Ctx {
    const char* w1; __amdgpu_buffer_rsrc_t hrs;
    int c, nch, g1first; u32 l16;
    const char* lb; const char* la;
    u32 ld, ldst0;
    u32 voffW[3];
    lcptr trbase, wexb; u32 wexs; u32 rs;
    float c0, c1, sd; lcptr xeb;
};
constexpr u32 P1_PART = 192u * F_ * 2u;
__device__ __forceinline__ int p1_w1g(const P1Ctx& X, int i) { return i < 6 ? X.g1first + 2 * (i - 2) : ((i == 6 && X.nch == 7) ? ((X.c & 1) ? 9 * X.c : 9 * X.c + 8) : X.g1first); }
__device__ __forceinline__ void p1_piece_a(const P1Ctx& X, int k) { asm volatile("s_add_u32 m0, %0, %1" :: "s"(X.ld), "n"(k * 8192) : "memory", "scc"); }
__device__ __forceinline__ void p1_piece_b(const P1Ctx& X, int k) {
    asm volatile("global_load_lds_dwordx4 %0, %1" :: "v"(X.voffW[k]), "s"(X.la) : "memory");
}
__device__ __forceinline__ void p1_piece(const P1Ctx& X, int k) { p1_piece_a(X, k); asm volatile("s_nop 0"); p1_piece_b(X, k); }
__device__ __forceinline__ void p1_top(P1Ctx& X, int i, int j) {
    if (j == 0) asm volatile("s_cmp_eq_u32 %0, 1\n\ts_cbranch_scc1 3f\n\ts_waitcnt vmcnt(12) lgkmcnt(0)\n\ts_barrier\n3:" :: "s"(i) : "memory", "scc");
    else asm volatile("s_waitcnt vmcnt(12) lgkmcnt(0)\n\ts_barrier" ::: "memory");
    X.ld = X.ldst0 + X.rs;
    X.rs = X.rs == 5 * P1_SLOT ? 0 : X.rs + P1_SLOT;
    if (j == 0) X.la = X.lb + 2 * P1_PART;
    if (j == 1) { X.lb = X.w1 + p1_w1g(X, i + 2) * 64; X.la = X.lb; }
    if (j == 2) X.la = X.lb + P1_PART;
}
__device__ __forceinline__ void p1_top3(P1Ctx& X) {
    P1_WAITBAR(6);
    X.lb = X.w1 + p1_w1g(X, 2) * 64;
    X.la = X.lb; X.ld = X.ldst0; p1_piece(X, 0); p1_piece(X, 1); p1_piece(X, 2);
    X.la = X.lb + P1_PART; X.ld = X.ldst0 + P1_SLOT; p1_piece(X, 0); p1_piece(X, 1); p1_piece(X, 2);
    X.rs = 2 * P1_SLOT;
}
struct Epi { float a0, a1, u0, u1; u32 pw[4]; };
__device__ __forceinline__ void p1_gelu_slot(int s, const P1Ctx& X, const f32x16& pa, Epi& E, u32 vo) {
    const int k = s >> 2, ph = s & 3;
    if (ph == 0) { E.a0 = pa[2 * k]; E.a1 = pa[2 * k + 1]; E.u0 = __builtin_fmaf(E.a0 * E.a0, X.c1, X.c0); E.u1 = __builtin_fmaf(E.a1 * E.a1, X.c1, X.c0); PIN(E.u0); PIN(E.u1); }
    if (ph == 1) { E.u0 = __builtin_amdgcn_exp2f(E.a0 * E.u0); E.u1 = __builtin_amdgcn_exp2f(E.a1 * E.u1); PIN(E.u0); PIN(E.u1); }
    if (ph == 2) { E.u0 = __builtin_amdgcn_rcpf(__builtin_fmaf(E.u0, X.sd, X.sd)); E.u1 = __builtin_amdgcn_rcpf(__builtin_fmaf(E.u1, X.sd, X.sd)); PIN(E.u0); PIN(E.u1); }
    if (ph == 3) { E.pw[k & 3] = cvtpk(E.a0 * E.u0, E.a1 * E.u1);
        if ((k & 3) == 3) __builtin_amdgcn_raw_buffer_store_b128((u32x4){E.pw[0], E.pw[1], E.pw[2], E.pw[3]}, X.hrs, vo + (k >> 2) * 1024, 0, 0); }
}
__device__ __forceinline__ void p1_w1_chunk(P1Ctx& X, int i, int g, int pg, bool st, const bf16x8 (&xf)[36], bf16x8 (&wf)[6], f32x16& acc, f32x16& pacc) {
    const u32 vo = st ? X.l16 + (u32)pg * 2048 : 0x40000000u;
    Epi E; bf16x8 we, xe;
    lcptr p = X.trbase;
    SBAR();
#pragma unroll
    for (int s = 0; s < 36; ++s) {
        if (s == 0 || s == 12 || s == 24) { p1_top(X, i, s / 12); p = X.trbase + X.rs; PIN(p); }
        if (s < 4) { pacc = MFMA32(wf[(s + 2) % 6], xf[32 + s], pacc); PIN(pacc); }
        if (s == 4) { f32x16 z = {}; acc = MFMA32(we, xe, z); }
        if (s >= 4) { acc = MFMA32(wf[(s - 4) % 6], xf[s - 4], acc); PIN(acc); }
        if (s % 4 == 2) p1_piece_a(X, (s % 12) >> 2);
        if (s == 1) { we = *(const LAS bf16x8*)(X.wexb + (g - 9 * X.c) * X.wexs); xe = *(const LAS bf16x8*)X.xeb; }
        if ((s & 1) == 0) {
            wf[(s + 1) % 6] = cat8(trd(p + ((s + 1) % 12) * 2048), trd(p + ((s + 1) % 12) * 2048 + 512));
            wf[s % 6] = cat8(trd(p + (s % 12) * 2048), trd(p + (s % 12) * 2048 + 512));
        }
        if (s % 4 == 2) p1_piece_b(X, (s % 12) >> 2);
        if (s >= 5 && s < 36) p1_gelu_slot(s - 5, X, pacc, E, vo);
        SBAR();
    }
    p1_gelu_slot(31, X, pacc, E, vo);
}
template <bool SWAP, int J, bool LD>
__device__ __forceinline__ void p1_qkv_stage(P1Ctx& X, lcptr p, const bf16x8 (&xf)[36], f32x16& acc) {
    bf16x8 wf[4];
    PIN(p);
#pragma unroll
    for (int s = 0; s < 12 + 3; ++s) {
        if (s < 12) wf[s & 3] = cat8(trd(p + s * 2048), trd(p + s * 2048 + 512));
        if (s >= 3) { const int u = s - 3;
            if (J == 0 && u == 0) { f32x16 z = {}; acc = SWAP ? MFMA32(xf[0], wf[0], z) : MFMA32(wf[0], xf[0], z); }
            else acc = SWAP ? MFMA32(xf[J * 12 + u], wf[u & 3], acc) : MFMA32(wf[u & 3], xf[J * 12 + u], acc); }
        if (LD && (s == 4 || s == 8 || s == 12)) p1_piece(X, (s >> 2) - 1);
        SBAR();
    }
}
__device__ __forceinline__ void p1_qkv_store(char* out, int lane, const f32x16& a, float sc) {
    u32 w[8];
#pragma unroll
    for (int k = 0; k < 8; ++k) w[k] = cvtpk(a[2 * k] * sc, a[2 * k + 1] * sc);
    *(u32x4*)(out + lane * 16) = (u32x4){w[0], w[1], w[2], w[3]};
    *(u32x4*)(out + 1024 + lane * 16) = (u32x4){w[4], w[5], w[6], w[7]};
}

__global__ __launch_bounds__(512, 2) void p1_kernel(const u16* __restrict__ x, const u16* __restrict__ wqkv, const u16* __restrict__ w1,
                                                     const u32x4* __restrict__ rowrec, const u32x4* __restrict__ wext, u16* __restrict__ qkvb, u16* __restrict__ hb, unsigned long long* stamps) {
    STAMP_DECL STAMP(0);
    extern __shared__ __attribute__((aligned(16))) char smem[];
    const int tid = threadIdx.x, lane = tid & 63, wid = RFL(tid >> 6);
    const int xcd = blockIdx.x & 7, idx = blockIdx.x >> 3;
    const int rb = (xcd & 1) * 8 + (idx & 7), c = (xcd >> 1) * 4 + (idx >> 3);
    const u32 lds0 = (u32)(uintptr_t)smem;
    const int r8 = lane >> 3, cp = lane & 7;
    const int csw = cp ^ (((r8 >> 1) & 1) << 2);
    int gq0, gq1, gs;
    if (c < 6) { gq0 = 4 * c; gq1 = 4 * c + 2; gs = 0; } else { const int q0 = 24 + 3 * (c - 6), odd = (c - 6) & 1; gq0 = q0 + odd; gq1 = 0; gs = odd ? q0 : q0 + 2; }
    const u32 voffQ = (u32)(wid * 8 + r8) * (NQKV_ * 2) + csw * 16;
    const char* wq0 = (const char*)wqkv + gq0 * 64;
    const char* xs = (const char*)x + (size_t)rb * 128 * (D_ * 2);
    u32 voffX[2];
#pragma unroll
    for (int i = 0; i < 2; ++i) { const int row = 16 * wid + 8 * i + r8; voffX[i] = (u32)row * (D_ * 2) + ((cp ^ ((row >> 1) & 7)) << 4); }
    const u32 XA = lds0 + P1_SLOT, XB = lds0 + 4 * P1_SLOT;
#define P1_LDQ(sb, j, slot) _Pragma("unroll") for (int k = 0; k < 3; ++k) glds16s((sb) + (size_t)((j) * 192 + k * 64) * (NQKV_ * 2), voffQ, lds0 + (slot) * P1_SLOT + wid * 1024 + k * 8192)
#define P1_LDX(piece, dst) _Pragma("unroll") for (int i = 0; i < 2; ++i) glds16s(xs + (piece) * 128, voffX[i], (dst) + (16 * wid + 8 * i) * 128)
    P1_LDQ(wq0, 0, 0);
    P1_LDX(0, XA); P1_LDX(1, XA + 16384); P1_LDX(2, XA + 32768);
    asm volatile("s_barrier" ::: "memory");
    P1_LDQ(wq0, 1, 3);
    P1_LDX(3, XB); P1_LDX(4, XB + 16384); P1_LDX(5, XB + 32768);
    if (wid * 64 + lane < 288) glds16(wext + 288 * c + wid * 64 + lane, lds0 + P1_WEXT + wid * 1024);
    if (wid >= 4) glds16(rowrec + rb * 256 + (wid - 4) * 64 + lane, lds0 + P1_ROW + (wid - 4) * 1024);
    if (tid < 4) *(LAS u32x4*)((lptr)(size_t)lds0 + P1_WEXT + 288 * 16 + tid * 16) = (u32x4){0u, 0u, 0u, 0u};
    const int rg = wid & 3, ch = wid >> 2;
    P1Ctx X; X.w1 = (const char*)w1; X.hrs = __builtin_amdgcn_make_buffer_rsrc((char*)hb + (size_t)(rb * 4 + rg) * 144 * 2048, 0, 144 * 2048, 0x00020000); X.c = c; X.l16 = lane * 16;
    const int nch = c < 6 ? 7 : 6; X.nch = nch; X.g1first = 9 * c + (c & 1);
    const int m = lane & 31, h = lane >> 5;
#pragma unroll
    for (int k = 0; k < 3; ++k) X.voffW[k] = (u32)(wid * 8 + r8 + 64 * k) * (F_ * 2) + csw * 16;
    X.ldst0 = lds0 + wid * 1024; X.rs = 0; X.ld = X.ldst0; X.lb = X.w1; X.la = X.w1;
    { const int s16 = (lane >> 4) & 1, q = (lane & 15) >> 2, p = lane & 3;
      X.trbase = (lcptr)(size_t)lds0 + (8 * h + q) * 128 + (((4 * ch + 2 * s16 + (p >> 1)) ^ (((q >> 1) & 1) << 2)) * 16) + (p & 1) * 8;
      X.wexb = (lcptr)(size_t)lds0 + (h ? P1_WEXT + 288 * 16 : P1_WEXT + m * 16); X.wexs = h ? 0 : 512; }
    const int wsingle = (c & 1) ? 9 * c : 9 * c + 8;
    bf16x8 xf[36];
    f32x16 accA, accB;
    const bool swp = c >= 10;
    const int xrow = rg * 32 + m, xz = (xrow >> 1) & 7;
    lcptr xa = (lcptr)(size_t)XA + xrow * 128, xb = (lcptr)(size_t)XB + xrow * 128;
#define P1_XF(s0, base) _Pragma("unroll") for (int s = 0; s < 12; ++s) xf[s0 + s] = *(const LAS bf16x8*)((base) + (s >> 2) * 16384 + (((2 * (s & 3) + h) ^ xz) << 4))
#define P1_QS(J, LD, P) do { if (swp) p1_qkv_stage<true, J, LD>(X, P, xf, accA); else p1_qkv_stage<false, J, LD>(X, P, xf, accA); } while (0)
    STAMP(1);
    P1_WAITBAR(9);
    STAMP(2);
    P1_XF(0, xa);
    P1_WAITBAR(9);
    P1_LDX(6, XA); P1_LDX(7, XA + 16384); P1_LDX(8, XA + 32768);
    P1_QS(0, false, X.trbase);
    STAMP(3);
    P1_WAITBAR(6);
    STAMP(4);
    P1_LDQ(wq0, 2, 0);
    { lcptr rr = (lcptr)(size_t)lds0 + P1_ROW + xrow * 32;
      const u32x4 b = *(const LAS u32x4*)(rr + 16);
      X.xeb = h ? (lcptr)(size_t)lds0 + P1_WEXT + 288 * 16 : rr;
      X.c0 = __uint_as_float(b[0]); X.c1 = __uint_as_float(b[1]); X.sd = __uint_as_float(b[2]); }
    P1_XF(12, xb);
    P1_QS(1, false, X.trbase + 3 * P1_SLOT);
    STAMP(5);
    P1_WAITBAR(3);
    STAMP(6);
    if (c < 6) { const char* wq1 = (const char*)wqkv + gq1 * 64; P1_LDQ(wq1, 0, 3); P1_LDQ(wq1, 1, 4); P1_LDQ(wq1, 2, 5); }
    else {
        const bool hi = csw >= 4; const u32 pitch = hi ? F_ * 2 : NQKV_ * 2;
        const char* lp = (hi ? (const char*)w1 + wsingle * 64 : (const char*)wqkv + gs * 64) + (size_t)((u32)(wid * 8 + r8) * pitch) + (csw & 3) * 16;
#pragma unroll
        for (int k = 0; k < 9; ++k) { glds16(lp, lds0 + 3 * P1_SLOT + wid * 1024 + k * 8192); lp += pitch * 64; }
    }
    P1_XF(24, xa);
    P1_WAITBAR(9);
    P1_QS(2, false, X.trbase);
    char* qout = (char*)qkvb + (size_t)(rb * 4 + rg) * 54 * 2048;
    p1_qkv_store(qout + (gq0 + ch) * 2048, lane, accA, gq0 + ch < 18 ? QSCALE : 1.f);
    STAMP(7);
    const bool c1q = c < 6 || ch == 0;
    p1_top3(X);
    if (c1q) {
        p1_top(X, 1, 0); P1_QS(0, true, X.trbase + X.rs);
        p1_top(X, 1, 1); P1_QS(1, true, X.trbase + X.rs);
        p1_top(X, 1, 2); P1_QS(2, true, X.trbase + X.rs);
        const int g = c < 6 ? gq1 + ch : gs;
        p1_qkv_store(qout + g * 2048, lane, accA, g < 18 ? QSCALE : 1.f);
    }
    STAMP(8);
    const int i1 = c1q ? 2 : 1;
    const int iend = (nch == 7 && ch == 1) ? 6 : nch;
#define P1_G(i) ((i) == 1 ? wsingle : p1_w1g(X, i) + ((i) < 6 ? ch : 0))
    bf16x8 wf[6];
#pragma unroll
    for (int k = 0; k < 6; ++k) wf[k] = xf[k];
#define P1_ST1
#define P1_ST2
    for (int i = i1; i < iend; i += 2) {
        p1_w1_chunk(X, i, P1_G(i), P1_G(i - 1), i > i1, xf, wf, accA, accB); P1_ST1
        if (i + 1 < iend) p1_w1_chunk(X, i + 1, P1_G(i + 1), P1_G(i), true, xf, wf, accB, accA); P1_ST2
    }
    int ie = iend; asm volatile("" : "+s"(ie));
    if (((ie - i1) & 1) != 0) accB = accA;
    if (ie < nch) P1_WAITBAR(0);
    { Epi E; const u32 vo = X.l16 + (u32)P1_G(ie - 1) * 2048;
#pragma unroll
      for (int s = 0; s < 4; ++s) accB = MFMA32(wf[(s + 2) % 6], xf[32 + s], accB);
#pragma unroll
      for (int s = 0; s < 32; ++s) p1_gelu_slot(s, X, accB, E, vo); }
    if (ie < nch) { P1_WAITBAR(0); P1_WAITBAR(0); }
    asm volatile("s_waitcnt vmcnt(0)" ::: "memory");
    STAMP(15);
    STAMP_OUT(stamps, 16);
}
constexpr int AT_K = 0, AT_V = 12 * 4096, AT_Q = 24 * 4096, AT_LDS = AT_Q + 4 * 4096;
__device__ __forceinline__ int qk_off(int c) { const int cc = c & 31; return (c >> 5) * 2048 + (2 * (cc >> 4) + ((cc >> 2) & 1)) * 512 + (((cc >> 3) & 1) * 4 + (cc & 3)) * 2; }
__device__ __forceinline__ float xmax16(float v) { auto r = __builtin_amdgcn_permlane16_swap(__float_as_uint(v), __float_as_uint(v), false, false); return fmaxf(__uint_as_float(r[0]), __uint_as_float(r[1])); }
__device__ __forceinline__ float xmax32(float v) { auto r = __builtin_amdgcn_permlane32_swap(__float_as_uint(v), __float_as_uint(v), false, false); return fmaxf(__uint_as_float(r[0]), __uint_as_float(r[1])); }
__device__ __forceinline__ float max3f(float a, float b, float c) { return __builtin_fmaxf(__builtin_fmaxf(a, b), c); }

template <bool EARLY>
__device__ __forceinline__ void attn_unit(lcptr L, int c0  , int cones, int w, int lane, int Q0, float gate, char* ob, int head) {
    const int i16 = lane & 15, g = lane >> 4;
    const int lb0 = w >> 1;
    const int o0 = qk_off(c0 + 2 * g), o1 = qk_off(c0 + 2 * g + 8), o2 = qk_off(c0 + 16);
    bf16x8 qf;
    { lcptr qp = L + AT_Q + (w >> 1) * 4096 + ((w & 1) * 16 + i16) * 16;
      const u32 a = *(const LAS u32*)(qp + o0), b = *(const LAS u32*)(qp + o1);
      u32 c = *(const LAS u32*)(qp + o2); if (g != 0) c = 0u;
      qf = __builtin_bit_cast(bf16x8, (u32x4){a, b, c, 0u}); }
    lcptr kb0 = L + AT_K + lb0 * 4096 + i16 * 16;
    lcptr k0 = kb0 + o0, k1 = kb0 + o1, k2 = kb0 + o2;
    PIN(k0); PIN(k1); PIN(k2);
    f32x4 s[18];
#pragma unroll
    for (int t = 0; t < 18; ++t) {
        const int o = (t >> 1) * 4096 + (t & 1) * 256;
        const u32x4 kw = {*(const LAS u32*)(k0 + o), *(const LAS u32*)(k1 + o), *(const LAS u32*)(k2 + o), 0u};
        s[t] = MFMA16(__builtin_bit_cast(bf16x8, kw), qf, ((f32x4){0.f, 0.f, 0.f, 0.f}));
    }
    const int q = Q0 + 16 * w + i16, kb = Q0 - 256 + lb0 * 32 + 4 * g;
    const int dq = q - kb;
#pragma unroll
    for (int t = 0; t < 18; ++t) {
        if (EARLY) {
#pragma unroll
            for (int r = 0; r < 4; ++r) { const int d = dq - 16 * t - r; const bool ok = (u32)d <= 255u && (kb + 16 * t + r) >= 0; s[t][r] = ok ? s[t][r] : -INFINITY; }
        } else if (t < 2) {
#pragma unroll
            for (int r = 0; r < 4; ++r) s[t][r] = (dq - 16 * t - r <= 255) ? s[t][r] : -INFINITY;
        } else if (t >= 16) {
#pragma unroll
            for (int r = 0; r < 4; ++r) s[t][r] = (dq - 16 * t - r >= 0) ? s[t][r] : -INFINITY;
        }
    }
    float m0 = max3f(s[0][0], s[0][1], s[0][2]), m1 = max3f(s[0][3], s[1][0], s[1][1]);
    m0 = max3f(m0, s[1][2], s[1][3]);
#pragma unroll
    for (int t = 2; t < 18; t += 2) { m0 = max3f(m0, s[t][0], s[t][1]); m1 = max3f(m1, s[t][2], s[t][3]); m0 = max3f(m0, s[t + 1][0], s[t + 1][1]); m1 = max3f(m1, s[t + 1][2], s[t + 1][3]); }
    const float m = xmax32(xmax16(fmaxf(m0, m1)));
    const int ca = c0 + i16, cb = (i16 < 2) ? c0 + 16 + i16 : (i16 == 2 ? cones : c0);
    lcptr va = L + AT_V + lb0 * 4096 + (ca >> 5) * 2048 + (g & 1) * 512 + (ca & 31) * 16 + (g >> 1) * 8;
    lcptr vb = L + AT_V + lb0 * 4096 + (cb >> 5) * 2048 + (g & 1) * 512 + (cb & 31) * 16 + (g >> 1) * 8;
    PIN(va); PIN(vb);
    f32x4 oa = {0.f, 0.f, 0.f, 0.f}, obb = {0.f, 0.f, 0.f, 0.f};
#pragma unroll
    for (int b = 0; b < 9; ++b) {
        const u32x2 a0 = *(const LAS u32x2*)(va + b * 4096), a1 = *(const LAS u32x2*)(va + b * 4096 + 1024);
        const u32x2 b0 = *(const LAS u32x2*)(vb + b * 4096), b1 = *(const LAS u32x2*)(vb + b * 4096 + 1024);
        u32x4 pw;
        pw[0] = cvtpk_c(__builtin_amdgcn_exp2f(s[2 * b][0] - m), __builtin_amdgcn_exp2f(s[2 * b][1] - m));
        pw[1] = cvtpk_c(__builtin_amdgcn_exp2f(s[2 * b][2] - m), __builtin_amdgcn_exp2f(s[2 * b][3] - m));
        pw[2] = cvtpk_c(__builtin_amdgcn_exp2f(s[2 * b + 1][0] - m), __builtin_amdgcn_exp2f(s[2 * b + 1][1] - m));
        pw[3] = cvtpk_c(__builtin_amdgcn_exp2f(s[2 * b + 1][2] - m), __builtin_amdgcn_exp2f(s[2 * b + 1][3] - m));
        const bf16x8 pf = __builtin_bit_cast(bf16x8, pw);
        oa = MFMA16(__builtin_bit_cast(bf16x8, ((u32x4){a0[0], a0[1], a1[0], a1[1]})), pf, oa);
        obb = MFMA16(__builtin_bit_cast(bf16x8, ((u32x4){b0[0], b0[1], b1[0], b1[1]})), pf, obb);
    }
    const float l = __shfl(obb[2], i16);
    const float sc = gate * __builtin_amdgcn_rcpf(l);
    char* base = ob + (size_t)(q >> 5) * 18 * 2048 + (q & 31) * 16;
    const int col = 18 * head + 4 * g;
    auto off = [](int c) { const int cc = c & 31; return (c >> 5) * 2048 + (2 * (cc >> 4) + ((cc >> 2) & 1)) * 512 + (((cc >> 3) & 1) * 4 + (cc & 3)) * 2; };
    *(u32*)(base + off(col)) = cvtpk(oa[0] * sc, oa[1] * sc);
    *(u32*)(base + off(col + 2)) = cvtpk(oa[2] * sc, oa[3] * sc);
    if (g == 0) *(u32*)(base + off(18 * head + 16)) = cvtpk(obb[0] * sc, obb[1] * sc);
}

__global__ __launch_bounds__(1024, 4) void attn_kernel(const u16* __restrict__ qkvb, const float* __restrict__ st, u16* __restrict__ ob, unsigned long long* stamps) {
    STAMP_DECL STAMP(0);
    extern __shared__ __attribute__((aligned(16))) char smem[];
    const int tid = threadIdx.x, lane = tid & 63, wid = RFL(tid >> 6);
    const int qb = blockIdx.x >> 4, hp = blockIdx.x & 15;
    const int Q0 = qb * 128, cg0 = (36 * hp) >> 5, c0 = 36 * hp - 32 * cg0;
    const u32 lds0 = (u32)(uintptr_t)smem;
#pragma unroll
    for (int i = 0; i < 4; ++i) {
        const int p = wid + 16 * i;
        if (p < 56) {
            const int u = p >> 1, gsel = p & 1;
            const int sec = u < 12 ? 1 : (u < 24 ? 2 : 0), ub = u < 12 ? u : (u < 24 ? u - 12 : u - 24);
            int rb = qb * 4 + ub - (sec ? 8 : 0); if (rb < 0) rb = 0;
            glds16s_off1k((const char*)qkvb + (size_t)(rb * 54 + sec * 18 + cg0 + gsel) * 2048, lane * 16, lds0 + u * 4096 + gsel * 2048);
        }
    }
    const int w = wid & 7, hsel = wid >> 3;
    const int q = Q0 + 16 * w + (lane & 15);
    const float gate = st[q * 8 + 7];
    STAMP(1);
    asm volatile("s_waitcnt vmcnt(0)\n\ts_barrier" ::: "memory");
    STAMP(2);
    const int cones = (c0 + 36) & 63;
    if (tid < 48) { const int kb = tid >> 2, pc = tid & 3; *(LAS u32x4*)((lptr)(size_t)lds0 + AT_V + kb * 4096 + (cones >> 5) * 2048 + pc * 512 + (cones & 31) * 16) = (u32x4){0x3f803f80u, 0x3f803f80u, 0x3f803f80u, 0x3f803f80u}; }
    asm volatile("s_waitcnt lgkmcnt(0)\n\ts_barrier" ::: "memory");
    STAMP(3);
    if (qb < 2) attn_unit<true>((lcptr)(size_t)lds0, c0 + 18 * hsel, cones, w, lane, Q0, gate, (char*)ob, 2 * hp + hsel);
    else attn_unit<false>((lcptr)(size_t)lds0, c0 + 18 * hsel, cones, w, lane, Q0, gate, (char*)ob, 2 * hp + hsel);
    STAMP(4);
    STAMP_OUT(stamps, 5);
}
constexpr int P3_A = 32768, P3_B = 64 * 288, P3_SLOT = P3_A + P3_B, P3_LDS = 3 * P3_SLOT;
struct P3Ctx {
    const char* pA; const char* pB;
    const char* oA; const char* oB;
    u32 aStride;
    int tl, nst; u32 sl;
    u32 dA, dB;
    u32 vA, vB0, vB1, vB2;
    lcptr ab, bb;
};
__device__ __forceinline__ void glds_c2(int tl, int nst, const void* sbase, u32 voff, u32 lds_base) {
    u32 sv;
    asm volatile("s_cmp_ge_i32 %4, %5\n\ts_cbranch_scc1 1f\n\ts_mov_b32 %0, m0\n\ts_mov_b32 m0, %3\n\ts_nop 0\n\tglobal_load_lds_dwordx4 %1, %2\n\tglobal_load_lds_dwordx4 %1, %2 offset:1024\n\ts_mov_b32 m0, %0\n1:"
                 : "=&s"(sv) : "v"(voff), "s"(sbase), "s"(lds_base), "s"(tl), "s"(nst) : "memory", "scc");
}
__device__ __forceinline__ void glds_c1(int tl, int nst, const void* sbase, u32 voff, u32 lds_base) {
    u32 sv;
    asm volatile("s_cmp_ge_i32 %4, %5\n\ts_cbranch_scc1 1f\n\ts_mov_b32 %0, m0\n\ts_mov_b32 m0, %3\n\ts_nop 0\n\tglobal_load_lds_dwordx4 %1, %2\n\ts_mov_b32 m0, %0\n1:"
                 : "=&s"(sv) : "v"(voff), "s"(sbase), "s"(lds_base), "s"(tl), "s"(nst) : "memory", "scc");
}
__device__ __forceinline__ void glds_cq(int tl, int nst, const void* sbase, u32 voff, u32 lds_base) {
    u32 sv;
    asm volatile("s_cmp_ge_i32 %4, %5\n\ts_cbranch_scc1 1f\n\ts_mov_b32 %0, m0\n\ts_mov_b32 m0, %3\n\ts_mov_b64 exec, 0xffff\n\tglobal_load_lds_dwordx4 %1, %2\n\ts_mov_b64 exec, -1\n\ts_mov_b32 m0, %0\n1:"
                 : "=&s"(sv) : "v"(voff), "s"(sbase), "s"(lds_base), "s"(tl), "s"(nst) : "memory", "scc");
}
__device__ __forceinline__ void p3_load_part(P3Ctx& X, int k) {
    if (k == 0) glds_c2(X.tl, X.nst, X.pA, X.vA, X.dA + X.sl);
    if (k == 1) glds_c2(X.tl, X.nst, X.pA + 2048, X.vA, X.dA + X.sl + 2048);
    if (k == 2) glds_c1(X.tl, X.nst, X.pB, X.vB0, X.dB + X.sl);
    if (k == 3) glds_c1(X.tl, X.nst, X.pB, X.vB1, X.dB + X.sl + 1024);
    if (k == 4) { glds_cq(X.tl, X.nst, X.pB, X.vB2, X.dB + X.sl + 2048);
        X.tl += 1; const bool sw = X.tl == 9; X.pA = sw ? X.oA : X.pA + 4096; X.pB = sw ? X.oB : X.pB + 64 * 1152; X.sl = X.sl == 2 * P3_SLOT ? 0 : X.sl + P3_SLOT; }
}
struct P3Frag { bf16x8 h[4]; bf16x8 w[5]; };
__device__ __forceinline__ void p3_waitbar(int rem) {
    asm volatile("s_cmp_lt_i32 %0, 1\n\ts_cbranch_scc1 2f\n1:\ts_waitcnt vmcnt(7) lgkmcnt(0)\n\ts_barrier\n\t.subsection 1\n2:\ts_waitcnt vmcnt(0)\n\ts_branch 1b\n\t.subsection 0" :: "s"(rem) : "memory", "scc");
}
template <int NT, int HF, bool RD, bool MM>
__device__ __forceinline__ void p3_half(P3Ctx& X, int t, u32 so, P3Frag& R, const P3Frag& P, f32x4 (&acc)[4][5]) {
    lcptr a = X.ab + so + HF * 2048, b = X.bb + so + HF * (32 * 288);
    if (RD) { if (HF == 0) p3_waitbar(X.nst - 1 - t); PIN(a); PIN(b); }
    s16x4 lo;
    SBAR();
#pragma unroll
    for (int i = 0; i < 4 * NT; ++i) {
        if (MM) acc[i & 3][i >> 2] = MFMA16(P.w[i >> 2], P.h[i & 3], acc[i & 3][i >> 2]);
        if (RD) {
            if (i < 4) R.h[i] = *(const LAS bf16x8*)(a + (i >> 1) * 4096 + (i & 1) * 256);
            else if (i < 4 + 2 * NT) { const int j = i - 4; if (j & 1) R.w[j >> 1] = cat8(lo, trd(b + (j >> 1) * 32 + 8 * 288)); else lo = trd(b + (j >> 1) * 32); }
            if (HF == 0) { if (i == (NT == 5 ? 2 : 6)) p3_load_part(X, 0); if (i == (NT == 5 ? 10 : 13)) p3_load_part(X, 1); }
            else { if (i == (NT == 5 ? 1 : 4)) p3_load_part(X, 2); if (i == (NT == 5 ? 8 : 10)) p3_load_part(X, 3); if (i == (NT == 5 ? 15 : 14)) p3_load_part(X, 4); }
        }
        SBAR();
    }
}
template <int NT>
__device__ __forceinline__ void p3_loop(P3Ctx& X, int nhalf, f32x4 (&acc)[4][5]) {
    P3Frag F0, F1;
    u32 so = 0;
    p3_half<NT, 0, true, false>(X, 0, so, F0, F1, acc);
    p3_half<NT, 1, true, true>(X, 0, so, F1, F0, acc);
    int t = 1;
    for (; 2 * t + 1 < nhalf; ++t) {
        so = so == 2 * P3_SLOT ? 0 : so + P3_SLOT;
        p3_half<NT, 0, true, true>(X, t, so, F0, F1, acc);
        p3_half<NT, 1, true, true>(X, t, so, F1, F0, acc);
    }
    if (2 * t < nhalf) {
        so = so == 2 * P3_SLOT ? 0 : so + P3_SLOT;
        p3_half<NT, 0, true, true>(X, t, so, F0, F1, acc);
        p3_half<NT, 1, false, true>(X, t, so, F1, F0, acc);
    } else p3_half<NT, 0, false, true>(X, t, so, F0, F1, acc);
}
__global__ __launch_bounds__(512, 2) void p3_kernel(const u16* __restrict__ hb, const u16* __restrict__ ob, const u16* __restrict__ w2, const u16* __restrict__ wo,
                                                     u16* __restrict__ part, unsigned long long* stamps) {
    STAMP_DECL STAMP(0);
    extern __shared__ __attribute__((aligned(16))) char smem[];
    const int tid = threadIdx.x, lane = tid & 63, wid = RFL(tid >> 6);
    const int sp = blockIdx.x & 7, idx = blockIdx.x >> 3;
    const int mt = idx & 7, nt = idx >> 3;
    const u32 lds0 = (u32)(uintptr_t)smem;
    const int mq = wid >> 1, nh = (wid & 1) ^ (wid >> 2);
    const int i16 = lane & 15, kq = lane >> 4;
    P3Ctx X;
    const int no = (sp & 3) == 0 ? 3 : 2, o0 = (sp >> 2) * 9 + ((sp & 3) == 0 ? 0 : 1 + 2 * (sp & 3));
    const int nhalf = 18 + no; X.nst = (nhalf + 1) >> 1; X.tl = 0; X.sl = 0;
    X.pA = (const char*)hb + (size_t)((mt * 8 + wid) * 144 + 18 * sp) * 2048;
    X.oA = (const char*)ob + (size_t)((mt * 8 + wid) * 18 + o0) * 2048;
    X.pB = (const char*)w2 + (size_t)(576 * sp) * 1152 + 288 * nt;
    X.oB = (const char*)wo + (size_t)(32 * o0) * 1152 + 288 * nt;
    X.dA = lds0 + wid * 4096; X.dB = lds0 + P3_A + wid * 144 * 16;
    X.vA = lane * 16;
    { const int e0 = wid * 144 + lane, e1 = e0 + 64, e2 = e0 + 128;
      X.vB0 = (e0 / 18) * 1152 + (e0 % 18) * 16; X.vB1 = (e1 / 18) * 1152 + (e1 % 18) * 16; X.vB2 = (e2 / 18) * 1152 + (e2 % 18) * 16; }
    X.ab = (lcptr)(size_t)lds0 + (mq * 2) * 4096 + kq * 512 + i16 * 16;
    X.bb = (lcptr)(size_t)lds0 + P3_A + (16 * (kq >> 1) + 4 * (kq & 1) + (i16 >> 2)) * 288 + (nh ? 5 * 32 : 0) + (i16 & 3) * 8;
#pragma unroll
    for (int t = 0; t < 2; ++t)
#pragma unroll
        for (int k = 0; k < 5; ++k) p3_load_part(X, k);
    f32x4 acc[4][5];
#pragma unroll
    for (int m = 0; m < 4; ++m)
#pragma unroll
        for (int n = 0; n < 5; ++n) acc[m][n] = (f32x4){0.f, 0.f, 0.f, 0.f};
    STAMP(1);
    if (nh == 0) p3_loop<5>(X, nhalf, acc); else p3_loop<4>(X, nhalf, acc);
    STAMP(2);
    u16* pb = part + ((size_t)((sp * 8 + mt) * 4 + nt) * 144) * 256;
    const int NTN = nh ? 4 : 5;
#pragma unroll
    for (int m = 0; m < 4; ++m)
#pragma unroll
        for (int n = 0; n < 5; ++n)
            if (n < NTN) *(u32x2*)(pb + ((mq * 4 + m) * 9 + (nh ? 5 : 0) + n) * 256 + lane * 4) = (u32x2){cvtpk_c(acc[m][n][0], acc[m][n][1]), cvtpk_c(acc[m][n][2], acc[m][n][3])};
    STAMP(3);
    STAMP_OUT(stamps, 4);
}
__global__ __launch_bounds__(256) void k4_reduce(const u16* __restrict__ part, const float* __restrict__ st, const u16* __restrict__ b2, u16* __restrict__ out) {
    const int gid = blockIdx.x * 256 + threadIdx.x;
    const int l2 = gid & 31, f = gid >> 5;
    const int n = f % 9, mtile = (f / 9) & 15, nt = (f / 144) & 3, mt = f / 576;
    float v[8] = {0.f, 0.f, 0.f, 0.f, 0.f, 0.f, 0.f, 0.f};
    u32x4 p[8];
#pragma unroll
    for (int s = 0; s < 8; ++s) p[s] = *(const u32x4*)(part + (size_t)s * (8 * 4 * 144 * 256) + (size_t)gid * 8);
#pragma unroll
    for (int s = 0; s < 8; ++s)
#pragma unroll
        for (int j = 0; j < 4; ++j) { v[2 * j] += bflo(p[s][j]); v[2 * j + 1] += bfhi(p[s][j]); }
#pragma unroll
    for (int e = 0; e < 2; ++e) {
        const int lane = 2 * l2 + e;
        const int row = mt * 256 + mtile * 16 + (lane & 15), col = nt * 144 + n * 16 + (lane >> 4) * 4;
        const float og = 1.f - st[row * 8 + 7];
        const u32x2 bb = *(const u32x2*)(b2 + col);
        u32x2 o; o[0] = cvtpk(v[4 * e] + og * bflo(bb[0]), v[4 * e + 1] + og * bfhi(bb[0])); o[1] = cvtpk(v[4 * e + 2] + og * bflo(bb[1]), v[4 * e + 3] + og * bfhi(bb[1]));
        *(u32x2*)(out + (size_t)row * D_ + col) = o;
    }
}
extern "C" void kernel_launch(void* const* d_in, const int* in_sizes, int n_in, void* d_out, int out_size, void* d_ws, size_t ws_size, hipStream_t stream) {
    const u16* x = (const u16*)d_in[0]; const u16* wqkv = (const u16*)d_in[1]; const u16* wo = (const u16*)d_in[2];
    const u16* w1 = (const u16*)d_in[4]; const u16* b1 = (const u16*)d_in[5]; const u16* w2 = (const u16*)d_in[6]; const u16* b2 = (const u16*)d_in[7]; const u16* gw = (const u16*)d_in[8];
    char* ws = (char*)d_ws;
    u32x4* rowrec = (u32x4*)ws;
    u32x4* wext = (u32x4*)(ws + 65536);
    char* big = ws + 65536 + 73728 + 8192;
    u16* qkvb = (u16*)big;
    u16* hb = (u16*)(big + 7077888);
    u16* ob = (u16*)(big + 7077888 + 18874368);
    u16* part = (u16*)(big + 7077888 + 18874368 + 2359296);
    (void)hipFuncSetAttribute((const void*)p1_kernel, hipFuncAttributeMaxDynamicSharedMemorySize, P1_LDS);
    (void)hipFuncSetAttribute((const void*)attn_kernel, hipFuncAttributeMaxDynamicSharedMemorySize, AT_LDS);
    (void)hipFuncSetAttribute((const void*)p3_kernel, hipFuncAttributeMaxDynamicSharedMemorySize, P3_LDS);
    k0_stats<<<256 + 36, 512, 0, stream>>>(x, gw, w1, b1, rowrec, wext);
    p1_kernel<<<256, 512, P1_LDS, stream>>>(x, wqkv, w1, rowrec, wext, qkvb, hb, (unsigned long long*)(ws + (200u << 20)));
    attn_kernel<<<256, 1024, AT_LDS, stream>>>(qkvb, (const float*)rowrec, ob, (unsigned long long*)(ws + (202u << 20)));
    p3_kernel<<<256, 512, P3_LDS, stream>>>(hb, ob, w2, wo, part, (unsigned long long*)(ws + (201u << 20)));
    k4_reduce<<<576, 256, 0, stream>>>(part, (const float*)rowrec, b2, (u16*)d_out);
}
```

```cpp
#include <hip/hip_runtime.h>
#include <hip/hip_bf16.h>
typedef unsigned short u16;
typedef unsigned int u32;
#define LAS __attribute__((address_space(3)))
typedef short bf16x8 __attribute__((ext_vector_type(8)));
typedef short s16x4 __attribute__((ext_vector_type(4)));
typedef float f32x16 __attribute__((ext_vector_type(16)));
typedef float f32x4 __attribute__((ext_vector_type(4)));
typedef u32 u32x4 __attribute__((ext_vector_type(4)));
typedef u32 u32x2 __attribute__((ext_vector_type(2)));
typedef LAS char* lptr;
typedef LAS const char* lcptr;

constexpr int S_ = 2048, D_ = 576, H_ = 32, DH_ = 18, F_ = 4608, NQKV_ = 1728;

__device__ __forceinline__ float bf2f(u16 b) { return __uint_as_float(((u32)b) << 16); }
__device__ __forceinline__ float bflo(u32 w) { return __uint_as_float(w << 16); }
__device__ __forceinline__ float bfhi(u32 w) { return __uint_as_float(w & 0xffff0000u); }
typedef __bf16 bf16x2_t __attribute__((ext_vector_type(2)));
typedef float f32x2 __attribute__((ext_vector_type(2)));
__device__ __forceinline__ u32 cvtpk_c(float lo, float hi) { return __builtin_bit_cast(u32, __builtin_convertvector((f32x2){lo, hi}, bf16x2_t)); }
__device__ __forceinline__ u32 cvtpk(float lo, float hi) { u32 r; asm("v_cvt_pk_bf16_f32 %0, %1, %2" : "=v"(r) : "v"(lo), "v"(hi)); return r; }
__device__ __forceinline__ u16 f2bf(float f) { return (u16)(cvtpk(f, f) & 0xffffu); }
__device__ __forceinline__ void glds16(const void* g, u32 lds_base) {
    u32 sv;
    asm volatile("s_mov_b32 %0, m0\n\ts_mov_b32 m0, %2\n\ts_nop 0\n\tglobal_load_lds_dwordx4 %1, off\n\ts_mov_b32 m0, %0" : "=&s"(sv) : "v"(g), "s"(lds_base) : "memory");
}
__device__ __forceinline__ void glds16s(const void* sbase, u32 voff, u32 lds_base) {
    u32 sv;
    asm volatile("s_mov_b32 %0, m0\n\ts_mov_b32 m0, %3\n\ts_nop 0\n\tglobal_load_lds_dwordx4 %1, %2\n\ts_mov_b32 m0, %0" : "=&s"(sv) : "v"(voff), "s"(sbase), "s"(lds_base) : "memory");
}
__device__ __forceinline__ void glds16s_off1k(const void* sbase, u32 voff, u32 lds_base) {
    u32 sv;
    asm volatile("s_mov_b32 %0, m0\n\ts_mov_b32 m0, %3\n\ts_nop 0\n\tglobal_load_lds_dwordx4 %1, %2\n\tglobal_load_lds_dwordx4 %1, %2 offset:1024\n\ts_mov_b32 m0, %0" : "=&s"(sv) : "v"(voff), "s"(sbase), "s"(lds_base) : "memory");
}
__device__ __forceinline__ void glds16s_off1k_sc1(const void* sbase, u32 voff, u32 lds_base) {
    u32 sv;
    asm volatile("s_mov_b32 %0, m0\n\ts_mov_b32 m0, %3\n\ts_nop 0\n\tglobal_load_lds_dwordx4 %1, %2 sc1\n\tglobal_load_lds_dwordx4 %1, %2 offset:1024 sc1\n\ts_mov_b32 m0, %0" : "=&s"(sv) : "v"(voff), "s"(sbase), "s"(lds_base) : "memory");
}
__device__ __forceinline__ s16x4 trd(lcptr p) {
    typedef short v4i16_t __attribute__((ext_vector_type(4)));
    return __builtin_bit_cast(s16x4, __builtin_amdgcn_ds_read_tr16_b64_v4i16((LAS v4i16_t*)p));
}
__device__ __forceinline__ bf16x8 cat8(s16x4 a, s16x4 b) { return (bf16x8){a[0], a[1], a[2], a[3], b[0], b[1], b[2], b[3]}; }
#define MFMA32(a, b, c) __builtin_amdgcn_mfma_f32_32x32x16_bf16(a, b, c, 0, 0, 0)
#define MFMA16(a, b, c) __builtin_amdgcn_mfma_f32_16x16x32_bf16(a, b, c, 0, 0, 0)
#define SBAR() __builtin_amdgcn_sched_barrier(0)
#define PIN(x) asm volatile("" : "+v"(x))
#define RFL(x) __builtin_amdgcn_readfirstlane(x)

#define STAMP_DECL
#define STAMP(i)
#define STAMP_OUT(buf, n)

typedef unsigned long long u64;
typedef __attribute__((address_space(1))) u64 gu64;
constexpr int FLAG_STRIDE = 16;
constexpr int F_STAT = 0, F_QKV = 1, F_H = 2, F_O = 3, F_PART = 4, F_KINDS = 5;
constexpr size_t CTRL_BYTES = (size_t)(F_KINDS * 256 + 1) * FLAG_STRIDE * 8;
__device__ __forceinline__ gu64* flag_ptr(u64* ctrl, int kind, int wg) { return (gu64*)(ctrl + (size_t)(kind * 256 + wg) * FLAG_STRIDE); }
__device__ __forceinline__ gu64* epoch_ptr(u64* ctrl) { return (gu64*)(ctrl + (size_t)(F_KINDS * 256) * FLAG_STRIDE); }
__device__ __forceinline__ u64 ld_agent(gu64* p) { return __hip_atomic_load(p, __ATOMIC_RELAXED, __HIP_MEMORY_SCOPE_AGENT); }
__device__ __forceinline__ void st_agent(gu64* p, u64 v) { __hip_atomic_store(p, v, __ATOMIC_RELAXED, __HIP_MEMORY_SCOPE_AGENT); }
__device__ __forceinline__ void poll_flags(gu64* mine, bool use, u64 ep) {
    const unsigned long long t0 = __builtin_amdgcn_s_memrealtime();
    for (;;) {
        const u64 v = use ? ld_agent(mine) : ep;
        if (__all(v == ep)) break;
        if (__builtin_amdgcn_s_memrealtime() - t0 > 5000000ull) break;
        __builtin_amdgcn_s_sleep(1);
    }
}
__device__ __forceinline__ void poll_issue(u64& v, gu64* p) { asm volatile("global_load_dwordx2 %0, %1, off sc1" : "=v"(v) : "v"(p) : "memory"); }
__device__ __forceinline__ void poll_check(u64& v, gu64* p, bool use, u64 ep) { asm volatile("" : "+v"(v)); if (!__all(!use || v == ep)) poll_flags(p, use, ep); }
__device__ __forceinline__ void st16_sc1(u32x4 v, __amdgpu_buffer_rsrc_t rs, u32 voff) { __builtin_amdgcn_raw_buffer_store_b128(v, rs, voff, 0, 16); }
__device__ __forceinline__ u32x4 ld16_sc1(__amdgpu_buffer_rsrc_t rs, u32 voff) { return __builtin_amdgcn_raw_buffer_load_b128(rs, voff, 0, 16); }
__device__ __forceinline__ __amdgpu_buffer_rsrc_t mkrsrc(const void* p, u32 bytes) { return __builtin_amdgcn_make_buffer_rsrc((void*)p, 0, bytes, 0x00020000); }
#define INV_AGENT() asm volatile("buffer_inv sc1" ::: "memory")
#define WAIT_VM0() asm volatile("s_waitcnt vmcnt(0)" ::: "memory")
#define BARRIER() asm volatile("s_waitcnt lgkmcnt(0)\n\ts_barrier" ::: "memory")
struct Args {
    const u16* x; const u16* wqkv; const u16* wo; const u16* w1; const u16* b1; const u16* w2; const u16* b2; const u16* gw;
    u64* ctrl; u32x4* rowrec; u32x4* wext; char* qkvb; char* hb; char* ob; char* part; u16* out; unsigned long long* stamps;
};
constexpr int P1_SLOT = 24576, P1_RING = 6 * P1_SLOT, P1_WEXT = P1_RING, P1_ROW = P1_WEXT + 288 * 16 + 64  , P1_CS = P1_ROW + 128 * 32  , P1_LDS = P1_CS + 1024;
constexpr float GC0 = -2.f * 0.7978845608028654f * 1.4426950408889634f, GC1 = GC0 * 0.044715f;
__device__ __forceinline__ u32 bfbits(float f) { return cvtpk(f, 0.f) & 0xffffu; }
constexpr float QSCALE = 0.23570226039551584f * 1.4426950408889634f;
#define P1_WAITBAR(N) asm volatile("s_waitcnt vmcnt(" #N ") lgkmcnt(0)\n\ts_barrier" ::: "memory")

struct P1Ctx {
    const char* w1; __amdgpu_buffer_rsrc_t hrs;
    int c, nch, g1first; u32 l16;
    const char* lb; const char* la;
    u32 ld, ldst0;
    u32 voffW[3];
    lcptr trbase, wexb; u32 wexs; u32 rs;
    lcptr xeb, rcb;
};
constexpr u32 P1_PART = 192u * F_ * 2u;
__device__ __forceinline__ int p1_w1g(const P1Ctx& X, int i) { return i < 6 ? X.g1first + 2 * (i - 2) : ((i == 6 && X.nch == 7) ? ((X.c & 1) ? 9 * X.c : 9 * X.c + 8) : X.g1first); }
__device__ __forceinline__ void p1_piece_a(const P1Ctx& X, int k) { asm volatile("s_add_u32 m0, %0, %1" :: "s"(X.ld), "n"(k * 8192) : "memory", "scc"); }
__device__ __forceinline__ void p1_piece_b(const P1Ctx& X, int k) {
    asm volatile("global_load_lds_dwordx4 %0, %1" :: "v"(X.voffW[k]), "s"(X.la) : "memory");
}
__device__ __forceinline__ void p1_piece(const P1Ctx& X, int k) { p1_piece_a(X, k); asm volatile("s_nop 0"); p1_piece_b(X, k); }
__device__ __forceinline__ void p1_top(P1Ctx& X, int i, int j) {
    if (j == 0) asm volatile("s_cmp_le_u32 %0, 2\n\ts_cbranch_scc1 3f\n\ts_waitcnt vmcnt(12) lgkmcnt(0)\n\ts_barrier\n3:" :: "s"(i) : "memory", "scc");
    else asm volatile("s_waitcnt vmcnt(12) lgkmcnt(0)\n\ts_barrier" ::: "memory");
    X.ld = X.ldst0 + X.rs;
    X.rs = X.rs == 5 * P1_SLOT ? 0 : X.rs + P1_SLOT;
    if (j == 0) X.la = X.lb + 2 * P1_PART;
    if (j == 1) { X.lb = X.w1 + p1_w1g(X, i + 2) * 64; X.la = X.lb; }
    if (j == 2) X.la = X.lb + P1_PART;
}
__device__ __forceinline__ void p1_top3(P1Ctx& X) {
    X.lb = X.w1 + p1_w1g(X, 2) * 64;
    X.la = X.lb; X.ld = X.ldst0; p1_piece(X, 0); p1_piece(X, 1); p1_piece(X, 2);
    X.la = X.lb + P1_PART; X.ld = X.ldst0 + P1_SLOT; p1_piece(X, 0); p1_piece(X, 1); p1_piece(X, 2);
    X.rs = 2 * P1_SLOT;
}
struct Epi { float a0, a1, u0, u1, c0, c1, sd; u32 pw[4]; };
__device__ __forceinline__ void p1_gelu_slot(int s, const P1Ctx& X, const f32x16& pa, Epi& E, u32 vo) {
    const int k = s >> 2, ph = s & 3;
    if (ph == 0) { E.a0 = pa[2 * k]; E.a1 = pa[2 * k + 1]; E.u0 = __builtin_fmaf(E.a0 * E.a0, E.c1, E.c0); E.u1 = __builtin_fmaf(E.a1 * E.a1, E.c1, E.c0); PIN(E.u0); PIN(E.u1); }
    if (ph == 1) { E.u0 = __builtin_amdgcn_exp2f(E.a0 * E.u0); E.u1 = __builtin_amdgcn_exp2f(E.a1 * E.u1); PIN(E.u0); PIN(E.u1); }
    if (ph == 2) { E.u0 = __builtin_amdgcn_rcpf(__builtin_fmaf(E.u0, E.sd, E.sd)); E.u1 = __builtin_amdgcn_rcpf(__builtin_fmaf(E.u1, E.sd, E.sd)); PIN(E.u0); PIN(E.u1); }
    if (ph == 3) { E.pw[k & 3] = cvtpk(E.a0 * E.u0, E.a1 * E.u1);
        if ((k & 3) == 3) __builtin_amdgcn_raw_buffer_store_b128((u32x4){E.pw[0], E.pw[1], E.pw[2], E.pw[3]}, X.hrs, vo + (k >> 2) * 1024, 0, 16); }
}
__device__ __forceinline__ void p1_consts(const P1Ctx& X, Epi& E) { const f32x4 k = *(const LAS f32x4*)X.rcb; E.c0 = k[0]; E.c1 = k[1]; E.sd = k[2]; }
__device__ __forceinline__ void p1_w1_chunk(P1Ctx& X, int i, int pg, bool st, const bf16x8 (&xf)[36], bf16x8 (&wf)[6], f32x16& acc, f32x16& pacc) {
    const u32 vo = st ? X.l16 + (u32)pg * 2048 : 0x40000000u;
    Epi E; bf16x8 we, xe;
    lcptr p = X.trbase;
    SBAR();
#pragma unroll
    for (int s = 0; s < 36; ++s) {
        if (s == 0 || s == 12 || s == 24) { p1_top(X, i, s / 12); p = X.trbase + X.rs; PIN(p); }
        if (s < 4) { pacc = MFMA32(wf[(s + 2) % 6], xf[32 + s], pacc); PIN(pacc); }
        if (s == 4) { pacc = MFMA32(we, xe, pacc); PIN(pacc); f32x16 z = {}; acc = MFMA32(wf[0], xf[0], z); PIN(acc); }
        if (s > 4) { acc = MFMA32(wf[(s - 4) % 6], xf[s - 4], acc); PIN(acc); }
        if (s % 4 == 2) p1_piece_a(X, (s % 12) >> 2);
        if (s == 1) { we = *(const LAS bf16x8*)(X.wexb + (st ? pg - 9 * X.c : 0) * X.wexs); xe = *(const LAS bf16x8*)X.xeb; }
        if (s == 3) p1_consts(X, E);
        if ((s & 1) == 0) {
            wf[(s + 1) % 6] = cat8(trd(p + ((s + 1) % 12) * 2048), trd(p + ((s + 1) % 12) * 2048 + 512));
            wf[s % 6] = cat8(trd(p + (s % 12) * 2048), trd(p + (s % 12) * 2048 + 512));
        }
        if (s % 4 == 2) p1_piece_b(X, (s % 12) >> 2);
        if (s >= 6) p1_gelu_slot(s - 6, X, pacc, E, vo);
        SBAR();
    }
    p1_gelu_slot(30, X, pacc, E, vo); p1_gelu_slot(31, X, pacc, E, vo);
}
template <bool SWAP, int J, bool LD>
__device__ __forceinline__ void p1_qkv_stage(P1Ctx& X, lcptr p, const bf16x8 (&xf)[36], f32x16& acc) {
    bf16x8 wf[4];
    PIN(p);
#pragma unroll
    for (int s = 0; s < 12 + 3; ++s) {
        if (s < 12) wf[s & 3] = cat8(trd(p + s * 2048), trd(p + s * 2048 + 512));
        if (s >= 3) { const int u = s - 3;
            if (J == 0 && u == 0) { f32x16 z = {}; acc = SWAP ? MFMA32(xf[0], wf[0], z) : MFMA32(wf[0], xf[0], z); }
            else acc = SWAP ? MFMA32(xf[J * 12 + u], wf[u & 3], acc) : MFMA32(wf[u & 3], xf[J * 12 + u], acc); }
        if (LD && (s == 4 || s == 8 || s == 12)) p1_piece(X, (s >> 2) - 1);
        SBAR();
    }
}
__device__ __forceinline__ void p1_qkv_store(__amdgpu_buffer_rsrc_t rs, u32 off, int lane, const f32x16& a, float sc) {
    u32 w[8];
#pragma unroll
    for (int k = 0; k < 8; ++k) w[k] = cvtpk(a[2 * k] * sc, a[2 * k + 1] * sc);
    st16_sc1((u32x4){w[0], w[1], w[2], w[3]}, rs, off + lane * 16);
    st16_sc1((u32x4){w[4], w[5], w[6], w[7]}, rs, off + 1024 + lane * 16);
}

__device__ __forceinline__ int p1_wg(int rb, int c) { return ((rb >> 3) | ((c >> 2) << 1)) + 8 * ((rb & 7) | ((c & 3) << 3)); }
__device__ __forceinline__ float dpp_ror4(float v) { return __builtin_bit_cast(float, __builtin_amdgcn_update_dpp(0, __builtin_bit_cast(int, v), 0x124, 0xf, 0xf, false)); }
__device__ __forceinline__ float dpp_ror8(float v) { return __builtin_bit_cast(float, __builtin_amdgcn_update_dpp(0, __builtin_bit_cast(int, v), 0x128, 0xf, 0xf, false)); }
__device__ __forceinline__ float xadd16(float v) { auto r = __builtin_amdgcn_permlane16_swap(__float_as_uint(v), __float_as_uint(v), false, false); return __uint_as_float(r[0]) + __uint_as_float(r[1]); }
__device__ __forceinline__ float xadd32(float v) { auto r = __builtin_amdgcn_permlane32_swap(__float_as_uint(v), __float_as_uint(v), false, false); return __uint_as_float(r[0]) + __uint_as_float(r[1]); }
template <int CTRL> __device__ __forceinline__ float dpp_mov(float v) { return __builtin_bit_cast(float, __builtin_amdgcn_update_dpp(0, __builtin_bit_cast(int, v), CTRL, 0xf, 0xf, false)); }
__device__ __forceinline__ float wave_sum(float v) {
    v += dpp_mov<0x121>(v); v += dpp_mov<0x122>(v); v += dpp_mov<0x124>(v); v += dpp_mov<0x128>(v);
    return xadd32(xadd16(v));
}
#define LD16A(dst, voff, sbase) asm volatile("global_load_dwordx4 %0, %1, %2" : "=v"(dst) : "v"(voff), "s"(sbase) : "memory")

__device__ __forceinline__ int p1_qkv_owner(int G) { return G < 24 ? G >> 2 : 6 + (G - 24) / 3; }
__device__ __forceinline__ gu64* attn_flag(const Args& A, int lane) {
    const int xcd = blockIdx.x & 7, idx = blockIdx.x >> 3, qb = idx >> 1, hp = xcd + 8 * (idx & 1), cg0 = (36 * hp) >> 5;
    const int gi = lane / 3, rr = lane - 3 * gi; int rbx = qb - rr; if (rbx < 0) rbx = 0;
    return flag_ptr(A.ctrl, F_QKV, p1_wg(rbx, p1_qkv_owner((lane < 18 ? (gi >> 1) * 18 + cg0 + (gi & 1) : 0))));
}
constexpr int AT_K = 0, AT_V = 12 * 4096, AT_Q = 24 * 4096, AT_LDS = AT_Q + 4 * 4096;
__device__ __forceinline__ void attn_stage(const Args& A, u32 lds0, int wid, int lane) {
    const int xcd = blockIdx.x & 7, idx = blockIdx.x >> 3, qb = idx >> 1, hp = xcd + 8 * (idx & 1), cg0 = (36 * hp) >> 5;
#pragma unroll
    for (int i = 0; i < 7; ++i) {
        const int p = wid + 8 * i;
        const int u = p >> 1, gsel = p & 1;
        const int sec = u < 12 ? 1 : (u < 24 ? 2 : 0), ub = u < 12 ? u : (u < 24 ? u - 12 : u - 24);
        int rb = qb * 4 + ub - (sec ? 8 : 0); if (rb < 0) rb = 0;
        glds16s_off1k_sc1(A.qkvb + (size_t)(rb * 54 + sec * 18 + cg0 + gsel) * 2048, lane * 16, lds0 + u * 4096 + gsel * 2048);
    }
}
__device__ __forceinline__ void p1_phase(const Args& A, char* smem, u64& ep) {
    STAMP_DECL STAMP(0);
    const int tid = threadIdx.x, lane = tid & 63, wid = RFL(tid >> 6);
    const int xcd = blockIdx.x & 7, idx = blockIdx.x >> 3;
    const int rb = (xcd & 1) * 8 + (idx & 7), c = (xcd >> 1) * 4 + (idx >> 3);
    const u32 lds0 = (u32)(uintptr_t)smem;
    const int r8 = lane >> 3, cp = lane & 7;
    const int csw = cp ^ (((r8 >> 1) & 1) << 2);
    int gq0, gq1, gs;
    if (c < 6) { gq0 = 4 * c; gq1 = 4 * c + 2; gs = 0; } else { const int q0 = 24 + 3 * (c - 6), odd = (c - 6) & 1; gq0 = q0 + odd; gq1 = 0; gs = odd ? q0 : q0 + 2; }
    const u32 voffQ = (u32)(wid * 8 + r8) * (NQKV_ * 2) + csw * 16;
    const char* wq0 = (const char*)A.wqkv + gq0 * 64;
    const char* xs = (const char*)A.x + (size_t)rb * 128 * (D_ * 2);
    u32 voffX[2];
#pragma unroll
    for (int i = 0; i < 2; ++i) { const int row = 16 * wid + 8 * i + r8; voffX[i] = (u32)row * (D_ * 2) + ((cp ^ ((row >> 1) & 7)) << 4); }
    const u32 XA = lds0 + P1_SLOT, XB = lds0 + 4 * P1_SLOT;
#define P1_LDQ(sb, j, slot) _Pragma("unroll") for (int k = 0; k < 3; ++k) glds16s((sb) + (size_t)((j) * 192 + k * 64) * (NQKV_ * 2), voffQ, lds0 + (slot) * P1_SLOT + wid * 1024 + k * 8192)
#define P1_LDX(piece, dst) _Pragma("unroll") for (int i = 0; i < 2; ++i) glds16s(xs + (piece) * 128, voffX[i], (dst) + (16 * wid + 8 * i) * 128)
    P1_LDQ(wq0, 0, 0);
    P1_LDX(0, XA); P1_LDX(1, XA + 16384); P1_LDX(2, XA + 32768);
    asm volatile("s_barrier" ::: "memory");
    P1_LDQ(wq0, 1, 3);
    P1_LDX(3, XB); P1_LDX(4, XB + 16384); P1_LDX(5, XB + 32768);
    const int srow = rb * 128 + c * 8 + wid;
    u32x4 sx0, sx1, sg0, sg1, sw[5]; u64 e0; u32 sb1;
    { const u32 o0 = lane * 16, o1 = 1024 + (lane & 7) * 16;
      const char* xr = (const char*)A.x + (size_t)srow * (D_ * 2);
      LD16A(sx0, o0, xr); LD16A(sx1, o1, xr); LD16A(sg0, o0, (const char*)A.gw); LD16A(sg1, o1, (const char*)A.gw);
      const char* wc = (const char*)A.w1 + (size_t)(72 * wid) * (F_ * 2) + (9 * c + (rb < 9 ? rb : 0)) * 64;
#pragma unroll
      for (int i = 0; i < 5; ++i) { int r = (lane >> 2) + 16 * i; if (r > 71) r = 71; const u32 o = (u32)r * (F_ * 2) + (lane & 3) * 16; LD16A(sw[i], o, wc); }
      asm volatile("global_load_ushort %0, %1, %2" : "=v"(sb1) : "v"((u32)(lane & 31) * 2), "s"((const char*)A.b1 + (9 * c + (rb < 9 ? rb : 0)) * 64) : "memory");
      asm volatile("global_load_dwordx2 %0, %1, %2 sc1" : "=v"(e0) : "v"(0u), "s"(epoch_ptr(A.ctrl)) : "memory"); }
    if (tid < 4) *(LAS u32x4*)((lptr)(size_t)lds0 + P1_WEXT + 288 * 16 + tid * 16) = (u32x4){0u, 0u, 0u, 0u};
    const int rg = wid & 3, ch = wid >> 2;
    P1Ctx X; X.w1 = (const char*)A.w1; X.hrs = mkrsrc(A.hb + (size_t)(rb * 4 + rg) * 144 * 2048, 144 * 2048); X.c = c; X.l16 = lane * 16;
    const int nch = c < 6 ? 7 : 6; X.nch = nch; X.g1first = 9 * c + (c & 1);
    const int m = lane & 31, h = lane >> 5;
#pragma unroll
    for (int k = 0; k < 3; ++k) X.voffW[k] = (u32)(wid * 8 + r8 + 64 * k) * (F_ * 2) + csw * 16;
    X.ldst0 = lds0 + wid * 1024; X.rs = 0; X.ld = X.ldst0; X.lb = X.w1; X.la = X.w1;
    { const int s16 = (lane >> 4) & 1, q = (lane & 15) >> 2, p = lane & 3;
      X.trbase = (lcptr)(size_t)lds0 + (8 * h + q) * 128 + (((4 * ch + 2 * s16 + (p >> 1)) ^ (((q >> 1) & 1) << 2)) * 16) + (p & 1) * 8;
      X.wexb = (lcptr)(size_t)lds0 + (h ? P1_WEXT + 288 * 16 : P1_WEXT + m * 16); X.wexs = h ? 0 : 512; }
    const int wsingle = (c & 1) ? 9 * c : 9 * c + 8;
    bf16x8 xf[36];
    f32x16 accA, accB;
    const bool swp = c >= 10;
    const int xrow = rg * 32 + m, xz = (xrow >> 1) & 7;
    X.rcb = (lcptr)(size_t)lds0 + P1_ROW + xrow * 32 + 16;
    X.xeb = h ? (lcptr)(size_t)lds0 + P1_WEXT + 288 * 16 : X.rcb - 16;
    lcptr xa = (lcptr)(size_t)XA + xrow * 128, xb = (lcptr)(size_t)XB + xrow * 128;
#define P1_XF(s0, base) _Pragma("unroll") for (int s = 0; s < 12; ++s) xf[s0 + s] = *(const LAS bf16x8*)((base) + (s >> 2) * 16384 + (((2 * (s & 3) + h) ^ xz) << 4))
#define P1_QS(J, LD, P) do { if (swp) p1_qkv_stage<true, J, LD>(X, P, xf, accA); else p1_qkv_stage<false, J, LD>(X, P, xf, accA); } while (0)
    STAMP(1);
    P1_WAITBAR(20);
    STAMP(2);
    P1_XF(0, xa);
    P1_WAITBAR(20);
    P1_LDX(6, XA); P1_LDX(7, XA + 16384); P1_LDX(8, XA + 32768);
    P1_QS(0, false, X.trbase);
    STAMP(3);
    P1_WAITBAR(6);
    STAMP(4);
    P1_LDQ(wq0, 2, 0);
    P1_XF(12, xb);
    P1_QS(1, false, X.trbase + 3 * P1_SLOT);
    asm volatile("" : "+v"(sx0), "+v"(sx1), "+v"(sg0), "+v"(sg1), "+v"(sw[0]), "+v"(sw[1]), "+v"(sw[2]), "+v"(sw[3]), "+v"(sw[4]), "+v"(e0), "+v"(sb1) :: "memory");
    ep = ((u64)(u32)RFL((u32)(e0 >> 32)) << 32 | (u32)RFL((u32)e0)) + 1;
    {
        typedef __bf16 bf2 __attribute__((ext_vector_type(2)));
        const bool two = lane < 8;
        const bf2 one2 = __builtin_bit_cast(bf2, 0x3f803f80u);
        float s = 0.f, gsum = 0.f, q2 = 0.f;
#pragma unroll
        for (int j = 0; j < 8; ++j) { const u32 xw = j < 4 ? sx0[j] : (two ? sx1[j - 4] : 0u), gw = j < 4 ? sg0[j] : sg1[j - 4]; const bf2 xv = __builtin_bit_cast(bf2, xw);
            s = __builtin_amdgcn_fdot2_f32_bf16(xv, one2, s, false); gsum = __builtin_amdgcn_fdot2_f32_bf16(xv, __builtin_bit_cast(bf2, gw), gsum, false); q2 = __builtin_amdgcn_fdot2_f32_bf16(xv, xv, q2, false); }
        s = wave_sum(s); gsum = wave_sum(gsum); q2 = wave_sum(q2);
        const float m = s * (1.f / 576.f);
        const float q = fmaxf(q2 - 576.f * m * m, 0.f);
        const float sd = __builtin_amdgcn_sqrtf(q * (1.f / 576.f) + 1e-5f), r = __builtin_amdgcn_rcpf(sd);
        const float eg = __builtin_amdgcn_exp2f(-1.4426950408889634f * gsum), gate = __builtin_amdgcn_rcpf(1.f + eg);
        const float sdg = sd * (1.f + __builtin_amdgcn_exp2f(1.4426950408889634f * gsum));
        const u32 mh = bfbits(-m), ml = bfbits(-m - __uint_as_float(mh << 16)), sh = bfbits(sd), sl = bfbits(sd - __uint_as_float(sh << 16));
        const u32x4 rec = lane == 0 ? (u32x4){mh | (mh << 16), ml | (sh << 16), sl, 0u} : (u32x4){__float_as_uint(GC0 * r), __float_as_uint(GC1 * r * r * r), __float_as_uint(sdg), __float_as_uint(gate)};
        if (lane < 2) st16_sc1(rec, mkrsrc(A.rowrec, S_ * 32), srow * 32 + lane * 16);
        if (rb < 9) {
        float cs[8];
#pragma unroll
        for (int j = 0; j < 8; ++j) cs[j] = 0.f;
#pragma unroll
        for (int i = 0; i < 5; ++i)
#pragma unroll
            for (int j = 0; j < 4; ++j) { const u32 wv = (i < 4 || lane < 32) ? sw[i][j] : 0u; cs[2 * j] += bflo(wv); cs[2 * j + 1] += bfhi(wv); }
#pragma unroll
        for (int j = 0; j < 8; ++j) { float v = cs[j]; v += dpp_ror4(v); v += dpp_ror8(v); cs[j] = xadd32(xadd16(v)); }
        if (lane < 4) { LAS f32x4* d = (LAS f32x4*)((lptr)(size_t)lds0 + P1_CS + wid * 128 + lane * 32); d[0] = (f32x4){cs[0], cs[1], cs[2], cs[3]}; d[1] = (f32x4){cs[4], cs[5], cs[6], cs[7]}; }
        }
    }
    STAMP(5);
    P1_WAITBAR(3);
    STAMP(6);
    if (wid == 0 && lane < 32 && rb < 9) {
        float t = 0.f;
#pragma unroll
        for (int w = 0; w < 8; ++w) t += *(const LAS float*)((lcptr)(size_t)lds0 + P1_CS + w * 128 + lane * 4);
        const int col = (9 * c + rb) * 32 + lane;
        const u32 chh = bfbits(t), cl = bfbits(t - __uint_as_float(chh << 16)), bb = sb1;
        st16_sc1((u32x4){chh | (cl << 16), chh | (bb << 16), bb, 0u}, mkrsrc(A.wext, F_ * 16), col * 16);
    }
    if (c < 6) { const char* wq1 = (const char*)A.wqkv + gq1 * 64; P1_LDQ(wq1, 0, 3); P1_LDQ(wq1, 1, 4); P1_LDQ(wq1, 2, 5); }
    else {
        const bool hi = csw >= 4; const u32 pitch = hi ? F_ * 2 : NQKV_ * 2;
        const char* lp = (hi ? (const char*)A.w1 + wsingle * 64 : (const char*)A.wqkv + gs * 64) + (size_t)((u32)(wid * 8 + r8) * pitch) + (csw & 3) * 16;
#pragma unroll
        for (int k = 0; k < 9; ++k) { glds16(lp, lds0 + 3 * P1_SLOT + wid * 1024 + k * 8192); lp += pitch * 64; }
    }
    P1_XF(24, xa);
    P1_WAITBAR(9);
    P1_QS(2, false, X.trbase);
    const __amdgpu_buffer_rsrc_t qrs = mkrsrc(A.qkvb + (size_t)(rb * 4 + rg) * 54 * 2048, 54 * 2048);
    p1_qkv_store(qrs, (gq0 + ch) * 2048, lane, accA, gq0 + ch < 18 ? QSCALE : 1.f);
    STAMP(7);
    P1_WAITBAR(0);
    if (tid == 0) st_agent(flag_ptr(A.ctrl, F_STAT, blockIdx.x), ep);
    p1_top3(X);
    const bool c1q = c < 6 || ch == 0;
    bf16x8 wf[6];
#pragma unroll
    for (int k = 0; k < 6; ++k) wf[k] = xf[k];
    if (c1q) {
        p1_top(X, 1, 0); P1_QS(0, true, X.trbase + X.rs);
        p1_top(X, 1, 1); P1_QS(1, true, X.trbase + X.rs);
        p1_top(X, 1, 2); P1_QS(2, true, X.trbase + X.rs);
        const int g = c < 6 ? gq1 + ch : gs;
        p1_qkv_store(qrs, g * 2048, lane, accA, g < 18 ? QSCALE : 1.f);
    } else p1_w1_chunk(X, 1, 0, false, xf, wf, accB, accA);
    STAMP(8);
    { const int k = lane < 16 ? p1_wg(rb, lane) : p1_wg(lane - 16, c);
      poll_flags(flag_ptr(A.ctrl, F_STAT, k), lane < 25, ep);
      u32x4 t0 = {}, t1 = {};
      if (tid < 256) t0 = ld16_sc1(mkrsrc(A.rowrec, S_ * 32), (rb * 256 + tid) * 16);
      if (tid < 288) t1 = ld16_sc1(mkrsrc(A.wext, F_ * 16), (288 * c + tid) * 16);
      if (tid < 256) *(LAS u32x4*)((lptr)(size_t)lds0 + P1_ROW + tid * 16) = t0;
      if (tid < 288) *(LAS u32x4*)((lptr)(size_t)lds0 + P1_WEXT + tid * 16) = t1; }
    P1_WAITBAR(0);
    if (tid == 0) st_agent(flag_ptr(A.ctrl, F_QKV, blockIdx.x), ep);
    const int iend = (nch == 7 && ch == 1) ? 6 : nch;
#define P1_G(i) ((i) == 1 ? wsingle : p1_w1g(X, i) + ((i) < 6 ? ch : 0))
#define P1_ST1
#define P1_ST2
    u64 fq = 0;
    for (int i = 2; i < iend; i += 2) {
        p1_w1_chunk(X, i, P1_G(i - 1), i > 2 || !c1q, xf, wf, accA, accB); P1_ST1
        if (wid == 0) { if (i == 2) poll_issue(fq, attn_flag(A, lane)); else if (i == 4) { poll_check(fq, attn_flag(A, lane), lane < 18, ep); } }
        if (i + 1 < iend) p1_w1_chunk(X, i + 1, P1_G(i), true, xf, wf, accB, accA); P1_ST2
    }
    if (iend < nch) { P1_WAITBAR(12); P1_WAITBAR(9); P1_WAITBAR(6); }
    BARRIER();
    attn_stage(A, lds0, wid, lane);
    int ie = iend; asm volatile("" : "+s"(ie));
    if ((ie & 1) != 0) accB = accA;
    { Epi E; const int lg = P1_G(ie - 1); const u32 vo = X.l16 + (u32)lg * 2048;
      const bf16x8 we = *(const LAS bf16x8*)(X.wexb + (lg - 9 * X.c) * X.wexs), xe = *(const LAS bf16x8*)X.xeb;
      p1_consts(X, E);
#pragma unroll
      for (int s = 0; s < 4; ++s) accB = MFMA32(wf[(s + 2) % 6], xf[32 + s], accB);
      accB = MFMA32(we, xe, accB);
#pragma unroll
      for (int s = 0; s < 32; ++s) p1_gelu_slot(s, X, accB, E, vo); }
    STAMP(15);
    STAMP_OUT(A.stamps, 16);
}
__device__ __forceinline__ int qk_off(int c) { const int cc = c & 31; return (c >> 5) * 2048 + (2 * (cc >> 4) + ((cc >> 2) & 1)) * 512 + (((cc >> 3) & 1) * 4 + (cc & 3)) * 2; }
__device__ __forceinline__ float xmax16(float v) { auto r = __builtin_amdgcn_permlane16_swap(__float_as_uint(v), __float_as_uint(v), false, false); return fmaxf(__uint_as_float(r[0]), __uint_as_float(r[1])); }
__device__ __forceinline__ float xmax32(float v) { auto r = __builtin_amdgcn_permlane32_swap(__float_as_uint(v), __float_as_uint(v), false, false); return fmaxf(__uint_as_float(r[0]), __uint_as_float(r[1])); }
__device__ __forceinline__ float max3f(float a, float b, float c) { return __builtin_fmaxf(__builtin_fmaxf(a, b), c); }

template <bool EARLY>
__device__ __forceinline__ void attn_mask(f32x4& s, int t, int dq, int kb) {
    if (EARLY) {
#pragma unroll
        for (int r = 0; r < 4; ++r) { const int d = dq - 16 * t - r; const bool ok = (u32)d <= 255u && (kb + 16 * t + r) >= 0; s[r] = ok ? s[r] : -INFINITY; }
    } else if (t < 2) {
#pragma unroll
        for (int r = 0; r < 4; ++r) s[r] = (dq - 16 * t - r <= 255) ? s[r] : -INFINITY;
    } else if (t >= 16) {
#pragma unroll
        for (int r = 0; r < 4; ++r) s[r] = (dq - 16 * t - r >= 0) ? s[r] : -INFINITY;
    }
}
#define ATS_ARG
#define ATS_PASS
template <bool EARLY>
__device__ __forceinline__ void attn_units(lcptr L, int c00  , int cones, int w, int lane, int Q0, float gate, __amdgpu_buffer_rsrc_t ors, int head0, gu64* fhp, u64 ep ATS_ARG) {
    u64 fh = 0; if (w == 0) poll_issue(fh, fhp);
    const int i16 = lane & 15, g = lane >> 4;
    const int lb0 = w >> 1;
    const int q = Q0 + 16 * w + i16, kb = Q0 - 256 + lb0 * 32 + 4 * g, dq = q - kb;
    bf16x8 kf[2][18], qf[2];
    f32x4 nm[2];
#pragma unroll
    for (int u = 0; u < 2; ++u) {
        const int c0 = c00 + 18 * u;
        const int p2 = c0 & 2, op = qk_off(c0 + p2 + 4 * g), os = qk_off(p2 ? c0 - 2 : c0 + 16);
        lcptr qp = L + AT_Q + (w >> 1) * 4096 + ((w & 1) * 16 + i16) * 16;
        const u32x2 qa = *(const LAS u32x2*)(qp + op), qc = *(const LAS u32x2*)(qp + os);
        qf[u] = __builtin_bit_cast(bf16x8, (u32x4){qa[0], qa[1], (g == 0 && !p2) ? qc[0] : 0u, (g == 0 && p2) ? qc[1] : 0u});
        lcptr kb0 = L + AT_K + lb0 * 4096 + i16 * 16;
        lcptr k0 = kb0 + op, k1 = kb0 + os;
        PIN(k0); PIN(k1);
#pragma unroll
        for (int t = 0; t < 18; ++t) { const int o = (t >> 1) * 4096 + (t & 1) * 256;
            const u32x2 ka = *(const volatile LAS u32x2*)(k0 + o), kc = *(const volatile LAS u32x2*)(k1 + o);
            kf[u][t] = __builtin_bit_cast(bf16x8, (u32x4){ka[0], ka[1], kc[0], kc[1]}); }
    }
    lcptr va[2], vb[2];
#pragma unroll
    for (int u = 0; u < 2; ++u) {
        const int c0 = c00 + 18 * u, ca = c0 + i16, cb = (i16 < 2) ? c0 + 16 + i16 : (i16 == 2 ? cones : c0);
        va[u] = L + AT_V + lb0 * 4096 + (ca >> 5) * 2048 + (g & 1) * 512 + (ca & 31) * 16 + (g >> 1) * 8;
        vb[u] = L + AT_V + lb0 * 4096 + (cb >> 5) * 2048 + (g & 1) * 512 + (cb & 31) * 16 + (g >> 1) * 8;
        PIN(va[u]); PIN(vb[u]);
    }
    SBAR();
    STAMP(5);
#pragma unroll
    for (int u = 0; u < 2; ++u) {
        f32x4 s[18];
#pragma unroll
        for (int t = 0; t < 18; ++t) s[t] = MFMA16(kf[u][t], qf[u], ((f32x4){0.f, 0.f, 0.f, 0.f}));
#pragma unroll
        for (int t = 0; t < 18; ++t) attn_mask<EARLY>(s[t], t, dq, kb);
        float m0 = max3f(s[0][0], s[0][1], s[0][2]), m1 = max3f(s[0][3], s[1][0], s[1][1]);
        m0 = max3f(m0, s[1][2], s[1][3]);
#pragma unroll
        for (int t = 2; t < 18; t += 2) { m0 = max3f(m0, s[t][0], s[t][1]); m1 = max3f(m1, s[t][2], s[t][3]); m0 = max3f(m0, s[t + 1][0], s[t + 1][1]); m1 = max3f(m1, s[t + 1][2], s[t + 1][3]); }
        const float m = -xmax32(xmax16(fmaxf(m0, m1)));
        nm[u] = (f32x4){m, m, m, m};
    }
    if (w == 0) { WAIT_VM0(); poll_check(fh, fhp, lane < 4, ep); }
    STAMP(6);
    u32x2 vf[3][4]; f32x4 sc2[2][2];
#define AT_VRD(k) do { const int b_ = (k) >> 1, u_ = (k) & 1; vf[(k) % 3][0] = *(const volatile LAS u32x2*)(va[u_] + b_ * 4096); vf[(k) % 3][1] = *(const volatile LAS u32x2*)(va[u_] + b_ * 4096 + 1024); \
        vf[(k) % 3][2] = *(const volatile LAS u32x2*)(vb[u_] + b_ * 4096); vf[(k) % 3][3] = *(const volatile LAS u32x2*)(vb[u_] + b_ * 4096 + 1024); } while (0)
#define AT_QK(k) do { const int b_ = (k) >> 1, u_ = (k) & 1; sc2[(k) & 1][0] = MFMA16(kf[u_][2 * b_], qf[u_], nm[u_]); sc2[(k) & 1][1] = MFMA16(kf[u_][2 * b_ + 1], qf[u_], nm[u_]); } while (0)
    f32x4 oa[2] = {{0.f, 0.f, 0.f, 0.f}, {0.f, 0.f, 0.f, 0.f}}, obb[2] = {{0.f, 0.f, 0.f, 0.f}, {0.f, 0.f, 0.f, 0.f}};
    AT_VRD(0); AT_VRD(1); AT_QK(0);
    SBAR();
#pragma unroll
    for (int k = 0; k < 18; ++k) {
        const int b = k >> 1, u = k & 1;
        f32x4 s0 = sc2[k & 1][0], s1 = sc2[k & 1][1];
        if (k + 1 < 18) { const int b_ = (k + 1) >> 1, u_ = (k + 1) & 1; sc2[(k + 1) & 1][0] = MFMA16(kf[u_][2 * b_], qf[u_], nm[u_]); sc2[(k + 1) & 1][1] = MFMA16(kf[u_][2 * b_ + 1], qf[u_], nm[u_]); PIN(sc2[(k + 1) & 1][0]); PIN(sc2[(k + 1) & 1][1]); }
        if (k + 2 < 18) AT_VRD(k + 2);
        attn_mask<EARLY>(s0, 2 * b, dq, kb); attn_mask<EARLY>(s1, 2 * b + 1, dq, kb);
        u32x4 pw;
        pw[0] = cvtpk_c(__builtin_amdgcn_exp2f(s0[0]), __builtin_amdgcn_exp2f(s0[1])); pw[1] = cvtpk_c(__builtin_amdgcn_exp2f(s0[2]), __builtin_amdgcn_exp2f(s0[3]));
        pw[2] = cvtpk_c(__builtin_amdgcn_exp2f(s1[0]), __builtin_amdgcn_exp2f(s1[1])); pw[3] = cvtpk_c(__builtin_amdgcn_exp2f(s1[2]), __builtin_amdgcn_exp2f(s1[3]));
        const bf16x8 pf = __builtin_bit_cast(bf16x8, pw);
        oa[u] = MFMA16(__builtin_bit_cast(bf16x8, ((u32x4){vf[k % 3][0][0], vf[k % 3][0][1], vf[k % 3][1][0], vf[k % 3][1][1]})), pf, oa[u]);
        obb[u] = MFMA16(__builtin_bit_cast(bf16x8, ((u32x4){vf[k % 3][2][0], vf[k % 3][2][1], vf[k % 3][3][0], vf[k % 3][3][1]})), pf, obb[u]);
        PIN(oa[u]); PIN(obb[u]);
        SBAR();
    }
    STAMP(7);
    auto off = [](int c) { const int cc = c & 31; return (c >> 5) * 2048 + (2 * (cc >> 4) + ((cc >> 2) & 1)) * 512 + (((cc >> 3) & 1) * 4 + (cc & 3)) * 2; };
    const u32 base = (u32)(q >> 5) * 18 * 2048 + (q & 31) * 16;
    const float sc0 = gate * __builtin_amdgcn_rcpf(__shfl(obb[0][2], i16)), sc1 = gate * __builtin_amdgcn_rcpf(__shfl(obb[1][2], i16));
    const u32 p1 = cvtpk(oa[1][0] * sc1, oa[1][1] * sc1), r1 = cvtpk(oa[1][2] * sc1, oa[1][3] * sc1), t1 = cvtpk(obb[1][0] * sc1, obb[1][1] * sc1);
    const u32 nx = (u32)__shfl((int)(g == 0 ? t1 : p1), (lane + 16) & 63);
    const int col0 = 18 * head0;
    typedef u32 u32x2s __attribute__((ext_vector_type(2)));
    __builtin_amdgcn_raw_buffer_store_b64((u32x2s){cvtpk(oa[0][0] * sc0, oa[0][1] * sc0), cvtpk(oa[0][2] * sc0, oa[0][3] * sc0)}, ors, base + off(col0 + 4 * g), 0, 16);
    __builtin_amdgcn_raw_buffer_store_b64((u32x2s){r1, nx}, ors, base + off(col0 + 20 + 4 * g), 0, 16);
    if (g == 0) __builtin_amdgcn_raw_buffer_store_b64((u32x2s){cvtpk(obb[0][0] * sc0, obb[0][1] * sc0), p1}, ors, base + off(col0 + 16), 0, 16);
}

__device__ __forceinline__ int attn_wg(int qb, int hp) { return (hp & 7) + 8 * (2 * qb + (hp >> 3)); }
__device__ __forceinline__ void acquire_agent() { __builtin_amdgcn_fence(__ATOMIC_ACQUIRE, "agent"); WAIT_VM0(); }

__device__ __forceinline__ void attn_phase(const Args& A, char* smem, u64 ep) {
    STAMP_DECL STAMP(0);
    const int tid = threadIdx.x, lane = tid & 63, wid = RFL(tid >> 6);
    const int xcd = blockIdx.x & 7, idx = blockIdx.x >> 3;
    const int qb = idx >> 1, hp = xcd + 8 * (idx & 1);
    const int Q0 = qb * 128, cg0 = (36 * hp) >> 5, c0 = 36 * hp - 32 * cg0;
    const u32 lds0 = (u32)(uintptr_t)smem;
    gu64* fhp;
    { const int sp = blockIdx.x & 7, mt = (blockIdx.x >> 3) & 7; fhp = flag_ptr(A.ctrl, F_H, p1_wg(2 * mt + (lane & 1), 2 * sp + ((lane >> 1) & 1))); }
    const int q = Q0 + 16 * wid + (lane & 15);
    const float gate = __uint_as_float(__builtin_amdgcn_raw_buffer_load_b32(mkrsrc(A.rowrec, S_ * 32), q * 32 + 28, 0, 16));
    WAIT_VM0(); BARRIER();
    if (tid == 0) st_agent(flag_ptr(A.ctrl, F_H, blockIdx.x), ep);
    STAMP(2);
    const int cones = (c0 + 36) & 63;
    if (tid < 48) { const int kb = tid >> 2, pc = tid & 3; *(LAS u32x4*)((lptr)(size_t)lds0 + AT_V + kb * 4096 + (cones >> 5) * 2048 + pc * 512 + (cones & 31) * 16) = (u32x4){0x3f803f80u, 0x3f803f80u, 0x3f803f80u, 0x3f803f80u}; }
    BARRIER();
    STAMP(3);
    const __amdgpu_buffer_rsrc_t ors = mkrsrc(A.ob, S_ * D_ * 2);
    if (qb < 2) attn_units<true>((lcptr)(size_t)lds0, c0, cones, wid, lane, Q0, gate, ors, 2 * hp, fhp, ep ATS_PASS); else attn_units<false>((lcptr)(size_t)lds0, c0, cones, wid, lane, Q0, gate, ors, 2 * hp, fhp, ep ATS_PASS);
    STAMP(8);
    BARRIER();
    STAMP(4);
    STAMP_OUT(A.stamps + (2u << 17), 9);
}
constexpr int P3_A = 32768, P3_B = 64 * 288, P3_SLOT = P3_A + P3_B, P3_LDS = 3 * P3_SLOT;
struct P3Ctx {
    const char* pA; const char* pB;
    const char* oA; const char* oB;
    u32 aStride;
    int tl, nst; u32 sl;
    u32 dA, dB;
    u32 vA, vB0, vB1, vB2;
    lcptr ab, bb;
};
__device__ __forceinline__ void glds_c2(int tl, int nst, const void* sbase, u32 voff, u32 lds_base) {
    u32 sv;
    asm volatile("s_cmp_ge_i32 %4, %5\n\ts_cbranch_scc1 1f\n\ts_mov_b32 %0, m0\n\ts_mov_b32 m0, %3\n\ts_nop 0\n\tglobal_load_lds_dwordx4 %1, %2 sc1\n\tglobal_load_lds_dwordx4 %1, %2 offset:1024 sc1\n\ts_mov_b32 m0, %0\n1:"
                 : "=&s"(sv) : "v"(voff), "s"(sbase), "s"(lds_base), "s"(tl), "s"(nst) : "memory", "scc");
}
__device__ __forceinline__ void glds_c1(int tl, int nst, const void* sbase, u32 voff, u32 lds_base) {
    u32 sv;
    asm volatile("s_cmp_ge_i32 %4, %5\n\ts_cbranch_scc1 1f\n\ts_mov_b32 %0, m0\n\ts_mov_b32 m0, %3\n\ts_nop 0\n\tglobal_load_lds_dwordx4 %1, %2\n\ts_mov_b32 m0, %0\n1:"
                 : "=&s"(sv) : "v"(voff), "s"(sbase), "s"(lds_base), "s"(tl), "s"(nst) : "memory", "scc");
}
__device__ __forceinline__ void glds_cq(int tl, int nst, const void* sbase, u32 voff, u32 lds_base) {
    u32 sv;
    asm volatile("s_cmp_ge_i32 %4, %5\n\ts_cbranch_scc1 1f\n\ts_mov_b32 %0, m0\n\ts_mov_b32 m0, %3\n\ts_mov_b64 exec, 0xffff\n\tglobal_load_lds_dwordx4 %1, %2\n\ts_mov_b64 exec, -1\n\ts_mov_b32 m0, %0\n1:"
                 : "=&s"(sv) : "v"(voff), "s"(sbase), "s"(lds_base), "s"(tl), "s"(nst) : "memory", "scc");
}
__device__ __forceinline__ void p3_load_part(P3Ctx& X, int k) {
    if (k == 0) glds_c2(X.tl, X.nst, X.pA, X.vA, X.dA + X.sl);
    if (k == 1) glds_c2(X.tl, X.nst, X.pA + 2048, X.vA, X.dA + X.sl + 2048);
    if (k == 2) glds_c1(X.tl, X.nst, X.pB, X.vB0, X.dB + X.sl);
    if (k == 3) glds_c1(X.tl, X.nst, X.pB, X.vB1, X.dB + X.sl + 1024);
    if (k == 4) { glds_cq(X.tl, X.nst, X.pB, X.vB2, X.dB + X.sl + 2048);
        X.tl += 1; const bool sw = X.tl == 9; X.pA = sw ? X.oA : X.pA + 4096; X.pB = sw ? X.oB : X.pB + 64 * 1152; X.sl = X.sl == 2 * P3_SLOT ? 0 : X.sl + P3_SLOT; }
}
struct P3Frag { bf16x8 h[4]; bf16x8 w[5]; };
__device__ __forceinline__ void p3_waitbar(int rem) {
    asm volatile("s_cmp_lt_i32 %0, 1\n\ts_cbranch_scc1 2f\n1:\ts_waitcnt vmcnt(7) lgkmcnt(0)\n\ts_barrier\n\t.subsection 1\n2:\ts_waitcnt vmcnt(0)\n\ts_branch 1b\n\t.subsection 0" :: "s"(rem) : "memory", "scc");
}
template <int NT, int HF, bool RD, bool MM>
__device__ __forceinline__ void p3_half(P3Ctx& X, int t, u32 so, P3Frag& R, const P3Frag& P, f32x4 (&acc)[4][5]) {
    lcptr a = X.ab + so + HF * 2048, b = X.bb + so + HF * (32 * 288);
    if (RD) { if (HF == 0) p3_waitbar(X.nst - 1 - t); PIN(a); PIN(b); }
    s16x4 lo;
    SBAR();
#pragma unroll
    for (int i = 0; i < 4 * NT; ++i) {
        if (MM) acc[i & 3][i >> 2] = MFMA16(P.w[i >> 2], P.h[i & 3], acc[i & 3][i >> 2]);
        if (RD) {
            if (i < 4) R.h[i] = *(const LAS bf16x8*)(a + (i >> 1) * 4096 + (i & 1) * 256);
            else if (i < 4 + 2 * NT) { const int j = i - 4; if (j & 1) R.w[j >> 1] = cat8(lo, trd(b + (j >> 1) * 32 + 8 * 288)); else lo = trd(b + (j >> 1) * 32); }
            if (HF == 0) { if (i == (NT == 5 ? 2 : 6)) p3_load_part(X, 0); if (i == (NT == 5 ? 10 : 13)) p3_load_part(X, 1); }
            else { if (i == (NT == 5 ? 1 : 4)) p3_load_part(X, 2); if (i == (NT == 5 ? 8 : 10)) p3_load_part(X, 3); if (i == (NT == 5 ? 15 : 14)) p3_load_part(X, 4); }
        }
        SBAR();
    }
}
template <int NT>
__device__ __forceinline__ void p3_loop(P3Ctx& X, int nhalf, f32x4 (&acc)[4][5], bool w0, gu64* fo, bool fo_use, u64 ep, gu64* own_o) {
    u64 fv = 0;
    WAIT_VM0(); BARRIER();
    if (threadIdx.x == 0) st_agent(own_o, ep);
    P3Frag F0, F1;
    u32 so = 0;
    p3_half<NT, 0, true, false>(X, 0, so, F0, F1, acc);
    p3_half<NT, 1, true, true>(X, 0, so, F1, F0, acc);
    int t = 1;
    for (; 2 * t + 1 < nhalf; ++t) {
        so = so == 2 * P3_SLOT ? 0 : so + P3_SLOT;
        if (w0) { if (t == 2) poll_issue(fv, fo); else if (t == 4) poll_check(fv, fo, fo_use, ep); }
        p3_half<NT, 0, true, true>(X, t, so, F0, F1, acc);
        p3_half<NT, 1, true, true>(X, t, so, F1, F0, acc);
    }
    if (2 * t < nhalf) {
        so = so == 2 * P3_SLOT ? 0 : so + P3_SLOT;
        p3_half<NT, 0, true, true>(X, t, so, F0, F1, acc);
        p3_half<NT, 1, false, true>(X, t, so, F1, F0, acc);
    } else p3_half<NT, 0, false, true>(X, t, so, F0, F1, acc);
}
__device__ __forceinline__ void p3_phase(const Args& A, char* smem, u64 ep) {
    STAMP_DECL STAMP(0);
    const int tid = threadIdx.x, lane = tid & 63, wid = RFL(tid >> 6);
    const int sp = blockIdx.x & 7, idx = blockIdx.x >> 3;
    const int mt = idx & 7, nt = idx >> 3;
    const u32 lds0 = (u32)(uintptr_t)smem;
    const int mq = wid >> 1, nh = (wid & 1) ^ (wid >> 2);
    const int i16 = lane & 15, kq = lane >> 4;
    P3Ctx X;
    const int no = (sp & 3) == 0 ? 3 : 2, o0 = (sp >> 2) * 9 + ((sp & 3) == 0 ? 0 : 1 + 2 * (sp & 3));
    STAMP(4);
    const int nhalf = 18 + no; X.nst = (nhalf + 1) >> 1; X.tl = 0; X.sl = 0;
    X.pA = A.hb + (size_t)((mt * 8 + wid) * 144 + 18 * sp) * 2048;
    X.oA = A.ob + (size_t)((mt * 8 + wid) * 18 + o0) * 2048;
    X.pB = (const char*)A.w2 + (size_t)(576 * sp) * 1152 + 288 * nt;
    X.oB = (const char*)A.wo + (size_t)(32 * o0) * 1152 + 288 * nt;
    X.dA = lds0 + wid * 4096; X.dB = lds0 + P3_A + wid * 144 * 16;
    X.vA = lane * 16;
    { const int e0 = wid * 144 + lane, e1 = e0 + 64, e2 = e0 + 128;
      X.vB0 = (e0 / 18) * 1152 + (e0 % 18) * 16; X.vB1 = (e1 / 18) * 1152 + (e1 % 18) * 16; X.vB2 = (e2 / 18) * 1152 + (e2 % 18) * 16; }
    X.ab = (lcptr)(size_t)lds0 + (mq * 2) * 4096 + kq * 512 + i16 * 16;
    X.bb = (lcptr)(size_t)lds0 + P3_A + (16 * (kq >> 1) + 4 * (kq & 1) + (i16 >> 2)) * 288 + (nh ? 5 * 32 : 0) + (i16 & 3) * 8;
#pragma unroll
    for (int t = 0; t < 2; ++t)
#pragma unroll
        for (int k = 0; k < 5; ++k) p3_load_part(X, k);
    f32x4 acc[4][5];
#pragma unroll
    for (int m = 0; m < 4; ++m)
#pragma unroll
        for (int n = 0; n < 5; ++n) acc[m][n] = (f32x4){0.f, 0.f, 0.f, 0.f};
    STAMP(1);
    const int hp0 = (32 * o0) / 36, hp1 = (32 * (o0 + no) - 1) / 36, hp = hp0 + (lane >> 1);
    gu64* fo = flag_ptr(A.ctrl, F_O, attn_wg(2 * mt + (lane & 1), hp <= hp1 ? hp : hp0));
    if (nh == 0) p3_loop<5>(X, nhalf, acc, wid == 0, fo, hp <= hp1, ep, flag_ptr(A.ctrl, F_O, blockIdx.x)); else p3_loop<4>(X, nhalf, acc, wid == 0, fo, hp <= hp1, ep, flag_ptr(A.ctrl, F_O, blockIdx.x));
    STAMP(2);
    const u32 tile = (u32)(mt * 4 + nt) * 8 * (72 * 1024);
    const __amdgpu_buffer_rsrc_t prs = mkrsrc(A.part + tile, 8 * 72 * 1024);
    const int NTN = nh ? 4 : 5;
#pragma unroll
    for (int pr = 0; pr < 2; ++pr)
#pragma unroll
        for (int n = 0; n < 5; ++n)
            if (n < NTN) st16_sc1((u32x4){cvtpk_c(acc[2 * pr][n][0], acc[2 * pr][n][1]), cvtpk_c(acc[2 * pr][n][2], acc[2 * pr][n][3]),
                                          cvtpk_c(acc[2 * pr + 1][n][0], acc[2 * pr + 1][n][1]), cvtpk_c(acc[2 * pr + 1][n][2], acc[2 * pr + 1][n][3])}, prs,
                                  sp * (72 * 1024) + ((mq * 2 + pr) * 9 + (nh ? 5 : 0) + n) * 1024 + lane * 16);
    WAIT_VM0(); BARRIER();
    if (tid == 0) st_agent(flag_ptr(A.ctrl, F_PART, blockIdx.x), ep);
    STAMP(3);
    if (tid < 320) {
        const bool act = tid < 288;
        const int ck0 = act ? tid : 0;
        u32x2 bb[2]; float og[2][2];
#pragma unroll
        for (int r = 0; r < 2; ++r) { const int ck = ck0 + 288 * r, n = ck >> 6, ln = ck & 63;
            bb[r] = *(const u32x2*)(A.b2 + nt * 144 + n * 16 + (ln >> 4) * 4);
#pragma unroll
            for (int e = 0; e < 2; ++e) og[r][e] = 1.f - __uint_as_float(__builtin_amdgcn_raw_buffer_load_b32(mkrsrc(A.rowrec, S_ * 32), (mt * 256 + 32 * sp + 16 * e + (ln & 15)) * 32 + 28, 0, 16)); }
        poll_flags(flag_ptr(A.ctrl, F_PART, (lane & 7) + 8 * idx), lane < 8, ep);
        u32x4 pv[2][8];
#pragma unroll
        for (int r = 0; r < 2; ++r)
#pragma unroll
            for (int s = 0; s < 8; ++s) { const int ck = ck0 + 288 * r; pv[r][s] = ld16_sc1(prs, s * (72 * 1024) + (sp * 9 + (ck >> 6)) * 1024 + (ck & 63) * 16); }
#pragma unroll
        for (int r = 0; r < 2; ++r) {
            float v[8] = {0.f, 0.f, 0.f, 0.f, 0.f, 0.f, 0.f, 0.f};
#pragma unroll
            for (int s = 0; s < 8; ++s)
#pragma unroll
                for (int j = 0; j < 4; ++j) { v[2 * j] += bflo(pv[r][s][j]); v[2 * j + 1] += bfhi(pv[r][s][j]); }
            const int ck = ck0 + 288 * r, n = ck >> 6, ln = ck & 63;
#pragma unroll
            for (int e = 0; e < 2; ++e) {
                const int row = mt * 256 + 32 * sp + 16 * e + (ln & 15), col = nt * 144 + n * 16 + (ln >> 4) * 4;
                u32x2 o; o[0] = cvtpk(v[4 * e] + og[r][e] * bflo(bb[r][0]), v[4 * e + 1] + og[r][e] * bfhi(bb[r][0])); o[1] = cvtpk(v[4 * e + 2] + og[r][e] * bflo(bb[r][1]), v[4 * e + 3] + og[r][e] * bfhi(bb[r][1]));
                if (act) *(u32x2*)(A.out + (size_t)row * D_ + col) = o;
            }
        }
    }
    STAMP(5);
    STAMP_OUT(A.stamps + (1u << 17), 6);
}
constexpr int MEGA_LDS = 163840;
static_assert(P1_LDS <= MEGA_LDS && AT_LDS <= MEGA_LDS && P3_LDS <= MEGA_LDS, "LDS");
__global__ __launch_bounds__(512, 2) void mega_kernel(Args A) {
    extern __shared__ __attribute__((aligned(16))) char smem[];
    u64 ep;
    typedef __attribute__((address_space(4))) const u64* ArgP;
#if defined(__HIP_DEVICE_COMPILE__)
    ArgP ap = (ArgP)__builtin_amdgcn_kernarg_segment_ptr();
#else
    ArgP ap = nullptr;
#endif
#define LOAD_ARGS(a) Args a; { asm volatile("" : "+s"(ap)); u64 w_[17]; _Pragma("unroll") for (int i_ = 0; i_ < 17; ++i_) w_[i_] = ap[i_]; __builtin_memcpy(&a, w_, sizeof(Args)); }
    static_assert(sizeof(Args) == 17 * 8, "Args: 17 pointers");
    { LOAD_ARGS(a) p1_phase(a, smem, ep); }
    { LOAD_ARGS(a) attn_phase(a, smem, ep); }
    { LOAD_ARGS(a) p3_phase(a, smem, ep); }
    if (blockIdx.x == 0 && threadIdx.x < 64) {
        LOAD_ARGS(a) u64* ctrl = a.ctrl;
#pragma unroll
        for (int i = 0; i < 4; ++i) poll_flags(flag_ptr(ctrl, F_STAT, threadIdx.x + 64 * i), true, ep);
        if (threadIdx.x == 0) st_agent(epoch_ptr(ctrl), ep);
    }
}
extern "C" void kernel_launch(void* const* d_in, const int* in_sizes, int n_in, void* d_out, int out_size, void* d_ws, size_t ws_size, hipStream_t stream) {
    char* ws = (char*)d_ws;
    Args A;
    A.x = (const u16*)d_in[0]; A.wqkv = (const u16*)d_in[1]; A.wo = (const u16*)d_in[2]; A.w1 = (const u16*)d_in[4]; A.b1 = (const u16*)d_in[5];
    A.w2 = (const u16*)d_in[6]; A.b2 = (const u16*)d_in[7]; A.gw = (const u16*)d_in[8];
    A.rowrec = (u32x4*)ws;
    A.wext = (u32x4*)(ws + 65536);
    char* big = ws + 147456;
    A.qkvb = big;
    A.hb = big + 7077888;
    A.ob = big + 7077888 + 18874368;
    A.part = big + 7077888 + 18874368 + 2359296;
    A.ctrl = (u64*)(big + 7077888 + 18874368 + 2359296 + 18874368);
    A.out = (u16*)d_out;
    A.stamps = (unsigned long long*)(ws + (200u << 20));
    (void)hipFuncSetAttribute((const void*)mega_kernel, hipFuncAttributeMaxDynamicSharedMemorySize, MEGA_LDS);
    mega_kernel<<<256, 512, MEGA_LDS, stream>>>(A);
}
```

```cpp
#include <hip/hip_runtime.h>
#include <hip/hip_bf16.h>
typedef unsigned short u16;
typedef unsigned int u32;
#define LAS __attribute__((address_space(3)))
typedef short bf16x8 __attribute__((ext_vector_type(8)));
typedef short s16x4 __attribute__((ext_vector_type(4)));
typedef float f32x16 __attribute__((ext_vector_type(16)));
typedef float f32x4 __attribute__((ext_vector_type(4)));
typedef u32 u32x4 __attribute__((ext_vector_type(4)));
typedef u32 u32x2 __attribute__((ext_vector_type(2)));
typedef LAS char* lptr;
typedef LAS const char* lcptr;

constexpr int S_ = 2048, D_ = 576, H_ = 32, DH_ = 18, F_ = 4608, NQKV_ = 1728;

__device__ __forceinline__ float bf2f(u16 b) { return __uint_as_float(((u32)b) << 16); }
__device__ __forceinline__ float bflo(u32 w) { return __uint_as_float(w << 16); }
__device__ __forceinline__ float bfhi(u32 w) { return __uint_as_float(w & 0xffff0000u); }
typedef __bf16 bf16x2_t __attribute__((ext_vector_type(2)));
typedef float f32x2 __attribute__((ext_vector_type(2)));
__device__ __forceinline__ u32 cvtpk_c(float lo, float hi) { return __builtin_bit_cast(u32, __builtin_convertvector((f32x2){lo, hi}, bf16x2_t)); }
__device__ __forceinline__ u32 cvtpk(float lo, float hi) { u32 r; asm("v_cvt_pk_bf16_f32 %0, %1, %2" : "=v"(r) : "v"(lo), "v"(hi)); return r; }
__device__ __forceinline__ u16 f2bf(float f) { return (u16)(cvtpk(f, f) & 0xffffu); }
__device__ __forceinline__ void glds16(const void* g, u32 lds_base) {
    u32 sv;
    asm volatile("s_mov_b32 %0, m0\n\ts_mov_b32 m0, %2\n\ts_nop 0\n\tglobal_load_lds_dwordx4 %1, off\n\ts_mov_b32 m0, %0" : "=&s"(sv) : "v"(g), "s"(lds_base) : "memory");
}
__device__ __forceinline__ void glds16s(const void* sbase, u32 voff, u32 lds_base) {
    u32 sv;
    asm volatile("s_mov_b32 %0, m0\n\ts_mov_b32 m0, %3\n\ts_nop 0\n\tglobal_load_lds_dwordx4 %1, %2\n\ts_mov_b32 m0, %0" : "=&s"(sv) : "v"(voff), "s"(sbase), "s"(lds_base) : "memory");
}
__device__ __forceinline__ void glds16s_off1k(const void* sbase, u32 voff, u32 lds_base) {
    u32 sv;
    asm volatile("s_mov_b32 %0, m0\n\ts_mov_b32 m0, %3\n\ts_nop 0\n\tglobal_load_lds_dwordx4 %1, %2\n\tglobal_load_lds_dwordx4 %1, %2 offset:1024\n\ts_mov_b32 m0, %0" : "=&s"(sv) : "v"(voff), "s"(sbase), "s"(lds_base) : "memory");
}
__device__ __forceinline__ void glds16s_off1k_sc1(const void* sbase, u32 voff, u32 lds_base) {
    u32 sv;
    asm volatile("s_mov_b32 %0, m0\n\ts_mov_b32 m0, %3\n\ts_nop 0\n\tglobal_load_lds_dwordx4 %1, %2 sc1\n\tglobal_load_lds_dwordx4 %1, %2 offset:1024 sc1\n\ts_mov_b32 m0, %0" : "=&s"(sv) : "v"(voff), "s"(sbase), "s"(lds_base) : "memory");
}
__device__ __forceinline__ s16x4 trd(lcptr p) {
    typedef short v4i16_t __attribute__((ext_vector_type(4)));
    return __builtin_bit_cast(s16x4, __builtin_amdgcn_ds_read_tr16_b64_v4i16((LAS v4i16_t*)p));
}
__device__ __forceinline__ bf16x8 cat8(s16x4 a, s16x4 b) { return (bf16x8){a[0], a[1], a[2], a[3], b[0], b[1], b[2], b[3]}; }
#define MFMA32(a, b, c) __builtin_amdgcn_mfma_f32_32x32x16_bf16(a, b, c, 0, 0, 0)
#define MFMA16(a, b, c) __builtin_amdgcn_mfma_f32_16x16x32_bf16(a, b, c, 0, 0, 0)
#define SBAR() __builtin_amdgcn_sched_barrier(0)
#define PIN(x) asm volatile("" : "+v"(x))
#define RFL(x) __builtin_amdgcn_readfirstlane(x)

#define STAMP_DECL
#define STAMP(i)
#define STAMP_OUT(buf, n)

typedef unsigned long long u64;
typedef __attribute__((address_space(1))) u64 gu64;
constexpr int FLAG_STRIDE = 16;
constexpr int F_STAT = 0, F_QKV = 1, F_H = 2, F_O = 3, F_PART = 4, F_KINDS = 5;
constexpr size_t CTRL_BYTES = (size_t)(F_KINDS * 256 + 1) * FLAG_STRIDE * 8;
__device__ __forceinline__ gu64* flag_ptr(u64* ctrl, int kind, int wg) { return (gu64*)(ctrl + (size_t)(kind * 256 + wg) * FLAG_STRIDE); }
__device__ __forceinline__ gu64* epoch_ptr(u64* ctrl) { return (gu64*)(ctrl + (size_t)(F_KINDS * 256) * FLAG_STRIDE); }
__device__ __forceinline__ u64 ld_agent(gu64* p) { return __hip_atomic_load(p, __ATOMIC_RELAXED, __HIP_MEMORY_SCOPE_AGENT); }
__device__ __forceinline__ void st_agent(gu64* p, u64 v) { __hip_atomic_store(p, v, __ATOMIC_RELAXED, __HIP_MEMORY_SCOPE_AGENT); }
__device__ __forceinline__ void poll_flags(gu64* mine, bool use, u64 ep) {
    const unsigned long long t0 = __builtin_amdgcn_s_memrealtime();
    for (;;) {
        const u64 v = use ? ld_agent(mine) : ep;
        if (__all(v == ep)) break;
        if (__builtin_amdgcn_s_memrealtime() - t0 > 5000000ull) break;
        __builtin_amdgcn_s_sleep(1);
    }
}
__device__ __forceinline__ void poll_issue(u64& v, gu64* p) { asm volatile("global_load_dwordx2 %0, %1, off sc1" : "=v"(v) : "v"(p) : "memory"); }
__device__ __forceinline__ void poll_check(u64& v, gu64* p, bool use, u64 ep) { asm volatile("" : "+v"(v)); if (!__all(!use || v == ep)) poll_flags(p, use, ep); }
__device__ __forceinline__ void st16_sc1(u32x4 v, __amdgpu_buffer_rsrc_t rs, u32 voff) { __builtin_amdgcn_raw_buffer_store_b128(v, rs, voff, 0, 16); }
__device__ __forceinline__ u32x4 ld16_sc1(__amdgpu_buffer_rsrc_t rs, u32 voff) { return __builtin_amdgcn_raw_buffer_load_b128(rs, voff, 0, 16); }
__device__ __forceinline__ __amdgpu_buffer_rsrc_t mkrsrc(const void* p, u32 bytes) { return __builtin_amdgcn_make_buffer_rsrc((void*)p, 0, bytes, 0x00020000); }
#define INV_AGENT() asm volatile("buffer_inv sc1" ::: "memory")
#define WAIT_VM0() asm volatile("s_waitcnt vmcnt(0)" ::: "memory")
#define BARRIER() asm volatile("s_waitcnt lgkmcnt(0)\n\ts_barrier" ::: "memory")
struct Args {
    const u16* x; const u16* wqkv; const u16* wo; const u16* w1; const u16* b1; const u16* w2; const u16* b2; const u16* gw;
    u64* ctrl; u32x4* rowrec; u32x4* wext; char* qkvb; char* hb; char* ob; char* part; u16* out; unsigned long long* stamps;
};
constexpr int P1_SLOT = 24576, P1_RING = 6 * P1_SLOT, P1_WEXT = P1_RING, P1_ROW = P1_WEXT + 288 * 16 + 64  , P1_CS = P1_ROW + 128 * 32  , P1_LDS = P1_CS + 1024;
constexpr float GC0 = -2.f * 0.7978845608028654f * 1.4426950408889634f, GC1 = GC0 * 0.044715f;
__device__ __forceinline__ u32 bfbits(float f) { return cvtpk(f, 0.f) & 0xffffu; }
constexpr float QSCALE = 0.23570226039551584f * 1.4426950408889634f;
#define P1_WAITBAR(N) asm volatile("s_waitcnt vmcnt(" #N ") lgkmcnt(0)\n\ts_barrier" ::: "memory")

struct P1Ctx {
    const char* w1; __amdgpu_buffer_rsrc_t hrs;
    int c, nch, g1first; u32 l16;
    const char* lb; const char* la;
    u32 ld, ldst0;
    u32 voffW[3];
    lcptr trbase, wexb; u32 wexs; u32 rs;
    lcptr xeb, rcb;
};
constexpr u32 P1_PART = 192u * F_ * 2u;
__device__ __forceinline__ int p1_w1g(const P1Ctx& X, int i) { return i < 6 ? X.g1first + 2 * (i - 2) : ((i == 6 && X.nch == 7) ? ((X.c & 1) ? 9 * X.c : 9 * X.c + 8) : X.g1first); }
__device__ __forceinline__ void p1_piece_a(const P1Ctx& X, int k) { asm volatile("s_add_u32 m0, %0, %1" :: "s"(X.ld), "n"(k * 8192) : "memory", "scc"); }
__device__ __forceinline__ void p1_piece_b(const P1Ctx& X, int k) {
    asm volatile("global_load_lds_dwordx4 %0, %1" :: "v"(X.voffW[k]), "s"(X.la) : "memory");
}
__device__ __forceinline__ void p1_piece(const P1Ctx& X, int k) { p1_piece_a(X, k); asm volatile("s_nop 0"); p1_piece_b(X, k); }
__device__ __forceinline__ void p1_top(P1Ctx& X, int i, int j) {
    if (j == 0) asm volatile("s_cmp_le_u32 %0, 2\n\ts_cbranch_scc1 3f\n\ts_waitcnt vmcnt(12) lgkmcnt(0)\n\ts_barrier\n3:" :: "s"(i) : "memory", "scc");
    else asm volatile("s_waitcnt vmcnt(12) lgkmcnt(0)\n\ts_barrier" ::: "memory");
    X.ld = X.ldst0 + X.rs;
    X.rs = X.rs == 5 * P1_SLOT ? 0 : X.rs + P1_SLOT;
    if (j == 0) X.la = X.lb + 2 * P1_PART;
    if (j == 1) { X.lb = X.w1 + p1_w1g(X, i + 2) * 64; X.la = X.lb; }
    if (j == 2) X.la = X.lb + P1_PART;
}
__device__ __forceinline__ void p1_top3(P1Ctx& X) {
    X.lb = X.w1 + p1_w1g(X, 2) * 64;
    X.la = X.lb; X.ld = X.ldst0; p1_piece(X, 0); p1_piece(X, 1); p1_piece(X, 2);
    X.la = X.lb + P1_PART; X.ld = X.ldst0 + P1_SLOT; p1_piece(X, 0); p1_piece(X, 1); p1_piece(X, 2);
    X.rs = 2 * P1_SLOT;
}
struct Epi { float a0, a1, u0, u1, c0, c1, sd; u32 pw[4]; };
__device__ __forceinline__ void p1_gelu_slot(int s, const P1Ctx& X, const f32x16& pa, Epi& E, u32 vo) {
    const int k = s >> 2, ph = s & 3;
    if (ph == 0) { E.a0 = pa[2 * k]; E.a1 = pa[2 * k + 1]; E.u0 = __builtin_fmaf(E.a0 * E.a0, E.c1, E.c0); E.u1 = __builtin_fmaf(E.a1 * E.a1, E.c1, E.c0); PIN(E.u0); PIN(E.u1); }
    if (ph == 1) { E.u0 = __builtin_amdgcn_exp2f(E.a0 * E.u0); E.u1 = __builtin_amdgcn_exp2f(E.a1 * E.u1); PIN(E.u0); PIN(E.u1); }
    if (ph == 2) { E.u0 = __builtin_amdgcn_rcpf(__builtin_fmaf(E.u0, E.sd, E.sd)); E.u1 = __builtin_amdgcn_rcpf(__builtin_fmaf(E.u1, E.sd, E.sd)); PIN(E.u0); PIN(E.u1); }
    if (ph == 3) { E.pw[k & 3] = cvtpk(E.a0 * E.u0, E.a1 * E.u1);
        if ((k & 3) == 3) __builtin_amdgcn_raw_buffer_store_b128((u32x4){E.pw[0], E.pw[1], E.pw[2], E.pw[3]}, X.hrs, vo + (k >> 2) * 1024, 0, 16); }
}
__device__ __forceinline__ void p1_consts(const P1Ctx& X, Epi& E) { const f32x4 k = *(const LAS f32x4*)X.rcb; E.c0 = k[0]; E.c1 = k[1]; E.sd = k[2]; }
__device__ __forceinline__ void p1_w1_chunk(P1Ctx& X, int i, int pg, bool st, const bf16x8 (&xf)[36], bf16x8 (&wf)[6], f32x16& acc, f32x16& pacc) {
    const u32 vo = st ? X.l16 + (u32)pg * 2048 : 0x40000000u;
    Epi E; bf16x8 we, xe;
    lcptr p = X.trbase;
    SBAR();
#pragma unroll
    for (int s = 0; s < 36; ++s) {
        if (s == 0 || s == 12 || s == 24) { p1_top(X, i, s / 12); p = X.trbase + X.rs; PIN(p); }
        if (s < 4) { pacc = MFMA32(wf[(s + 2) % 6], xf[32 + s], pacc); PIN(pacc); }
        if (s == 4) { pacc = MFMA32(we, xe, pacc); PIN(pacc); f32x16 z = {}; acc = MFMA32(wf[0], xf[0], z); PIN(acc); }
        if (s > 4) { acc = MFMA32(wf[(s - 4) % 6], xf[s - 4], acc); PIN(acc); }
        if (s % 4 == 2) p1_piece_a(X, (s % 12) >> 2);
        if (s == 1) { we = *(const LAS bf16x8*)(X.wexb + (st ? pg - 9 * X.c : 0) * X.wexs); xe = *(const LAS bf16x8*)X.xeb; }
        if (s == 3) p1_consts(X, E);
        if ((s & 1) == 0) {
            wf[(s + 1) % 6] = cat8(trd(p + ((s + 1) % 12) * 2048), trd(p + ((s + 1) % 12) * 2048 + 512));
            wf[s % 6] = cat8(trd(p + (s % 12) * 2048), trd(p + (s % 12) * 2048 + 512));
        }
        if (s % 4 == 2) p1_piece_b(X, (s % 12) >> 2);
        if (s >= 6) p1_gelu_slot(s - 6, X, pacc, E, vo);
        SBAR();
    }
    p1_gelu_slot(30, X, pacc, E, vo); p1_gelu_slot(31, X, pacc, E, vo);
}
template <bool SWAP, int J, bool LD>
__device__ __forceinline__ void p1_qkv_stage(P1Ctx& X, lcptr p, const bf16x8 (&xf)[36], f32x16& acc) {
    bf16x8 wf[4];
    PIN(p);
#pragma unroll
    for (int s = 0; s < 12 + 3; ++s) {
        if (s < 12) wf[s & 3] = cat8(trd(p + s * 2048), trd(p + s * 2048 + 512));
        if (s >= 3) { const int u = s - 3;
            if (J == 0 && u == 0) { f32x16 z = {}; acc = SWAP ? MFMA32(xf[0], wf[0], z) : MFMA32(wf[0], xf[0], z); }
            else acc = SWAP ? MFMA32(xf[J * 12 + u], wf[u & 3], acc) : MFMA32(wf[u & 3], xf[J * 12 + u], acc); }
        if (LD && (s == 4 || s == 8 || s == 12)) p1_piece(X, (s >> 2) - 1);
        SBAR();
    }
}
__device__ __forceinline__ void p1_qkv_store(__amdgpu_buffer_rsrc_t rs, u32 off, int lane, const f32x16& a, float sc) {
    u32 w[8];
#pragma unroll
    for (int k = 0; k < 8; ++k) w[k] = cvtpk(a[2 * k] * sc, a[2 * k + 1] * sc);
    st16_sc1((u32x4){w[0], w[1], w[2], w[3]}, rs, off + lane * 16);
    st16_sc1((u32x4){w[4], w[5], w[6], w[7]}, rs, off + 1024 + lane * 16);
}

__device__ __forceinline__ int p1_wg(int rb, int c) { return ((rb >> 3) | ((c >> 2) << 1)) + 8 * ((rb & 7) | ((c & 3) << 3)); }
__device__ __forceinline__ float dpp_ror4(float v) { return __builtin_bit_cast(float, __builtin_amdgcn_update_dpp(0, __builtin_bit_cast(int, v), 0x124, 0xf, 0xf, false)); }
__device__ __forceinline__ float dpp_ror8(float v) { return __builtin_bit_cast(float, __builtin_amdgcn_update_dpp(0, __builtin_bit_cast(int, v), 0x128, 0xf, 0xf, false)); }
__device__ __forceinline__ float xadd16(float v) { auto r = __builtin_amdgcn_permlane16_swap(__float_as_uint(v), __float_as_uint(v), false, false); return __uint_as_float(r[0]) + __uint_as_float(r[1]); }
__device__ __forceinline__ float xadd32(float v) { auto r = __builtin_amdgcn_permlane32_swap(__float_as_uint(v), __float_as_uint(v), false, false); return __uint_as_float(r[0]) + __uint_as_float(r[1]); }
template <int CTRL> __device__ __forceinline__ float dpp_mov(float v) { return __builtin_bit_cast(float, __builtin_amdgcn_update_dpp(0, __builtin_bit_cast(int, v), CTRL, 0xf, 0xf, false)); }
__device__ __forceinline__ float wave_sum(float v) {
    v += dpp_mov<0x121>(v); v += dpp_mov<0x122>(v); v += dpp_mov<0x124>(v); v += dpp_mov<0x128>(v);
    return xadd32(xadd16(v));
}
#define LD16A(dst, voff, sbase) asm volatile("global_load_dwordx4 %0, %1, %2" : "=v"(dst) : "v"(voff), "s"(sbase) : "memory")

__device__ __forceinline__ int p1_qkv_owner(int G) { return G < 24 ? G >> 2 : 6 + (G - 24) / 3; }
__device__ __forceinline__ gu64* attn_flag(const Args& A, int lane) {
    const int xcd = blockIdx.x & 7, idx = blockIdx.x >> 3, qb = idx >> 1, hp = xcd + 8 * (idx & 1), cg0 = (36 * hp) >> 5;
    const int gi = lane / 3, rr = lane - 3 * gi; int rbx = qb - rr; if (rbx < 0) rbx = 0;
    return flag_ptr(A.ctrl, F_QKV, p1_wg(rbx, p1_qkv_owner((lane < 18 ? (gi >> 1) * 18 + cg0 + (gi & 1) : 0))));
}
constexpr int AT_K = 0, AT_V = 12 * 4096, AT_Q = 24 * 4096, AT_LDS = AT_Q + 4 * 4096;
__device__ __forceinline__ void attn_stage(const Args& A, u32 lds0, int wid, int lane) {
    const int xcd = blockIdx.x & 7, idx = blockIdx.x >> 3, qb = idx >> 1, hp = xcd + 8 * (idx & 1), cg0 = (36 * hp) >> 5;
#pragma unroll
    for (int i = 0; i < 7; ++i) {
        const int p = wid + 8 * i;
        const int u = p >> 1, gsel = p & 1;
        const int sec = u < 12 ? 1 : (u < 24 ? 2 : 0), ub = u < 12 ? u : (u < 24 ? u - 12 : u - 24);
        int rb = qb * 4 + ub - (sec ? 8 : 0); if (rb < 0) rb = 0;
        glds16s_off1k_sc1(A.qkvb + (size_t)(rb * 54 + sec * 18 + cg0 + gsel) * 2048, lane * 16, lds0 + u * 4096 + gsel * 2048);
    }
}
__device__ __forceinline__ void p1_phase(const Args& A, char* smem, u64& ep) {
    STAMP_DECL STAMP(0);
    const int tid = threadIdx.x, lane = tid & 63, wid = RFL(tid >> 6);
    const int xcd = blockIdx.x & 7, idx = blockIdx.x >> 3;
    const int rb = (xcd & 1) * 8 + (idx & 7), c = (xcd >> 1) * 4 + (idx >> 3);
    const u32 lds0 = (u32)(uintptr_t)smem;
    const int r8 = lane >> 3, cp = lane & 7;
    const int csw = cp ^ (((r8 >> 1) & 1) << 2);
    int gq0, gq1, gs;
    if (c < 6) { gq0 = 4 * c; gq1 = 4 * c + 2; gs = 0; } else { const int q0 = 24 + 3 * (c - 6), odd = (c - 6) & 1; gq0 = q0 + odd; gq1 = 0; gs = odd ? q0 : q0 + 2; }
    const u32 voffQ = (u32)(wid * 8 + r8) * (NQKV_ * 2) + csw * 16;
    const char* wq0 = (const char*)A.wqkv + gq0 * 64;
    const char* xs = (const char*)A.x + (size_t)rb * 128 * (D_ * 2);
    u32 voffX[2];
#pragma unroll
    for (int i = 0; i < 2; ++i) { const int row = 16 * wid + 8 * i + r8; voffX[i] = (u32)row * (D_ * 2) + ((cp ^ ((row >> 1) & 7)) << 4); }
    const u32 XA = lds0 + P1_SLOT, XB = lds0 + 4 * P1_SLOT;
#define P1_LDQ(sb, j, slot) _Pragma("unroll") for (int k = 0; k < 3; ++k) glds16s((sb) + (size_t)((j) * 192 + k * 64) * (NQKV_ * 2), voffQ, lds0 + (slot) * P1_SLOT + wid * 1024 + k * 8192)
#define P1_LDX(piece, dst) _Pragma("unroll") for (int i = 0; i < 2; ++i) glds16s(xs + (piece) * 128, voffX[i], (dst) + (16 * wid + 8 * i) * 128)
    P1_LDQ(wq0, 0, 0);
    P1_LDX(0, XA); P1_LDX(1, XA + 16384); P1_LDX(2, XA + 32768);
    asm volatile("s_barrier" ::: "memory");
    P1_LDQ(wq0, 1, 3);
    P1_LDX(3, XB); P1_LDX(4, XB + 16384); P1_LDX(5, XB + 32768);
    const int srow = rb * 128 + c * 8 + wid;
    u32x4 sx0, sx1, sg0, sg1, sw[5]; u64 e0; u32 sb1;
    { const u32 o0 = lane * 16, o1 = 1024 + (lane & 7) * 16;
      const char* xr = (const char*)A.x + (size_t)srow * (D_ * 2);
      LD16A(sx0, o0, xr); LD16A(sx1, o1, xr); LD16A(sg0, o0, (const char*)A.gw); LD16A(sg1, o1, (const char*)A.gw);
      const char* wc = (const char*)A.w1 + (size_t)(72 * wid) * (F_ * 2) + (9 * c + (rb < 9 ? rb : 0)) * 64;
#pragma unroll
      for (int i = 0; i < 5; ++i) { int r = (lane >> 2) + 16 * i; if (r > 71) r = 71; const u32 o = (u32)r * (F_ * 2) + (lane & 3) * 16; LD16A(sw[i], o, wc); }
      asm volatile("global_load_ushort %0, %1, %2" : "=v"(sb1) : "v"((u32)(lane & 31) * 2), "s"((const char*)A.b1 + (9 * c + (rb < 9 ? rb : 0)) * 64) : "memory");
      asm volatile("global_load_dwordx2 %0, %1, %2 sc1" : "=v"(e0) : "v"(0u), "s"(epoch_ptr(A.ctrl)) : "memory"); }
    if (tid < 4) *(LAS u32x4*)((lptr)(size_t)lds0 + P1_WEXT + 288 * 16 + tid * 16) = (u32x4){0u, 0u, 0u, 0u};
    const int rg = wid & 3, ch = wid >> 2;
    P1Ctx X; X.w1 = (const char*)A.w1; X.hrs = mkrsrc(A.hb + (size_t)(rb * 4 + rg) * 144 * 2048, 144 * 2048); X.c = c; X.l16 = lane * 16;
    const int nch = c < 6 ? 7 : 6; X.nch = nch; X.g1first = 9 * c + (c & 1);
    const int m = lane & 31, h = lane >> 5;
#pragma unroll
    for (int k = 0; k < 3; ++k) X.voffW[k] = (u32)(wid * 8 + r8 + 64 * k) * (F_ * 2) + csw * 16;
    X.ldst0 = lds0 + wid * 1024; X.rs = 0; X.ld = X.ldst0; X.lb = X.w1; X.la = X.w1;
    { const int s16 = (lane >> 4) & 1, q = (lane & 15) >> 2, p = lane & 3;
      X.trbase = (lcptr)(size_t)lds0 + (8 * h + q) * 128 + (((4 * ch + 2 * s16 + (p >> 1)) ^ (((q >> 1) & 1) << 2)) * 16) + (p & 1) * 8;
      X.wexb = (lcptr)(size_t)lds0 + (h ? P1_WEXT + 288 * 16 : P1_WEXT + m * 16); X.wexs = h ? 0 : 512; }
    const int wsingle = (c & 1) ? 9 * c : 9 * c + 8;
    bf16x8 xf[36];
    f32x16 accA, accB;
    const bool swp = c >= 10;
    const int xrow = rg * 32 + m, xz = (xrow >> 1) & 7;
    X.rcb = (lcptr)(size_t)lds0 + P1_ROW + xrow * 32 + 16;
    X.xeb = h ? (lcptr)(size_t)lds0 + P1_WEXT + 288 * 16 : X.rcb - 16;
    lcptr xa = (lcptr)(size_t)XA + xrow * 128, xb = (lcptr)(size_t)XB + xrow * 128;
#define P1_XF(s0, base) _Pragma("unroll") for (int s = 0; s < 12; ++s) xf[s0 + s] = *(const LAS bf16x8*)((base) + (s >> 2) * 16384 + (((2 * (s & 3) + h) ^ xz) << 4))
#define P1_QS(J, LD, P) do { if (swp) p1_qkv_stage<true, J, LD>(X, P, xf, accA); else p1_qkv_stage<false, J, LD>(X, P, xf, accA); } while (0)
    STAMP(1);
    P1_WAITBAR(20);
    STAMP(2);
    P1_XF(0, xa);
    P1_WAITBAR(20);
    P1_LDX(6, XA); P1_LDX(7, XA + 16384); P1_LDX(8, XA + 32768);
    P1_QS(0, false, X.trbase);
    STAMP(3);
    P1_WAITBAR(6);
    STAMP(4);
    P1_LDQ(wq0, 2, 0);
    P1_XF(12, xb);
    P1_QS(1, false, X.trbase + 3 * P1_SLOT);
    asm volatile("" : "+v"(sx0), "+v"(sx1), "+v"(sg0), "+v"(sg1), "+v"(sw[0]), "+v"(sw[1]), "+v"(sw[2]), "+v"(sw[3]), "+v"(sw[4]), "+v"(e0), "+v"(sb1) :: "memory");
    ep = ((u64)(u32)RFL((u32)(e0 >> 32)) << 32 | (u32)RFL((u32)e0)) + 1;
    {
        typedef __bf16 bf2 __attribute__((ext_vector_type(2)));
        const bool two = lane < 8;
        const bf2 one2 = __builtin_bit_cast(bf2, 0x3f803f80u);
        float s = 0.f, gsum = 0.f, q2 = 0.f;
#pragma unroll
        for (int j = 0; j < 8; ++j) { const u32 xw = j < 4 ? sx0[j] : (two ? sx1[j - 4] : 0u), gw = j < 4 ? sg0[j] : sg1[j - 4]; const bf2 xv = __builtin_bit_cast(bf2, xw);
            s = __builtin_amdgcn_fdot2_f32_bf16(xv, one2, s, false); gsum = __builtin_amdgcn_fdot2_f32_bf16(xv, __builtin_bit_cast(bf2, gw), gsum, false); q2 = __builtin_amdgcn_fdot2_f32_bf16(xv, xv, q2, false); }
        s = wave_sum(s); gsum = wave_sum(gsum); q2 = wave_sum(q2);
        const float m = s * (1.f / 576.f);
        const float q = fmaxf(q2 - 576.f * m * m, 0.f);
        const float sd = __builtin_amdgcn_sqrtf(q * (1.f / 576.f) + 1e-5f), r = __builtin_amdgcn_rcpf(sd);
        const float eg = __builtin_amdgcn_exp2f(-1.4426950408889634f * gsum), gate = __builtin_amdgcn_rcpf(1.f + eg);
        const float sdg = sd * (1.f + __builtin_amdgcn_exp2f(1.4426950408889634f * gsum));
        const u32 mh = bfbits(-m), ml = bfbits(-m - __uint_as_float(mh << 16)), sh = bfbits(sd), sl = bfbits(sd - __uint_as_float(sh << 16));
        const u32x4 rec = lane == 0 ? (u32x4){mh | (mh << 16), ml | (sh << 16), sl, 0u} : (u32x4){__float_as_uint(GC0 * r), __float_as_uint(GC1 * r * r * r), __float_as_uint(sdg), __float_as_uint(gate)};
        if (lane < 2) st16_sc1(rec, mkrsrc(A.rowrec, S_ * 32), srow * 32 + lane * 16);
        if (rb < 9) {
        float cs[8];
#pragma unroll
        for (int j = 0; j < 8; ++j) cs[j] = 0.f;
#pragma unroll
        for (int i = 0; i < 5; ++i)
#pragma unroll
            for (int j = 0; j < 4; ++j) { const u32 wv = (i < 4 || lane < 32) ? sw[i][j] : 0u; cs[2 * j] += bflo(wv); cs[2 * j + 1] += bfhi(wv); }
#pragma unroll
        for (int j = 0; j < 8; ++j) { float v = cs[j]; v += dpp_ror4(v); v += dpp_ror8(v); cs[j] = xadd32(xadd16(v)); }
        if (lane < 4) { LAS f32x4* d = (LAS f32x4*)((lptr)(size_t)lds0 + P1_CS + wid * 128 + lane * 32); d[0] = (f32x4){cs[0], cs[1], cs[2], cs[3]}; d[1] = (f32x4){cs[4], cs[5], cs[6], cs[7]}; }
        }
    }
    STAMP(5);
    P1_WAITBAR(3);
    STAMP(6);
    if (wid == 0 && lane < 32 && rb < 9) {
        float t = 0.f;
#pragma unroll
        for (int w = 0; w < 8; ++w) t += *(const LAS float*)((lcptr)(size_t)lds0 + P1_CS + w * 128 + lane * 4);
        const int col = (9 * c + rb) * 32 + lane;
        const u32 chh = bfbits(t), cl = bfbits(t - __uint_as_float(chh << 16)), bb = sb1;
        st16_sc1((u32x4){chh | (cl << 16), chh | (bb << 16), bb, 0u}, mkrsrc(A.wext, F_ * 16), col * 16);
    }
    if (c < 6) { const char* wq1 = (const char*)A.wqkv + gq1 * 64; P1_LDQ(wq1, 0, 3); P1_LDQ(wq1, 1, 4); P1_LDQ(wq1, 2, 5); }
    else {
        const bool hi = csw >= 4; const u32 pitch = hi ? F_ * 2 : NQKV_ * 2;
        const char* lp = (hi ? (const char*)A.w1 + wsingle * 64 : (const char*)A.wqkv + gs * 64) + (size_t)((u32)(wid * 8 + r8) * pitch) + (csw & 3) * 16;
#pragma unroll
        for (int k = 0; k < 9; ++k) { glds16(lp, lds0 + 3 * P1_SLOT + wid * 1024 + k * 8192); lp += pitch * 64; }
    }
    P1_XF(24, xa);
    P1_WAITBAR(9);
    P1_QS(2, false, X.trbase);
    const __amdgpu_buffer_rsrc_t qrs = mkrsrc(A.qkvb + (size_t)(rb * 4 + rg) * 54 * 2048, 54 * 2048);
    p1_qkv_store(qrs, (gq0 + ch) * 2048, lane, accA, gq0 + ch < 18 ? QSCALE : 1.f);
    STAMP(7);
    P1_WAITBAR(0);
    if (tid == 0) st_agent(flag_ptr(A.ctrl, F_STAT, blockIdx.x), ep);
    p1_top3(X);
    {
        const char* w2s = (const char*)A.w2 + (size_t)(576 * xcd) * 1152 + (size_t)idx * 20736;
        const int l = wid * 64 + lane;
        u64 pc; asm volatile("s_getpc_b64 %0" : "=s"(pc));
        asm volatile("s_mov_b32 m0, %0" :: "s"(lds0 + P1_CS) : "memory");
        if (l < 162) asm volatile("s_nop 0\n\tglobal_load_lds_dword %0, %1" :: "v"((u32)l * 128), "s"(w2s) : "memory");
        if (l < 21) asm volatile("s_nop 0\n\tglobal_load_lds_dword %0, %1" :: "v"((u32)l * 128), "s"((const char*)A.wo + (size_t)blockIdx.x * 2592) : "memory");
        if (wid == 7 && lane < 8) asm volatile("s_nop 0\n\tglobal_load_lds_dword %0, %1" :: "v"((u32)(idx * 8 + lane) * 128), "s"((const char*)(pc & ~(u64)127)) : "memory");
    }
    const bool c1q = c < 6 || ch == 0;
    bf16x8 wf[6];
#pragma unroll
    for (int k = 0; k < 6; ++k) wf[k] = xf[k];
    if (c1q) {
        p1_top(X, 1, 0); P1_QS(0, true, X.trbase + X.rs);
        p1_top(X, 1, 1); P1_QS(1, true, X.trbase + X.rs);
        p1_top(X, 1, 2); P1_QS(2, true, X.trbase + X.rs);
        const int g = c < 6 ? gq1 + ch : gs;
        p1_qkv_store(qrs, g * 2048, lane, accA, g < 18 ? QSCALE : 1.f);
    } else p1_w1_chunk(X, 1, 0, false, xf, wf, accB, accA);
    STAMP(8);
    { const int k = lane < 16 ? p1_wg(rb, lane) : p1_wg(lane - 16, c);
      poll_flags(flag_ptr(A.ctrl, F_STAT, k), lane < 25, ep);
      u32x4 t0 = {}, t1 = {};
      if (tid < 256) t0 = ld16_sc1(mkrsrc(A.rowrec, S_ * 32), (rb * 256 + tid) * 16);
      if (tid < 288) t1 = ld16_sc1(mkrsrc(A.wext, F_ * 16), (288 * c + tid) * 16);
      if (tid < 256) *(LAS u32x4*)((lptr)(size_t)lds0 + P1_ROW + tid * 16) = t0;
      if (tid < 288) *(LAS u32x4*)((lptr)(size_t)lds0 + P1_WEXT + tid * 16) = t1; }
    P1_WAITBAR(0);
    if (tid == 0) st_agent(flag_ptr(A.ctrl, F_QKV, blockIdx.x), ep);
    const int iend = (nch == 7 && ch == 1) ? 6 : nch;
#define P1_G(i) ((i) == 1 ? wsingle : p1_w1g(X, i) + ((i) < 6 ? ch : 0))
#define P1_ST1
#define P1_ST2
    u64 fq = 0;
    for (int i = 2; i < iend; i += 2) {
        p1_w1_chunk(X, i, P1_G(i - 1), i > 2 || !c1q, xf, wf, accA, accB); P1_ST1
        if (wid == 0) { if (i == 2) poll_issue(fq, attn_flag(A, lane)); else if (i == 4) { poll_check(fq, attn_flag(A, lane), lane < 18, ep); } }
        if (i + 1 < iend) p1_w1_chunk(X, i + 1, P1_G(i), true, xf, wf, accB, accA); P1_ST2
    }
    if (iend < nch) { P1_WAITBAR(12); P1_WAITBAR(9); P1_WAITBAR(6); }
    BARRIER();
    attn_stage(A, lds0, wid, lane);
    int ie = iend; asm volatile("" : "+s"(ie));
    if ((ie & 1) != 0) accB = accA;
    { Epi E; const int lg = P1_G(ie - 1); const u32 vo = X.l16 + (u32)lg * 2048;
      const bf16x8 we = *(const LAS bf16x8*)(X.wexb + (lg - 9 * X.c) * X.wexs), xe = *(const LAS bf16x8*)X.xeb;
      p1_consts(X, E);
#pragma unroll
      for (int s = 0; s < 4; ++s) accB = MFMA32(wf[(s + 2) % 6], xf[32 + s], accB);
      accB = MFMA32(we, xe, accB);
#pragma unroll
      for (int s = 0; s < 32; ++s) p1_gelu_slot(s, X, accB, E, vo); }
    STAMP(15);
    STAMP_OUT(A.stamps, 16);
}
__device__ __forceinline__ int qk_off(int c) { const int cc = c & 31; return (c >> 5) * 2048 + (2 * (cc >> 4) + ((cc >> 2) & 1)) * 512 + (((cc >> 3) & 1) * 4 + (cc & 3)) * 2; }
__device__ __forceinline__ float xmax16(float v) { auto r = __builtin_amdgcn_permlane16_swap(__float_as_uint(v), __float_as_uint(v), false, false); return fmaxf(__uint_as_float(r[0]), __uint_as_float(r[1])); }
__device__ __forceinline__ float xmax32(float v) { auto r = __builtin_amdgcn_permlane32_swap(__float_as_uint(v), __float_as_uint(v), false, false); return fmaxf(__uint_as_float(r[0]), __uint_as_float(r[1])); }
__device__ __forceinline__ float max3f(float a, float b, float c) { return __builtin_fmaxf(__builtin_fmaxf(a, b), c); }

template <bool EARLY>
__device__ __forceinline__ void attn_mask(f32x4& s, int t, int dq, int kb) {
    if (EARLY) {
#pragma unroll
        for (int r = 0; r < 4; ++r) { const int d = dq - 16 * t - r; const bool ok = (u32)d <= 255u && (kb + 16 * t + r) >= 0; s[r] = ok ? s[r] : -INFINITY; }
    } else if (t < 2) {
#pragma unroll
        for (int r = 0; r < 4; ++r) s[r] = (dq - 16 * t - r <= 255) ? s[r] : -INFINITY;
    } else if (t >= 16) {
#pragma unroll
        for (int r = 0; r < 4; ++r) s[r] = (dq - 16 * t - r >= 0) ? s[r] : -INFINITY;
    }
}
#define ATS_ARG
#define ATS_PASS
template <bool EARLY>
__device__ __forceinline__ void attn_units(lcptr L, int c00  , int cones, int w, int lane, int Q0, float gate, __amdgpu_buffer_rsrc_t ors, int head0, gu64* fhp, u64 ep ATS_ARG) {
    u64 fh = 0; if (w == 0) poll_issue(fh, fhp);
    const int i16 = lane & 15, g = lane >> 4;
    const int lb0 = w >> 1;
    const int q = Q0 + 16 * w + i16, kb = Q0 - 256 + lb0 * 32 + 4 * g, dq = q - kb;
    bf16x8 kf[2][18], qf[2];
    f32x4 nm[2];
#pragma unroll
    for (int u = 0; u < 2; ++u) {
        const int c0 = c00 + 18 * u;
        const int p2 = c0 & 2, op = qk_off(c0 + p2 + 4 * g), os = qk_off(p2 ? c0 - 2 : c0 + 16);
        lcptr qp = L + AT_Q + (w >> 1) * 4096 + ((w & 1) * 16 + i16) * 16;
        const u32x2 qa = *(const LAS u32x2*)(qp + op), qc = *(const LAS u32x2*)(qp + os);
        qf[u] = __builtin_bit_cast(bf16x8, (u32x4){qa[0], qa[1], (g == 0 && !p2) ? qc[0] : 0u, (g == 0 && p2) ? qc[1] : 0u});
        lcptr kb0 = L + AT_K + lb0 * 4096 + i16 * 16;
        lcptr k0 = kb0 + op, k1 = kb0 + os;
        PIN(k0); PIN(k1);
#pragma unroll
        for (int t = 0; t < 18; ++t) { const int o = (t >> 1) * 4096 + (t & 1) * 256;
            const u32x2 ka = *(const volatile LAS u32x2*)(k0 + o), kc = *(const volatile LAS u32x2*)(k1 + o);
            kf[u][t] = __builtin_bit_cast(bf16x8, (u32x4){ka[0], ka[1], kc[0], kc[1]}); }
    }
    lcptr va[2], vb[2];
#pragma unroll
    for (int u = 0; u < 2; ++u) {
        const int c0 = c00 + 18 * u, ca = c0 + i16, cb = (i16 < 2) ? c0 + 16 + i16 : (i16 == 2 ? cones : c0);
        va[u] = L + AT_V + lb0 * 4096 + (ca >> 5) * 2048 + (g & 1) * 512 + (ca & 31) * 16 + (g >> 1) * 8;
        vb[u] = L + AT_V + lb0 * 4096 + (cb >> 5) * 2048 + (g & 1) * 512 + (cb & 31) * 16 + (g >> 1) * 8;
        PIN(va[u]); PIN(vb[u]);
    }
    SBAR();
    STAMP(5);
#pragma unroll
    for (int u = 0; u < 2; ++u) {
        f32x4 s[18];
#pragma unroll
        for (int t = 0; t < 18; ++t) s[t] = MFMA16(kf[u][t], qf[u], ((f32x4){0.f, 0.f, 0.f, 0.f}));
#pragma unroll
        for (int t = 0; t < 18; ++t) attn_mask<EARLY>(s[t], t, dq, kb);
        float m0 = max3f(s[0][0], s[0][1], s[0][2]), m1 = max3f(s[0][3], s[1][0], s[1][1]);
        m0 = max3f(m0, s[1][2], s[1][3]);
#pragma unroll
        for (int t = 2; t < 18; t += 2) { m0 = max3f(m0, s[t][0], s[t][1]); m1 = max3f(m1, s[t][2], s[t][3]); m0 = max3f(m0, s[t + 1][0], s[t + 1][1]); m1 = max3f(m1, s[t + 1][2], s[t + 1][3]); }
        const float m = -xmax32(xmax16(fmaxf(m0, m1)));
        nm[u] = (f32x4){m, m, m, m};
    }
    if (w == 0) { WAIT_VM0(); poll_check(fh, fhp, lane < 4, ep); }
    STAMP(6);
    u32x2 vf[3][4]; f32x4 sc2[2][2];
#define AT_VRD(k) do { const int b_ = (k) >> 1, u_ = (k) & 1; vf[(k) % 3][0] = *(const volatile LAS u32x2*)(va[u_] + b_ * 4096); vf[(k) % 3][1] = *(const volatile LAS u32x2*)(va[u_] + b_ * 4096 + 1024); \
        vf[(k) % 3][2] = *(const volatile LAS u32x2*)(vb[u_] + b_ * 4096); vf[(k) % 3][3] = *(const volatile LAS u32x2*)(vb[u_] + b_ * 4096 + 1024); } while (0)
#define AT_QK(k) do { const int b_ = (k) >> 1, u_ = (k) & 1; sc2[(k) & 1][0] = MFMA16(kf[u_][2 * b_], qf[u_], nm[u_]); sc2[(k) & 1][1] = MFMA16(kf[u_][2 * b_ + 1], qf[u_], nm[u_]); } while (0)
    f32x4 oa[2] = {{0.f, 0.f, 0.f, 0.f}, {0.f, 0.f, 0.f, 0.f}}, obb[2] = {{0.f, 0.f, 0.f, 0.f}, {0.f, 0.f, 0.f, 0.f}};
    AT_VRD(0); AT_VRD(1); AT_QK(0);
    SBAR();
#pragma unroll
    for (int k = 0; k < 18; ++k) {
        const int b = k >> 1, u = k & 1;
        f32x4 s0 = sc2[k & 1][0], s1 = sc2[k & 1][1];
        if (k + 1 < 18) { const int b_ = (k + 1) >> 1, u_ = (k + 1) & 1; sc2[(k + 1) & 1][0] = MFMA16(kf[u_][2 * b_], qf[u_], nm[u_]); sc2[(k + 1) & 1][1] = MFMA16(kf[u_][2 * b_ + 1], qf[u_], nm[u_]); PIN(sc2[(k + 1) & 1][0]); PIN(sc2[(k + 1) & 1][1]); }
        if (k + 2 < 18) AT_VRD(k + 2);
        attn_mask<EARLY>(s0, 2 * b, dq, kb); attn_mask<EARLY>(s1, 2 * b + 1, dq, kb);
        u32x4 pw;
        pw[0] = cvtpk_c(__builtin_amdgcn_exp2f(s0[0]), __builtin_amdgcn_exp2f(s0[1])); pw[1] = cvtpk_c(__builtin_amdgcn_exp2f(s0[2]), __builtin_amdgcn_exp2f(s0[3]));
        pw[2] = cvtpk_c(__builtin_amdgcn_exp2f(s1[0]), __builtin_amdgcn_exp2f(s1[1])); pw[3] = cvtpk_c(__builtin_amdgcn_exp2f(s1[2]), __builtin_amdgcn_exp2f(s1[3]));
        const bf16x8 pf = __builtin_bit_cast(bf16x8, pw);
        oa[u] = MFMA16(__builtin_bit_cast(bf16x8, ((u32x4){vf[k % 3][0][0], vf[k % 3][0][1], vf[k % 3][1][0], vf[k % 3][1][1]})), pf, oa[u]);
        obb[u] = MFMA16(__builtin_bit_cast(bf16x8, ((u32x4){vf[k % 3][2][0], vf[k % 3][2][1], vf[k % 3][3][0], vf[k % 3][3][1]})), pf, obb[u]);
        PIN(oa[u]); PIN(obb[u]);
        SBAR();
    }
    STAMP(7);
    auto off = [](int c) { const int cc = c & 31; return (c >> 5) * 2048 + (2 * (cc >> 4) + ((cc >> 2) & 1)) * 512 + (((cc >> 3) & 1) * 4 + (cc & 3)) * 2; };
    const u32 base = (u32)(q >> 5) * 18 * 2048 + (q & 31) * 16;
    const float sc0 = gate * __builtin_amdgcn_rcpf(__shfl(obb[0][2], i16)), sc1 = gate * __builtin_amdgcn_rcpf(__shfl(obb[1][2], i16));
    const u32 p1 = cvtpk(oa[1][0] * sc1, oa[1][1] * sc1), r1 = cvtpk(oa[1][2] * sc1, oa[1][3] * sc1), t1 = cvtpk(obb[1][0] * sc1, obb[1][1] * sc1);
    const u32 nx = (u32)__shfl((int)(g == 0 ? t1 : p1), (lane + 16) & 63);
    const int col0 = 18 * head0;
    typedef u32 u32x2s __attribute__((ext_vector_type(2)));
    __builtin_amdgcn_raw_buffer_store_b64((u32x2s){cvtpk(oa[0][0] * sc0, oa[0][1] * sc0), cvtpk(oa[0][2] * sc0, oa[0][3] * sc0)}, ors, base + off(col0 + 4 * g), 0, 16);
    __builtin_amdgcn_raw_buffer_store_b64((u32x2s){r1, nx}, ors, base + off(col0 + 20 + 4 * g), 0, 16);
    if (g == 0) __builtin_amdgcn_raw_buffer_store_b64((u32x2s){cvtpk(obb[0][0] * sc0, obb[0][1] * sc0), p1}, ors, base + off(col0 + 16), 0, 16);
}

__device__ __forceinline__ int attn_wg(int qb, int hp) { return (hp & 7) + 8 * (2 * qb + (hp >> 3)); }
__device__ __forceinline__ void acquire_agent() { __builtin_amdgcn_fence(__ATOMIC_ACQUIRE, "agent"); WAIT_VM0(); }

__device__ __forceinline__ void attn_phase(const Args& A, char* smem, u64 ep) {
    STAMP_DECL STAMP(0);
    const int tid = threadIdx.x, lane = tid & 63, wid = RFL(tid >> 6);
    const int xcd = blockIdx.x & 7, idx = blockIdx.x >> 3;
    const int qb = idx >> 1, hp = xcd + 8 * (idx & 1);
    const int Q0 = qb * 128, cg0 = (36 * hp) >> 5, c0 = 36 * hp - 32 * cg0;
    const u32 lds0 = (u32)(uintptr_t)smem;
    gu64* fhp;
    { const int sp = blockIdx.x & 7, mt = (blockIdx.x >> 3) & 7; fhp = flag_ptr(A.ctrl, F_H, p1_wg(2 * mt + (lane & 1), 2 * sp + ((lane >> 1) & 1))); }
    const int q = Q0 + 16 * wid + (lane & 15);
    const float gate = __uint_as_float(__builtin_amdgcn_raw_buffer_load_b32(mkrsrc(A.rowrec, S_ * 32), q * 32 + 28, 0, 16));
    WAIT_VM0(); BARRIER();
    if (tid == 0) st_agent(flag_ptr(A.ctrl, F_H, blockIdx.x), ep);
    STAMP(2);
    const int cones = (c0 + 36) & 63;
    if (tid < 48) { const int kb = tid >> 2, pc = tid & 3; *(LAS u32x4*)((lptr)(size_t)lds0 + AT_V + kb * 4096 + (cones >> 5) * 2048 + pc * 512 + (cones & 31) * 16) = (u32x4){0x3f803f80u, 0x3f803f80u, 0x3f803f80u, 0x3f803f80u}; }
    BARRIER();
    STAMP(3);
    const __amdgpu_buffer_rsrc_t ors = mkrsrc(A.ob, S_ * D_ * 2);
    if (qb < 2) attn_units<true>((lcptr)(size_t)lds0, c0, cones, wid, lane, Q0, gate, ors, 2 * hp, fhp, ep ATS_PASS); else attn_units<false>((lcptr)(size_t)lds0, c0, cones, wid, lane, Q0, gate, ors, 2 * hp, fhp, ep ATS_PASS);
    STAMP(8);
    BARRIER();
    STAMP(4);
    STAMP_OUT(A.stamps + (2u << 17), 9);
}
constexpr int P3_A = 32768, P3_B = 64 * 288, P3_SLOT = P3_A + P3_B, P3_LDS = 3 * P3_SLOT;
struct P3Ctx {
    const char* pA; const char* pB;
    const char* oA; const char* oB;
    u32 aStride;
    int tl, nst; u32 sl;
    u32 dA, dB;
    u32 vA, vB0, vB1, vB2;
    lcptr ab, bb;
};
__device__ __forceinline__ void glds_c2(int tl, int nst, const void* sbase, u32 voff, u32 lds_base) {
    u32 sv;
    asm volatile("s_cmp_ge_i32 %4, %5\n\ts_cbranch_scc1 1f\n\ts_mov_b32 %0, m0\n\ts_mov_b32 m0, %3\n\ts_nop 0\n\tglobal_load_lds_dwordx4 %1, %2 sc1\n\tglobal_load_lds_dwordx4 %1, %2 offset:1024 sc1\n\ts_mov_b32 m0, %0\n1:"
                 : "=&s"(sv) : "v"(voff), "s"(sbase), "s"(lds_base), "s"(tl), "s"(nst) : "memory", "scc");
}
__device__ __forceinline__ void glds_c1(int tl, int nst, const void* sbase, u32 voff, u32 lds_base) {
    u32 sv;
    asm volatile("s_cmp_ge_i32 %4, %5\n\ts_cbranch_scc1 1f\n\ts_mov_b32 %0, m0\n\ts_mov_b32 m0, %3\n\ts_nop 0\n\tglobal_load_lds_dwordx4 %1, %2\n\ts_mov_b32 m0, %0\n1:"
                 : "=&s"(sv) : "v"(voff), "s"(sbase), "s"(lds_base), "s"(tl), "s"(nst) : "memory", "scc");
}
__device__ __forceinline__ void glds_cq(int tl, int nst, const void* sbase, u32 voff, u32 lds_base) {
    u32 sv;
    asm volatile("s_cmp_ge_i32 %4, %5\n\ts_cbranch_scc1 1f\n\ts_mov_b32 %0, m0\n\ts_mov_b32 m0, %3\n\ts_mov_b64 exec, 0xffff\n\tglobal_load_lds_dwordx4 %1, %2\n\ts_mov_b64 exec, -1\n\ts_mov_b32 m0, %0\n1:"
                 : "=&s"(sv) : "v"(voff), "s"(sbase), "s"(lds_base), "s"(tl), "s"(nst) : "memory", "scc");
}
__device__ __forceinline__ void p3_load_part(P3Ctx& X, int k) {
    if (k == 0) glds_c2(X.tl, X.nst, X.pA, X.vA, X.dA + X.sl);
    if (k == 1) glds_c2(X.tl, X.nst, X.pA + 2048, X.vA, X.dA + X.sl + 2048);
    if (k == 2) glds_c1(X.tl, X.nst, X.pB, X.vB0, X.dB + X.sl);
    if (k == 3) glds_c1(X.tl, X.nst, X.pB, X.vB1, X.dB + X.sl + 1024);
    if (k == 4) { glds_cq(X.tl, X.nst, X.pB, X.vB2, X.dB + X.sl + 2048);
        X.tl += 1; const bool sw = X.tl == 9; X.pA = sw ? X.oA : X.pA + 4096; X.pB = sw ? X.oB : X.pB + 64 * 1152; X.sl = X.sl == 2 * P3_SLOT ? 0 : X.sl + P3_SLOT; }
}
struct P3Frag { bf16x8 h[4]; bf16x8 w[5]; };
__device__ __forceinline__ void p3_waitbar(int rem) {
    asm volatile("s_cmp_lt_i32 %0, 1\n\ts_cbranch_scc1 2f\n1:\ts_waitcnt vmcnt(7) lgkmcnt(0)\n\ts_barrier\n\t.subsection 1\n2:\ts_waitcnt vmcnt(0)\n\ts_branch 1b\n\t.subsection 0" :: "s"(rem) : "memory", "scc");
}
template <int NT, int HF, bool RD, bool MM>
__device__ __forceinline__ void p3_half(P3Ctx& X, int t, u32 so, P3Frag& R, const P3Frag& P, f32x4 (&acc)[4][5]) {
    lcptr a = X.ab + so + HF * 2048, b = X.bb + so + HF * (32 * 288);
    if (RD) { if (HF == 0) p3_waitbar(X.nst - 1 - t); PIN(a); PIN(b); }
    s16x4 lo;
    SBAR();
#pragma unroll
    for (int i = 0; i < 4 * NT; ++i) {
        if (MM) acc[i & 3][i >> 2] = MFMA16(P.w[i >> 2], P.h[i & 3], acc[i & 3][i >> 2]);
        if (RD) {
            if (i < 4) R.h[i] = *(const LAS bf16x8*)(a + (i >> 1) * 4096 + (i & 1) * 256);
            else if (i < 4 + 2 * NT) { const int j = i - 4; if (j & 1) R.w[j >> 1] = cat8(lo, trd(b + (j >> 1) * 32 + 8 * 288)); else lo = trd(b + (j >> 1) * 32); }
            if (HF == 0) { if (i == (NT == 5 ? 2 : 6)) p3_load_part(X, 0); if (i == (NT == 5 ? 10 : 13)) p3_load_part(X, 1); }
            else { if (i == (NT == 5 ? 1 : 4)) p3_load_part(X, 2); if (i == (NT == 5 ? 8 : 10)) p3_load_part(X, 3); if (i == (NT == 5 ? 15 : 14)) p3_load_part(X, 4); }
        }
        SBAR();
    }
}
template <int NT>
__device__ __forceinline__ void p3_loop(P3Ctx& X, int nhalf, f32x4 (&acc)[4][5], bool w0, gu64* fo, bool fo_use, u64 ep, gu64* own_o) {
    u64 fv = 0;
    WAIT_VM0(); BARRIER();
    if (threadIdx.x == 0) st_agent(own_o, ep);
    P3Frag F0, F1;
    u32 so = 0;
    p3_half<NT, 0, true, false>(X, 0, so, F0, F1, acc);
    p3_half<NT, 1, true, true>(X, 0, so, F1, F0, acc);
    int t = 1;
    for (; 2 * t + 1 < nhalf; ++t) {
        so = so == 2 * P3_SLOT ? 0 : so + P3_SLOT;
        if (w0) { if (t == 2) poll_issue(fv, fo); else if (t == 4) poll_check(fv, fo, fo_use, ep); }
        p3_half<NT, 0, true, true>(X, t, so, F0, F1, acc);
        p3_half<NT, 1, true, true>(X, t, so, F1, F0, acc);
    }
    if (2 * t < nhalf) {
        so = so == 2 * P3_SLOT ? 0 : so + P3_SLOT;
        p3_half<NT, 0, true, true>(X, t, so, F0, F1, acc);
        p3_half<NT, 1, false, true>(X, t, so, F1, F0, acc);
    } else p3_half<NT, 0, false, true>(X, t, so, F0, F1, acc);
}
__device__ __forceinline__ void p3_phase(const Args& A, char* smem, u64 ep) {
    STAMP_DECL STAMP(0);
    const int tid = threadIdx.x, lane = tid & 63, wid = RFL(tid >> 6);
    const int sp = blockIdx.x & 7, idx = blockIdx.x >> 3;
    const int mt = idx & 7, nt = idx >> 3;
    const u32 lds0 = (u32)(uintptr_t)smem;
    const int mq = wid >> 1, nh = (wid & 1) ^ (wid >> 2);
    const int i16 = lane & 15, kq = lane >> 4;
    P3Ctx X;
    const int no = sp < 2 ? 1 : (sp < 4 ? 2 : 3), o0 = sp < 2 ? sp : (sp < 4 ? 2 * sp - 2 : 3 * sp - 6);
    STAMP(4);
    const int nhalf = 18 + no; X.nst = (nhalf + 1) >> 1; X.tl = 0; X.sl = 0;
    X.pA = A.hb + (size_t)((mt * 8 + wid) * 144 + 18 * sp) * 2048;
    X.oA = A.ob + (size_t)((mt * 8 + wid) * 18 + o0) * 2048;
    X.pB = (const char*)A.w2 + (size_t)(576 * sp) * 1152 + 288 * nt;
    X.oB = (const char*)A.wo + (size_t)(32 * o0) * 1152 + 288 * nt;
    X.dA = lds0 + wid * 4096; X.dB = lds0 + P3_A + wid * 144 * 16;
    X.vA = lane * 16;
    { const int e0 = wid * 144 + lane, e1 = e0 + 64, e2 = e0 + 128;
      X.vB0 = (e0 / 18) * 1152 + (e0 % 18) * 16; X.vB1 = (e1 / 18) * 1152 + (e1 % 18) * 16; X.vB2 = (e2 / 18) * 1152 + (e2 % 18) * 16; }
    X.ab = (lcptr)(size_t)lds0 + (mq * 2) * 4096 + kq * 512 + i16 * 16;
    X.bb = (lcptr)(size_t)lds0 + P3_A + (16 * (kq >> 1) + 4 * (kq & 1) + (i16 >> 2)) * 288 + (nh ? 5 * 32 : 0) + (i16 & 3) * 8;
#pragma unroll
    for (int t = 0; t < 2; ++t)
#pragma unroll
        for (int k = 0; k < 5; ++k) p3_load_part(X, k);
    f32x4 acc[4][5];
#pragma unroll
    for (int m = 0; m < 4; ++m)
#pragma unroll
        for (int n = 0; n < 5; ++n) acc[m][n] = (f32x4){0.f, 0.f, 0.f, 0.f};
    STAMP(1);
    const int hp0 = (32 * o0) / 36, hp1 = (32 * (o0 + no) - 1) / 36, hp = hp0 + (lane >> 1);
    gu64* fo = flag_ptr(A.ctrl, F_O, attn_wg(2 * mt + (lane & 1), hp <= hp1 ? hp : hp0));
    if (nh == 0) p3_loop<5>(X, nhalf, acc, wid == 0, fo, hp <= hp1, ep, flag_ptr(A.ctrl, F_O, blockIdx.x)); else p3_loop<4>(X, nhalf, acc, wid == 0, fo, hp <= hp1, ep, flag_ptr(A.ctrl, F_O, blockIdx.x));
    STAMP(2);
    const u32 tile = (u32)(mt * 4 + nt) * 8 * (72 * 1024);
    const __amdgpu_buffer_rsrc_t prs = mkrsrc(A.part + tile, 8 * 72 * 1024);
    const int NTN = nh ? 4 : 5;
#pragma unroll
    for (int pr = 0; pr < 2; ++pr)
#pragma unroll
        for (int n = 0; n < 5; ++n)
            if (n < NTN) st16_sc1((u32x4){cvtpk_c(acc[2 * pr][n][0], acc[2 * pr][n][1]), cvtpk_c(acc[2 * pr][n][2], acc[2 * pr][n][3]),
                                          cvtpk_c(acc[2 * pr + 1][n][0], acc[2 * pr + 1][n][1]), cvtpk_c(acc[2 * pr + 1][n][2], acc[2 * pr + 1][n][3])}, prs,
                                  sp * (72 * 1024) + ((mq * 2 + pr) * 9 + (nh ? 5 : 0) + n) * 1024 + lane * 16);
    WAIT_VM0(); BARRIER();
    if (tid == 0) st_agent(flag_ptr(A.ctrl, F_PART, blockIdx.x), ep);
    STAMP(3);
    if (tid < 320) {
        const bool act = tid < 288;
        const int ck0 = act ? tid : 0;
        u32x2 bb[2]; float og[2][2];
#pragma unroll
        for (int r = 0; r < 2; ++r) { const int ck = ck0 + 288 * r, n = ck >> 6, ln = ck & 63;
            bb[r] = *(const u32x2*)(A.b2 + nt * 144 + n * 16 + (ln >> 4) * 4);
#pragma unroll
            for (int e = 0; e < 2; ++e) og[r][e] = 1.f - __uint_as_float(__builtin_amdgcn_raw_buffer_load_b32(mkrsrc(A.rowrec, S_ * 32), (mt * 256 + 32 * sp + 16 * e + (ln & 15)) * 32 + 28, 0, 16)); }
        poll_flags(flag_ptr(A.ctrl, F_PART, (lane & 7) + 8 * idx), lane < 8, ep);
        u32x4 pv[2][8];
#pragma unroll
        for (int r = 0; r < 2; ++r)
#pragma unroll
            for (int s = 0; s < 8; ++s) { const int ck = ck0 + 288 * r; pv[r][s] = ld16_sc1(prs, s * (72 * 1024) + (sp * 9 + (ck >> 6)) * 1024 + (ck & 63) * 16); }
#pragma unroll
        for (int r = 0; r < 2; ++r) {
            float v[8] = {0.f, 0.f, 0.f, 0.f, 0.f, 0.f, 0.f, 0.f};
#pragma unroll
            for (int s = 0; s < 8; ++s)
#pragma unroll
                for (int j = 0; j < 4; ++j) { v[2 * j] += bflo(pv[r][s][j]); v[2 * j + 1] += bfhi(pv[r][s][j]); }
            const int ck = ck0 + 288 * r, n = ck >> 6, ln = ck & 63;
#pragma unroll
            for (int e = 0; e < 2; ++e) {
                const int row = mt * 256 + 32 * sp + 16 * e + (ln & 15), col = nt * 144 + n * 16 + (ln >> 4) * 4;
                u32x2 o; o[0] = cvtpk(v[4 * e] + og[r][e] * bflo(bb[r][0]), v[4 * e + 1] + og[r][e] * bfhi(bb[r][0])); o[1] = cvtpk(v[4 * e + 2] + og[r][e] * bflo(bb[r][1]), v[4 * e + 3] + og[r][e] * bfhi(bb[r][1]));
                if (act) *(u32x2*)(A.out + (size_t)row * D_ + col) = o;
            }
        }
    }
    STAMP(5);
    STAMP_OUT(A.stamps + (1u << 17), 6);
}
constexpr int MEGA_LDS = 163840;
static_assert(P1_LDS <= MEGA_LDS && AT_LDS <= MEGA_LDS && P3_LDS <= MEGA_LDS, "LDS");
__global__ __launch_bounds__(512, 2) void mega_kernel(Args A) {
    extern __shared__ __attribute__((aligned(16))) char smem[];
    u64 ep;
    typedef __attribute__((address_space(4))) const u64* ArgP;
#if defined(__HIP_DEVICE_COMPILE__)
    ArgP ap = (ArgP)__builtin_amdgcn_kernarg_segment_ptr();
#else
    ArgP ap = nullptr;
#endif
#define LOAD_ARGS(a) Args a; { asm volatile("" : "+s"(ap)); u64 w_[17]; _Pragma("unroll") for (int i_ = 0; i_ < 17; ++i_) w_[i_] = ap[i_]; __builtin_memcpy(&a, w_, sizeof(Args)); }
    static_assert(sizeof(Args) == 17 * 8, "Args: 17 pointers");
    { LOAD_ARGS(a) p1_phase(a, smem, ep); }
    { LOAD_ARGS(a) attn_phase(a, smem, ep); }
    { LOAD_ARGS(a) p3_phase(a, smem, ep); }
    if (blockIdx.x == 0 && threadIdx.x < 64) {
        LOAD_ARGS(a) u64* ctrl = a.ctrl;
#pragma unroll
        for (int i = 0; i < 4; ++i) poll_flags(flag_ptr(ctrl, F_STAT, threadIdx.x + 64 * i), true, ep);
        if (threadIdx.x == 0) st_agent(epoch_ptr(ctrl), ep);
    }
}
extern "C" void kernel_launch(void* const* d_in, const int* in_sizes, int n_in, void* d_out, int out_size, void* d_ws, size_t ws_size, hipStream_t stream) {
    char* ws = (char*)d_ws;
    Args A;
    A.x = (const u16*)d_in[0]; A.wqkv = (const u16*)d_in[1]; A.wo = (const u16*)d_in[2]; A.w1 = (const u16*)d_in[4]; A.b1 = (const u16*)d_in[5];
    A.w2 = (const u16*)d_in[6]; A.b2 = (const u16*)d_in[7]; A.gw = (const u16*)d_in[8];
    A.rowrec = (u32x4*)ws;
    A.wext = (u32x4*)(ws + 65536);
    char* big = ws + 147456;
    A.qkvb = big;
    A.hb = big + 7077888;
    A.ob = big + 7077888 + 18874368;
    A.part = big + 7077888 + 18874368 + 2359296;
    A.ctrl = (u64*)(big + 7077888 + 18874368 + 2359296 + 18874368);
    A.out = (u16*)d_out;
    A.stamps = (unsigned long long*)(ws + (200u << 20));
    (void)hipFuncSetAttribute((const void*)mega_kernel, hipFuncAttributeMaxDynamicSharedMemorySize, MEGA_LDS);
    mega_kernel<<<256, 512, MEGA_LDS, stream>>>(A);
}
```

```cpp
#include <hip/hip_runtime.h>
#include <hip/hip_bf16.h>
typedef unsigned short u16;
typedef unsigned int u32;
#define LAS __attribute__((address_space(3)))
typedef short bf16x8 __attribute__((ext_vector_type(8)));
typedef short s16x4 __attribute__((ext_vector_type(4)));
typedef float f32x16 __attribute__((ext_vector_type(16)));
typedef float f32x4 __attribute__((ext_vector_type(4)));
typedef u32 u32x4 __attribute__((ext_vector_type(4)));
typedef u32 u32x2 __attribute__((ext_vector_type(2)));
typedef LAS char* lptr;
typedef LAS const char* lcptr;

constexpr int S_ = 2048, D_ = 576, H_ = 32, DH_ = 18, F_ = 4608, NQKV_ = 1728;

__device__ __forceinline__ float bf2f(u16 b) { return __uint_as_float(((u32)b) << 16); }
__device__ __forceinline__ float bflo(u32 w) { return __uint_as_float(w << 16); }
__device__ __forceinline__ float bfhi(u32 w) { return __uint_as_float(w & 0xffff0000u); }
typedef __bf16 bf16x2_t __attribute__((ext_vector_type(2)));
typedef float f32x2 __attribute__((ext_vector_type(2)));
__device__ __forceinline__ u32 cvtpk_c(float lo, float hi) { return __builtin_bit_cast(u32, __builtin_convertvector((f32x2){lo, hi}, bf16x2_t)); }
__device__ __forceinline__ u32 cvtpk(float lo, float hi) { u32 r; asm("v_cvt_pk_bf16_f32 %0, %1, %2" : "=v"(r) : "v"(lo), "v"(hi)); return r; }
__device__ __forceinline__ u16 f2bf(float f) { return (u16)(cvtpk(f, f) & 0xffffu); }
__device__ __forceinline__ void glds16(const void* g, u32 lds_base) {
    u32 sv;
    asm volatile("s_mov_b32 %0, m0\n\ts_mov_b32 m0, %2\n\ts_nop 0\n\tglobal_load_lds_dwordx4 %1, off\n\ts_mov_b32 m0, %0" : "=&s"(sv) : "v"(g), "s"(lds_base) : "memory");
}
__device__ __forceinline__ void glds16s(const void* sbase, u32 voff, u32 lds_base) {
    u32 sv;
    asm volatile("s_mov_b32 %0, m0\n\ts_mov_b32 m0, %3\n\ts_nop 0\n\tglobal_load_lds_dwordx4 %1, %2\n\ts_mov_b32 m0, %0" : "=&s"(sv) : "v"(voff), "s"(sbase), "s"(lds_base) : "memory");
}
__device__ __forceinline__ void glds16s_off1k(const void* sbase, u32 voff, u32 lds_base) {
    u32 sv;
    asm volatile("s_mov_b32 %0, m0\n\ts_mov_b32 m0, %3\n\ts_nop 0\n\tglobal_load_lds_dwordx4 %1, %2\n\tglobal_load_lds_dwordx4 %1, %2 offset:1024\n\ts_mov_b32 m0, %0" : "=&s"(sv) : "v"(voff), "s"(sbase), "s"(lds_base) : "memory");
}
__device__ __forceinline__ void glds16s_off1k_sc1(const void* sbase, u32 voff, u32 lds_base) {
    u32 sv;
    asm volatile("s_mov_b32 %0, m0\n\ts_mov_b32 m0, %3\n\ts_nop 0\n\tglobal_load_lds_dwordx4 %1, %2 sc1\n\tglobal_load_lds_dwordx4 %1, %2 offset:1024 sc1\n\ts_mov_b32 m0, %0" : "=&s"(sv) : "v"(voff), "s"(sbase), "s"(lds_base) : "memory");
}
__device__ __forceinline__ s16x4 trd(lcptr p) {
    typedef short v4i16_t __attribute__((ext_vector_type(4)));
    return __builtin_bit_cast(s16x4, __builtin_amdgcn_ds_read_tr16_b64_v4i16((LAS v4i16_t*)p));
}
__device__ __forceinline__ bf16x8 cat8(s16x4 a, s16x4 b) { return (bf16x8){a[0], a[1], a[2], a[3], b[0], b[1], b[2], b[3]}; }
#define MFMA32(a, b, c) __builtin_amdgcn_mfma_f32_32x32x16_bf16(a, b, c, 0, 0, 0)
#define MFMA16(a, b, c) __builtin_amdgcn_mfma_f32_16x16x32_bf16(a, b, c, 0, 0, 0)
#define SBAR() __builtin_amdgcn_sched_barrier(0)
#define PIN(x) asm volatile("" : "+v"(x))
#define RFL(x) __builtin_amdgcn_readfirstlane(x)

#define STAMP_DECL
#define STAMP(i)
#define STAMP_OUT(buf, n)

typedef unsigned long long u64;
typedef __attribute__((address_space(1))) u64 gu64;
constexpr int FLAG_STRIDE = 16;
constexpr int F_STAT = 0, F_QKV = 1, F_H = 2, F_O = 3, F_PART = 4, F_KINDS = 5;
constexpr size_t CTRL_BYTES = (size_t)(F_KINDS * 256 + 1) * FLAG_STRIDE * 8;
__device__ __forceinline__ gu64* flag_ptr(u64* ctrl, int kind, int wg) { return (gu64*)(ctrl + (size_t)(kind * 256 + wg) * FLAG_STRIDE); }
__device__ __forceinline__ gu64* epoch_ptr(u64* ctrl) { return (gu64*)(ctrl + (size_t)(F_KINDS * 256) * FLAG_STRIDE); }
__device__ __forceinline__ u64 ld_agent(gu64* p) { return __hip_atomic_load(p, __ATOMIC_RELAXED, __HIP_MEMORY_SCOPE_AGENT); }
__device__ __forceinline__ void st_agent(gu64* p, u64 v) { __hip_atomic_store(p, v, __ATOMIC_RELAXED, __HIP_MEMORY_SCOPE_AGENT); }
__device__ __forceinline__ void poll_flags(gu64* mine, bool use, u64 ep) {
    const unsigned long long t0 = __builtin_amdgcn_s_memrealtime();
    for (;;) {
        const u64 v = use ? ld_agent(mine) : ep;
        if (__all(v == ep)) break;
        if (__builtin_amdgcn_s_memrealtime() - t0 > 5000000ull) break;
        __builtin_amdgcn_s_sleep(1);
    }
}
__device__ __forceinline__ void poll_issue(u64& v, gu64* p) { asm volatile("global_load_dwordx2 %0, %1, off sc1" : "=v"(v) : "v"(p) : "memory"); }
__device__ __forceinline__ void poll_check(u64& v, gu64* p, bool use, u64 ep) { asm volatile("" : "+v"(v)); if (!__all(!use || v == ep)) poll_flags(p, use, ep); }
__device__ __forceinline__ void st16_sc1(u32x4 v, __amdgpu_buffer_rsrc_t rs, u32 voff) { __builtin_amdgcn_raw_buffer_store_b128(v, rs, voff, 0, 16); }
__device__ __forceinline__ u32x4 ld16_sc1(__amdgpu_buffer_rsrc_t rs, u32 voff) { return __builtin_amdgcn_raw_buffer_load_b128(rs, voff, 0, 16); }
__device__ __forceinline__ __amdgpu_buffer_rsrc_t mkrsrc(const void* p, u32 bytes) { return __builtin_amdgcn_make_buffer_rsrc((void*)p, 0, bytes, 0x00020000); }
#define INV_AGENT() asm volatile("buffer_inv sc1" ::: "memory")
#define WAIT_VM0() asm volatile("s_waitcnt vmcnt(0)" ::: "memory")
#define BARRIER() asm volatile("s_waitcnt lgkmcnt(0)\n\ts_barrier" ::: "memory")
struct Args {
    const u16* x; const u16* wqkv; const u16* wo; const u16* w1; const u16* b1; const u16* w2; const u16* b2; const u16* gw;
    u64* ctrl; u32x4* rowrec; u32x4* wext; char* qkvb; char* hb; char* ob; char* part; u16* out; unsigned long long* stamps;
};
constexpr int P1_SLOT = 24576, P1_RING = 6 * P1_SLOT, P1_WEXT = P1_RING, P1_ROW = P1_WEXT + 288 * 16 + 64  , P1_CS = P1_ROW + 128 * 32  , P1_LDS = P1_CS + 1024 + 256  ;
constexpr float GC0 = -2.f * 0.7978845608028654f * 1.4426950408889634f, GC1 = GC0 * 0.044715f;
__device__ __forceinline__ u32 bfbits(float f) { return cvtpk(f, 0.f) & 0xffffu; }
constexpr float QSCALE = 0.23570226039551584f * 1.4426950408889634f;
#define P1_WAITBAR(N) asm volatile("s_waitcnt vmcnt(" #N ") lgkmcnt(0)\n\ts_barrier" ::: "memory")

struct P1Ctx {
    const char* w1; __amdgpu_buffer_rsrc_t hrs;
    int c, nch, g1first; u32 l16;
    const char* lb; const char* la;
    u32 ld, ldst0;
    u32 voffW[3];
    lcptr trbase, wexb; u32 wexs; u32 rs;
    lcptr xeb, rcb;
};
constexpr u32 P1_PART = 192u * F_ * 2u;
__device__ __forceinline__ int p1_w1g(const P1Ctx& X, int i) { return i < 6 ? X.g1first + 2 * (i - 2) : ((i == 6 && X.nch == 7) ? ((X.c & 1) ? 9 * X.c : 9 * X.c + 8) : X.g1first); }
__device__ __forceinline__ void p1_piece_a(const P1Ctx& X, int k) { asm volatile("s_add_u32 m0, %0, %1" :: "s"(X.ld), "n"(k * 8192) : "memory", "scc"); }
__device__ __forceinline__ void p1_piece_b(const P1Ctx& X, int k) {
    asm volatile("global_load_lds_dwordx4 %0, %1" :: "v"(X.voffW[k]), "s"(X.la) : "memory");
}
__device__ __forceinline__ void p1_piece(const P1Ctx& X, int k) { p1_piece_a(X, k); asm volatile("s_nop 0"); p1_piece_b(X, k); }
__device__ __forceinline__ void p1_top(P1Ctx& X, int i, int j) {
    if (j == 0) asm volatile("s_cmp_le_u32 %0, 2\n\ts_cbranch_scc1 3f\n\ts_waitcnt vmcnt(12) lgkmcnt(0)\n\ts_barrier\n3:" :: "s"(i) : "memory", "scc");
    else asm volatile("s_waitcnt vmcnt(12) lgkmcnt(0)\n\ts_barrier" ::: "memory");
    X.ld = X.ldst0 + X.rs;
    X.rs = X.rs == 5 * P1_SLOT ? 0 : X.rs + P1_SLOT;
    if (j == 0) X.la = X.lb + 2 * P1_PART;
    if (j == 1) { X.lb = X.w1 + p1_w1g(X, i + 2) * 64; X.la = X.lb; }
    if (j == 2) X.la = X.lb + P1_PART;
}
__device__ __forceinline__ void p1_top3(P1Ctx& X) {
    X.lb = X.w1 + p1_w1g(X, 2) * 64;
    X.la = X.lb; X.ld = X.ldst0; p1_piece(X, 0); p1_piece(X, 1); p1_piece(X, 2);
    X.la = X.lb + P1_PART; X.ld = X.ldst0 + P1_SLOT; p1_piece(X, 0); p1_piece(X, 1); p1_piece(X, 2);
    X.rs = 2 * P1_SLOT;
}
struct Epi { float a0, a1, u0, u1, c0, c1, sd; u32 pw[4]; };
__device__ __forceinline__ void p1_gelu_slot(int s, const P1Ctx& X, const f32x16& pa, Epi& E, u32 vo) {
    const int k = s >> 2, ph = s & 3;
    if (ph == 0) { E.a0 = pa[2 * k]; E.a1 = pa[2 * k + 1]; E.u0 = __builtin_fmaf(E.a0 * E.a0, E.c1, E.c0); E.u1 = __builtin_fmaf(E.a1 * E.a1, E.c1, E.c0); PIN(E.u0); PIN(E.u1); }
    if (ph == 1) { E.u0 = __builtin_amdgcn_exp2f(E.a0 * E.u0); E.u1 = __builtin_amdgcn_exp2f(E.a1 * E.u1); PIN(E.u0); PIN(E.u1); }
    if (ph == 2) { E.u0 = __builtin_amdgcn_rcpf(__builtin_fmaf(E.u0, E.sd, E.sd)); E.u1 = __builtin_amdgcn_rcpf(__builtin_fmaf(E.u1, E.sd, E.sd)); PIN(E.u0); PIN(E.u1); }
    if (ph == 3) { E.pw[k & 3] = cvtpk(E.a0 * E.u0, E.a1 * E.u1);
        if ((k & 3) == 3) __builtin_amdgcn_raw_buffer_store_b128((u32x4){E.pw[0], E.pw[1], E.pw[2], E.pw[3]}, X.hrs, vo + (k >> 2) * 1024, 0, 16); }
}
__device__ __forceinline__ void p1_consts(const P1Ctx& X, Epi& E) { const f32x4 k = *(const LAS f32x4*)X.rcb; E.c0 = k[0]; E.c1 = k[1]; E.sd = k[2]; }
__device__ __forceinline__ void p1_w1_chunk(P1Ctx& X, int i, int pg, bool st, const bf16x8 (&xf)[36], bf16x8 (&wf)[6], f32x16& acc, f32x16& pacc) {
    const u32 vo = st ? X.l16 + (u32)pg * 2048 : 0x40000000u;
    Epi E; bf16x8 we, xe;
    lcptr p = X.trbase;
    SBAR();
#pragma unroll
    for (int s = 0; s < 36; ++s) {
        if (s == 0 || s == 12 || s == 24) { p1_top(X, i, s / 12); p = X.trbase + X.rs; PIN(p); }
        if (s < 4) { pacc = MFMA32(wf[(s + 2) % 6], xf[32 + s], pacc); PIN(pacc); }
        if (s == 4) { pacc = MFMA32(we, xe, pacc); PIN(pacc); f32x16 z = {}; acc = MFMA32(wf[0], xf[0], z); PIN(acc); }
        if (s > 4) { acc = MFMA32(wf[(s - 4) % 6], xf[s - 4], acc); PIN(acc); }
        if (s % 4 == 2) p1_piece_a(X, (s % 12) >> 2);
        if (s == 1) { we = *(const LAS bf16x8*)(X.wexb + (st ? pg - 9 * X.c : 0) * X.wexs); xe = *(const LAS bf16x8*)X.xeb; }
        if (s == 3) p1_consts(X, E);
        if ((s & 1) == 0) {
            wf[(s + 1) % 6] = cat8(trd(p + ((s + 1) % 12) * 2048), trd(p + ((s + 1) % 12) * 2048 + 512));
            wf[s % 6] = cat8(trd(p + (s % 12) * 2048), trd(p + (s % 12) * 2048 + 512));
        }
        if (s % 4 == 2) p1_piece_b(X, (s % 12) >> 2);
        if (s >= 6) p1_gelu_slot(s - 6, X, pacc, E, vo);
        SBAR();
    }
    p1_gelu_slot(30, X, pacc, E, vo); p1_gelu_slot(31, X, pacc, E, vo);
}
template <bool SWAP, int J, bool LD>
__device__ __forceinline__ void p1_qkv_stage(P1Ctx& X, lcptr p, const bf16x8 (&xf)[36], f32x16& acc) {
    bf16x8 wf[4];
    PIN(p);
#pragma unroll
    for (int s = 0; s < 12 + 3; ++s) {
        if (s < 12) wf[s & 3] = cat8(trd(p + s * 2048), trd(p + s * 2048 + 512));
        if (s >= 3) { const int u = s - 3;
            if (J == 0 && u == 0) { f32x16 z = {}; acc = SWAP ? MFMA32(xf[0], wf[0], z) : MFMA32(wf[0], xf[0], z); }
            else acc = SWAP ? MFMA32(xf[J * 12 + u], wf[u & 3], acc) : MFMA32(wf[u & 3], xf[J * 12 + u], acc); }
        if (LD && (s == 4 || s == 8 || s == 12)) p1_piece(X, (s >> 2) - 1);
        SBAR();
    }
}
__device__ __forceinline__ void p1_qkv_store(__amdgpu_buffer_rsrc_t rs, u32 off, int lane, const f32x16& a, float sc) {
    u32 w[8];
#pragma unroll
    for (int k = 0; k < 8; ++k) w[k] = cvtpk(a[2 * k] * sc, a[2 * k + 1] * sc);
    st16_sc1((u32x4){w[0], w[1], w[2], w[3]}, rs, off + lane * 16);
    st16_sc1((u32x4){w[4], w[5], w[6], w[7]}, rs, off + 1024 + lane * 16);
}

__device__ __forceinline__ int p1_wg(int rb, int c) { return ((rb >> 3) | ((c >> 2) << 1)) + 8 * ((rb & 7) | ((c & 3) << 3)); }
__device__ __forceinline__ float dpp_ror4(float v) { return __builtin_bit_cast(float, __builtin_amdgcn_update_dpp(0, __builtin_bit_cast(int, v), 0x124, 0xf, 0xf, false)); }
__device__ __forceinline__ float dpp_ror8(float v) { return __builtin_bit_cast(float, __builtin_amdgcn_update_dpp(0, __builtin_bit_cast(int, v), 0x128, 0xf, 0xf, false)); }
__device__ __forceinline__ float xadd16(float v) { auto r = __builtin_amdgcn_permlane16_swap(__float_as_uint(v), __float_as_uint(v), false, false); return __uint_as_float(r[0]) + __uint_as_float(r[1]); }
__device__ __forceinline__ float xadd32(float v) { auto r = __builtin_amdgcn_permlane32_swap(__float_as_uint(v), __float_as_uint(v), false, false); return __uint_as_float(r[0]) + __uint_as_float(r[1]); }
template <int CTRL> __device__ __forceinline__ float dpp_mov(float v) { return __builtin_bit_cast(float, __builtin_amdgcn_update_dpp(0, __builtin_bit_cast(int, v), CTRL, 0xf, 0xf, false)); }
__device__ __forceinline__ float wave_sum(float v) {
    v += dpp_mov<0x121>(v); v += dpp_mov<0x122>(v); v += dpp_mov<0x124>(v); v += dpp_mov<0x128>(v);
    return xadd32(xadd16(v));
}
#define LD16A(dst, voff, sbase) asm volatile("global_load_dwordx4 %0, %1, %2" : "=v"(dst) : "v"(voff), "s"(sbase) : "memory")

__device__ __forceinline__ int p1_qkv_owner(int G) { return G < 24 ? G >> 2 : 6 + (G - 24) / 3; }
__device__ __forceinline__ gu64* attn_flag(const Args& A, int lane) {
    const int xcd = blockIdx.x & 7, idx = blockIdx.x >> 3, qb = idx >> 1, hp = xcd + 8 * (idx & 1), cg0 = (36 * hp) >> 5;
    const int gi = lane / 3, rr = lane - 3 * gi; int rbx = qb - rr; if (rbx < 0) rbx = 0;
    return flag_ptr(A.ctrl, F_QKV, p1_wg(rbx, p1_qkv_owner((lane < 18 ? (gi >> 1) * 18 + cg0 + (gi & 1) : 0))));
}
constexpr int AT_K = 0, AT_V = 12 * 4096, AT_Q = 24 * 4096, AT_LDS = AT_Q + 4 * 4096;
__device__ __forceinline__ void attn_stage(const Args& A, u32 lds0, int wid, int lane) {
    const int xcd = blockIdx.x & 7, idx = blockIdx.x >> 3, qb = idx >> 1, hp = xcd + 8 * (idx & 1), cg0 = (36 * hp) >> 5;
#pragma unroll
    for (int i = 0; i < 7; ++i) {
        const int p = wid + 8 * i;
        const int u = p >> 1, gsel = p & 1;
        const int sec = u < 12 ? 1 : (u < 24 ? 2 : 0), ub = u < 12 ? u : (u < 24 ? u - 12 : u - 24);
        int rb = qb * 4 + ub - (sec ? 8 : 0); if (rb < 0) rb = 0;
        glds16s_off1k_sc1(A.qkvb + (size_t)(rb * 54 + sec * 18 + cg0 + gsel) * 2048, lane * 16, lds0 + u * 4096 + gsel * 2048);
    }
}
__device__ __forceinline__ void p1_phase(const Args& A, char* smem, u64& ep) {
    STAMP_DECL STAMP(0);
    const int tid = threadIdx.x, lane = tid & 63, wid = RFL(tid >> 6);
    const int xcd = blockIdx.x & 7, idx = blockIdx.x >> 3;
    const int rb = (xcd & 1) * 8 + (idx & 7), c = (xcd >> 1) * 4 + (idx >> 3);
    const u32 lds0 = (u32)(uintptr_t)smem;
    const int r8 = lane >> 3, cp = lane & 7;
    const int csw = cp ^ (((r8 >> 1) & 1) << 2);
    int gq0, gq1, gs;
    if (c < 6) { gq0 = 4 * c; gq1 = 4 * c + 2; gs = 0; } else { const int q0 = 24 + 3 * (c - 6), odd = (c - 6) & 1; gq0 = q0 + odd; gq1 = 0; gs = odd ? q0 : q0 + 2; }
    const u32 voffQ = (u32)(wid * 8 + r8) * (NQKV_ * 2) + csw * 16;
    const char* wq0 = (const char*)A.wqkv + gq0 * 64;
    const char* xs = (const char*)A.x + (size_t)rb * 128 * (D_ * 2);
    u32 voffX[2];
#pragma unroll
    for (int i = 0; i < 2; ++i) { const int row = 16 * wid + 8 * i + r8; voffX[i] = (u32)row * (D_ * 2) + ((cp ^ ((row >> 1) & 7)) << 4); }
    const u32 XA = lds0 + P1_SLOT, XB = lds0 + 4 * P1_SLOT;
#define P1_LDQ(sb, j, slot) _Pragma("unroll") for (int k = 0; k < 3; ++k) glds16s((sb) + (size_t)((j) * 192 + k * 64) * (NQKV_ * 2), voffQ, lds0 + (slot) * P1_SLOT + wid * 1024 + k * 8192)
#define P1_LDX(piece, dst) _Pragma("unroll") for (int i = 0; i < 2; ++i) glds16s(xs + (piece) * 128, voffX[i], (dst) + (16 * wid + 8 * i) * 128)
    P1_LDQ(wq0, 0, 0);
    P1_LDX(0, XA); P1_LDX(1, XA + 16384); P1_LDX(2, XA + 32768);
    asm volatile("s_barrier" ::: "memory");
    P1_LDQ(wq0, 1, 3);
    P1_LDX(3, XB); P1_LDX(4, XB + 16384); P1_LDX(5, XB + 32768);
    const int srow = rb * 128 + c * 8 + wid;
    u32x4 sx0, sx1, sg0, sg1, sw[5]; u64 e0; u32 sb1;
    { const u32 o0 = lane * 16, o1 = 1024 + (lane & 7) * 16;
      const char* xr = (const char*)A.x + (size_t)srow * (D_ * 2);
      LD16A(sx0, o0, xr); LD16A(sx1, o1, xr); LD16A(sg0, o0, (const char*)A.gw); LD16A(sg1, o1, (const char*)A.gw);
      const char* wc = (const char*)A.w1 + (size_t)(72 * wid) * (F_ * 2) + (9 * c + (rb < 9 ? rb : 0)) * 64;
#pragma unroll
      for (int i = 0; i < 5; ++i) { int r = (lane >> 2) + 16 * i; if (r > 71) r = 71; const u32 o = (u32)r * (F_ * 2) + (lane & 3) * 16; LD16A(sw[i], o, wc); }
      asm volatile("global_load_ushort %0, %1, %2" : "=v"(sb1) : "v"((u32)(lane & 31) * 2), "s"((const char*)A.b1 + (9 * c + (rb < 9 ? rb : 0)) * 64) : "memory");
      asm volatile("global_load_dwordx2 %0, %1, %2 sc1" : "=v"(e0) : "v"(0u), "s"(epoch_ptr(A.ctrl)) : "memory"); }
    {
        const char* wa = c < 6 ? (const char*)A.wqkv + gq1 * 64 : (const char*)A.wqkv + gs * 64; const char* wb = (const char*)A.w1 + ((c & 1) ? 9 * c : 9 * c + 8) * 64;
        const char* pb = xs; u32 po = 0; bool pu = false;
        if (wid < 2) { const int k = wid * 64 + lane, l = (c & 3) * 96 + k; pu = k < 96; po = (u32)(l / 3) * (D_ * 2) + (6 + l % 3) * 128; }
        else if (wid == 2) { pb = wq0; pu = lane < 24; po = (u32)(384 + (rb & 7) * 24 + lane) * (NQKV_ * 2); }
        else if (wid < 5) { const int k = (wid - 3) * 64 + lane; pb = wa; pu = k < 72; po = (u32)((rb & 7) * 72 + k) * (NQKV_ * 2); }
        else if (wid < 7) { const int k = (wid - 5) * 64 + lane; pb = wb; pu = k < 72 && c >= 6; po = (u32)((rb & 7) * 72 + k) * (F_ * 2); }
        asm volatile("s_mov_b32 m0, %0" :: "s"(lds0 + P1_CS + 1024) : "memory");
        if (pu) asm volatile("s_nop 0\n\tglobal_load_lds_dword %0, %1" :: "v"(po), "s"(pb) : "memory");
    }
    if (tid < 4) *(LAS u32x4*)((lptr)(size_t)lds0 + P1_WEXT + 288 * 16 + tid * 16) = (u32x4){0u, 0u, 0u, 0u};
    const int rg = wid & 3, ch = wid >> 2;
    P1Ctx X; X.w1 = (const char*)A.w1; X.hrs = mkrsrc(A.hb + (size_t)(rb * 4 + rg) * 144 * 2048, 144 * 2048); X.c = c; X.l16 = lane * 16;
    const int nch = c < 6 ? 7 : 6; X.nch = nch; X.g1first = 9 * c + (c & 1);
    const int m = lane & 31, h = lane >> 5;
#pragma unroll
    for (int k = 0; k < 3; ++k) X.voffW[k] = (u32)(wid * 8 + r8 + 64 * k) * (F_ * 2) + csw * 16;
    X.ldst0 = lds0 + wid * 1024; X.rs = 0; X.ld = X.ldst0; X.lb = X.w1; X.la = X.w1;
    { const int s16 = (lane >> 4) & 1, q = (lane & 15) >> 2, p = lane & 3;
      X.trbase = (lcptr)(size_t)lds0 + (8 * h + q) * 128 + (((4 * ch + 2 * s16 + (p >> 1)) ^ (((q >> 1) & 1) << 2)) * 16) + (p & 1) * 8;
      X.wexb = (lcptr)(size_t)lds0 + (h ? P1_WEXT + 288 * 16 : P1_WEXT + m * 16); X.wexs = h ? 0 : 512; }
    const int wsingle = (c & 1) ? 9 * c : 9 * c + 8;
    bf16x8 xf[36];
    f32x16 accA, accB;
    const bool swp = c >= 10;
    const int xrow = rg * 32 + m, xz = (xrow >> 1) & 7;
    X.rcb = (lcptr)(size_t)lds0 + P1_ROW + xrow * 32 + 16;
    X.xeb = h ? (lcptr)(size_t)lds0 + P1_WEXT + 288 * 16 : X.rcb - 16;
    lcptr xa = (lcptr)(size_t)XA + xrow * 128, xb = (lcptr)(size_t)XB + xrow * 128;
#define P1_XF(s0, base) _Pragma("unroll") for (int s = 0; s < 12; ++s) xf[s0 + s] = *(const LAS bf16x8*)((base) + (s >> 2) * 16384 + (((2 * (s & 3) + h) ^ xz) << 4))
#define P1_QS(J, LD, P) do { if (swp) p1_qkv_stage<true, J, LD>(X, P, xf, accA); else p1_qkv_stage<false, J, LD>(X, P, xf, accA); } while (0)
    STAMP(1);
    P1_WAITBAR(20);
    STAMP(2);
    P1_XF(0, xa);
    P1_WAITBAR(20);
    P1_LDX(6, XA); P1_LDX(7, XA + 16384); P1_LDX(8, XA + 32768);
    P1_QS(0, false, X.trbase);
    STAMP(3);
    P1_WAITBAR(6);
    STAMP(4);
    P1_LDQ(wq0, 2, 0);
    P1_XF(12, xb);
    P1_QS(1, false, X.trbase + 3 * P1_SLOT);
    asm volatile("" : "+v"(sx0), "+v"(sx1), "+v"(sg0), "+v"(sg1), "+v"(sw[0]), "+v"(sw[1]), "+v"(sw[2]), "+v"(sw[3]), "+v"(sw[4]), "+v"(e0), "+v"(sb1) :: "memory");
    ep = ((u64)(u32)RFL((u32)(e0 >> 32)) << 32 | (u32)RFL((u32)e0)) + 1;
    {
        typedef __bf16 bf2 __attribute__((ext_vector_type(2)));
        const bool two = lane < 8;
        const bf2 one2 = __builtin_bit_cast(bf2, 0x3f803f80u);
        float s = 0.f, gsum = 0.f, q2 = 0.f;
#pragma unroll
        for (int j = 0; j < 8; ++j) { const u32 xw = j < 4 ? sx0[j] : (two ? sx1[j - 4] : 0u), gw = j < 4 ? sg0[j] : sg1[j - 4]; const bf2 xv = __builtin_bit_cast(bf2, xw);
            s = __builtin_amdgcn_fdot2_f32_bf16(xv, one2, s, false); gsum = __builtin_amdgcn_fdot2_f32_bf16(xv, __builtin_bit_cast(bf2, gw), gsum, false); q2 = __builtin_amdgcn_fdot2_f32_bf16(xv, xv, q2, false); }
        s = wave_sum(s); gsum = wave_sum(gsum); q2 = wave_sum(q2);
        const float m = s * (1.f / 576.f);
        const float q = fmaxf(q2 - 576.f * m * m, 0.f);
        const float sd = __builtin_amdgcn_sqrtf(q * (1.f / 576.f) + 1e-5f), r = __builtin_amdgcn_rcpf(sd);
        const float eg = __builtin_amdgcn_exp2f(-1.4426950408889634f * gsum), gate = __builtin_amdgcn_rcpf(1.f + eg);
        const float sdg = sd * (1.f + __builtin_amdgcn_exp2f(1.4426950408889634f * gsum));
        const u32 mh = bfbits(-m), ml = bfbits(-m - __uint_as_float(mh << 16)), sh = bfbits(sd), sl = bfbits(sd - __uint_as_float(sh << 16));
        const u32x4 rec = lane == 0 ? (u32x4){mh | (mh << 16), ml | (sh << 16), sl, 0u} : (u32x4){__float_as_uint(GC0 * r), __float_as_uint(GC1 * r * r * r), __float_as_uint(sdg), __float_as_uint(gate)};
        if (lane < 2) st16_sc1(rec, mkrsrc(A.rowrec, S_ * 32), srow * 32 + lane * 16);
        if (rb < 9) {
        float cs[8];
#pragma unroll
        for (int j = 0; j < 8; ++j) cs[j] = 0.f;
#pragma unroll
        for (int i = 0; i < 5; ++i)
#pragma unroll
            for (int j = 0; j < 4; ++j) { const u32 wv = (i < 4 || lane < 32) ? sw[i][j] : 0u; cs[2 * j] += bflo(wv); cs[2 * j + 1] += bfhi(wv); }
#pragma unroll
        for (int j = 0; j < 8; ++j) { float v = cs[j]; v += dpp_ror4(v); v += dpp_ror8(v); cs[j] = xadd32(xadd16(v)); }
        if (lane < 4) { LAS f32x4* d = (LAS f32x4*)((lptr)(size_t)lds0 + P1_CS + wid * 128 + lane * 32); d[0] = (f32x4){cs[0], cs[1], cs[2], cs[3]}; d[1] = (f32x4){cs[4], cs[5], cs[6], cs[7]}; }
        }
    }
    STAMP(5);
    P1_WAITBAR(3);
    STAMP(6);
    if (wid == 0 && lane < 32 && rb < 9) {
        float t = 0.f;
#pragma unroll
        for (int w = 0; w < 8; ++w) t += *(const LAS float*)((lcptr)(size_t)lds0 + P1_CS + w * 128 + lane * 4);
        const int col = (9 * c + rb) * 32 + lane;
        const u32 chh = bfbits(t), cl = bfbits(t - __uint_as_float(chh << 16)), bb = sb1;
        st16_sc1((u32x4){chh | (cl << 16), chh | (bb << 16), bb, 0u}, mkrsrc(A.wext, F_ * 16), col * 16);
    }
    if (c < 6) { const char* wq1 = (const char*)A.wqkv + gq1 * 64; P1_LDQ(wq1, 0, 3); P1_LDQ(wq1, 1, 4); P1_LDQ(wq1, 2, 5); }
    else {
        const bool hi = csw >= 4; const u32 pitch = hi ? F_ * 2 : NQKV_ * 2;
        const char* lp = (hi ? (const char*)A.w1 + wsingle * 64 : (const char*)A.wqkv + gs * 64) + (size_t)((u32)(wid * 8 + r8) * pitch) + (csw & 3) * 16;
#pragma unroll
        for (int k = 0; k < 9; ++k) { glds16(lp, lds0 + 3 * P1_SLOT + wid * 1024 + k * 8192); lp += pitch * 64; }
    }
    P1_XF(24, xa);
    P1_WAITBAR(9);
    P1_QS(2, false, X.trbase);
    const __amdgpu_buffer_rsrc_t qrs = mkrsrc(A.qkvb + (size_t)(rb * 4 + rg) * 54 * 2048, 54 * 2048);
    p1_qkv_store(qrs, (gq0 + ch) * 2048, lane, accA, gq0 + ch < 18 ? QSCALE : 1.f);
    STAMP(7);
    P1_WAITBAR(0);
    if (tid == 0) st_agent(flag_ptr(A.ctrl, F_STAT, blockIdx.x), ep);
    p1_top3(X);
    {
        const char* w2s = (const char*)A.w2 + (size_t)(576 * xcd) * 1152 + (size_t)idx * 20736;
        const int l = wid * 64 + lane;
        u64 pc; asm volatile("s_getpc_b64 %0" : "=s"(pc));
        asm volatile("s_mov_b32 m0, %0" :: "s"(lds0 + P1_CS) : "memory");
        if (l < 162) asm volatile("s_nop 0\n\tglobal_load_lds_dword %0, %1" :: "v"((u32)l * 128), "s"(w2s) : "memory");
        if (l < 21) asm volatile("s_nop 0\n\tglobal_load_lds_dword %0, %1" :: "v"((u32)l * 128), "s"((const char*)A.wo + (size_t)blockIdx.x * 2592) : "memory");
        if (wid == 7 && lane < 8) asm volatile("s_nop 0\n\tglobal_load_lds_dword %0, %1" :: "v"((u32)(idx * 8 + lane) * 128), "s"((const char*)(pc & ~(u64)127)) : "memory");
    }
    const bool c1q = c < 6 || ch == 0;
    bf16x8 wf[6];
#pragma unroll
    for (int k = 0; k < 6; ++k) wf[k] = xf[k];
    if (c1q) {
        p1_top(X, 1, 0); P1_QS(0, true, X.trbase + X.rs);
        p1_top(X, 1, 1); P1_QS(1, true, X.trbase + X.rs);
        p1_top(X, 1, 2); P1_QS(2, true, X.trbase + X.rs);
        const int g = c < 6 ? gq1 + ch : gs;
        p1_qkv_store(qrs, g * 2048, lane, accA, g < 18 ? QSCALE : 1.f);
    } else p1_w1_chunk(X, 1, 0, false, xf, wf, accB, accA);
    STAMP(8);
    { const int k = lane < 16 ? p1_wg(rb, lane) : p1_wg(lane - 16, c);
      poll_flags(flag_ptr(A.ctrl, F_STAT, k), lane < 25, ep);
      u32x4 t0 = {}, t1 = {};
      if (tid < 256) t0 = ld16_sc1(mkrsrc(A.rowrec, S_ * 32), (rb * 256 + tid) * 16);
      if (tid < 288) t1 = ld16_sc1(mkrsrc(A.wext, F_ * 16), (288 * c + tid) * 16);
      if (tid < 256) *(LAS u32x4*)((lptr)(size_t)lds0 + P1_ROW + tid * 16) = t0;
      if (tid < 288) *(LAS u32x4*)((lptr)(size_t)lds0 + P1_WEXT + tid * 16) = t1; }
    P1_WAITBAR(0);
    if (tid == 0) st_agent(flag_ptr(A.ctrl, F_QKV, blockIdx.x), ep);
    const int iend = (nch == 7 && ch == 1) ? 6 : nch;
#define P1_G(i) ((i) == 1 ? wsingle : p1_w1g(X, i) + ((i) < 6 ? ch : 0))
#define P1_ST1
#define P1_ST2
    u64 fq = 0;
    for (int i = 2; i < iend; i += 2) {
        p1_w1_chunk(X, i, P1_G(i - 1), i > 2 || !c1q, xf, wf, accA, accB); P1_ST1
        if (wid == 0) { if (i == 2) poll_issue(fq, attn_flag(A, lane)); else if (i == 4) { poll_check(fq, attn_flag(A, lane), lane < 18, ep); } }
        if (i + 1 < iend) p1_w1_chunk(X, i + 1, P1_G(i), true, xf, wf, accB, accA); P1_ST2
    }
    if (iend < nch) { P1_WAITBAR(12); P1_WAITBAR(9); P1_WAITBAR(6); }
    BARRIER();
    attn_stage(A, lds0, wid, lane);
    int ie = iend; asm volatile("" : "+s"(ie));
    if ((ie & 1) != 0) accB = accA;
    { Epi E; const int lg = P1_G(ie - 1); const u32 vo = X.l16 + (u32)lg * 2048;
      const bf16x8 we = *(const LAS bf16x8*)(X.wexb + (lg - 9 * X.c) * X.wexs), xe = *(const LAS bf16x8*)X.xeb;
      p1_consts(X, E);
#pragma unroll
      for (int s = 0; s < 4; ++s) accB = MFMA32(wf[(s + 2) % 6], xf[32 + s], accB);
      accB = MFMA32(we, xe, accB);
#pragma unroll
      for (int s = 0; s < 32; ++s) p1_gelu_slot(s, X, accB, E, vo); }
    STAMP(15);
    STAMP_OUT(A.stamps, 16);
}
__device__ __forceinline__ int qk_off(int c) { const int cc = c & 31; return (c >> 5) * 2048 + (2 * (cc >> 4) + ((cc >> 2) & 1)) * 512 + (((cc >> 3) & 1) * 4 + (cc & 3)) * 2; }
__device__ __forceinline__ float xmax16(float v) { auto r = __builtin_amdgcn_permlane16_swap(__float_as_uint(v), __float_as_uint(v), false, false); return fmaxf(__uint_as_float(r[0]), __uint_as_float(r[1])); }
__device__ __forceinline__ float xmax32(float v) { auto r = __builtin_amdgcn_permlane32_swap(__float_as_uint(v), __float_as_uint(v), false, false); return fmaxf(__uint_as_float(r[0]), __uint_as_float(r[1])); }
__device__ __forceinline__ float max3f(float a, float b, float c) { return __builtin_fmaxf(__builtin_fmaxf(a, b), c); }

template <bool EARLY>
__device__ __forceinline__ void attn_mask(f32x4& s, int t, int dq, int kb) {
    if (EARLY) {
#pragma unroll
        for (int r = 0; r < 4; ++r) { const int d = dq - 16 * t - r; const bool ok = (u32)d <= 255u && (kb + 16 * t + r) >= 0; s[r] = ok ? s[r] : -INFINITY; }
    } else if (t < 2) {
#pragma unroll
        for (int r = 0; r < 4; ++r) s[r] = (dq - 16 * t - r <= 255) ? s[r] : -INFINITY;
    } else if (t >= 16) {
#pragma unroll
        for (int r = 0; r < 4; ++r) s[r] = (dq - 16 * t - r >= 0) ? s[r] : -INFINITY;
    }
}
#define ATS_ARG
#define ATS_PASS
template <bool EARLY>
__device__ __forceinline__ void attn_units(lcptr L, int c00  , int cones, int w, int lane, int Q0, float gate, __amdgpu_buffer_rsrc_t ors, int head0, gu64* fhp, u64 ep ATS_ARG) {
    u64 fh = 0; if (w == 0) poll_issue(fh, fhp);
    const int i16 = lane & 15, g = lane >> 4;
    const int lb0 = w >> 1;
    const int q = Q0 + 16 * w + i16, kb = Q0 - 256 + lb0 * 32 + 4 * g, dq = q - kb;
    bf16x8 kf[2][18], qf[2];
    f32x4 nm[2];
#pragma unroll
    for (int u = 0; u < 2; ++u) {
        const int c0 = c00 + 18 * u;
        const int p2 = c0 & 2, op = qk_off(c0 + p2 + 4 * g), os = qk_off(p2 ? c0 - 2 : c0 + 16);
        lcptr qp = L + AT_Q + (w >> 1) * 4096 + ((w & 1) * 16 + i16) * 16;
        const u32x2 qa = *(const LAS u32x2*)(qp + op), qc = *(const LAS u32x2*)(qp + os);
        qf[u] = __builtin_bit_cast(bf16x8, (u32x4){qa[0], qa[1], (g == 0 && !p2) ? qc[0] : 0u, (g == 0 && p2) ? qc[1] : 0u});
        lcptr kb0 = L + AT_K + lb0 * 4096 + i16 * 16;
        lcptr k0 = kb0 + op, k1 = kb0 + os;
        PIN(k0); PIN(k1);
#pragma unroll
        for (int t = 0; t < 18; ++t) { const int o = (t >> 1) * 4096 + (t & 1) * 256;
            const u32x2 ka = *(const volatile LAS u32x2*)(k0 + o), kc = *(const volatile LAS u32x2*)(k1 + o);
            kf[u][t] = __builtin_bit_cast(bf16x8, (u32x4){ka[0], ka[1], kc[0], kc[1]}); }
    }
    lcptr va[2], vb[2];
#pragma unroll
    for (int u = 0; u < 2; ++u) {
        const int c0 = c00 + 18 * u, ca = c0 + i16, cb = (i16 < 2) ? c0 + 16 + i16 : (i16 == 2 ? cones : c0);
        va[u] = L + AT_V + lb0 * 4096 + (ca >> 5) * 2048 + (g & 1) * 512 + (ca & 31) * 16 + (g >> 1) * 8;
        vb[u] = L + AT_V + lb0 * 4096 + (cb >> 5) * 2048 + (g & 1) * 512 + (cb & 31) * 16 + (g >> 1) * 8;
        PIN(va[u]); PIN(vb[u]);
    }
    SBAR();
    STAMP(5);
#pragma unroll
    for (int u = 0; u < 2; ++u) {
        f32x4 s[18];
#pragma unroll
        for (int t = 0; t < 18; ++t) s[t] = MFMA16(kf[u][t], qf[u], ((f32x4){0.f, 0.f, 0.f, 0.f}));
#pragma unroll
        for (int t = 0; t < 18; ++t) attn_mask<EARLY>(s[t], t, dq, kb);
        float m0 = max3f(s[0][0], s[0][1], s[0][2]), m1 = max3f(s[0][3], s[1][0], s[1][1]);
        m0 = max3f(m0, s[1][2], s[1][3]);
#pragma unroll
        for (int t = 2; t < 18; t += 2) { m0 = max3f(m0, s[t][0], s[t][1]); m1 = max3f(m1, s[t][2], s[t][3]); m0 = max3f(m0, s[t + 1][0], s[t + 1][1]); m1 = max3f(m1, s[t + 1][2], s[t + 1][3]); }
        const float m = -xmax32(xmax16(fmaxf(m0, m1)));
        nm[u] = (f32x4){m, m, m, m};
    }
    if (w == 0) { WAIT_VM0(); poll_check(fh, fhp, lane < 4, ep); }
    STAMP(6);
    u32x2 vf[3][4]; f32x4 sc2[2][2];
#define AT_VRD(k) do { const int b_ = (k) >> 1, u_ = (k) & 1; vf[(k) % 3][0] = *(const volatile LAS u32x2*)(va[u_] + b_ * 4096); vf[(k) % 3][1] = *(const volatile LAS u32x2*)(va[u_] + b_ * 4096 + 1024); \
        vf[(k) % 3][2] = *(const volatile LAS u32x2*)(vb[u_] + b_ * 4096); vf[(k) % 3][3] = *(const volatile LAS u32x2*)(vb[u_] + b_ * 4096 + 1024); } while (0)
#define AT_QK(k) do { const int b_ = (k) >> 1, u_ = (k) & 1; sc2[(k) & 1][0] = MFMA16(kf[u_][2 * b_], qf[u_], nm[u_]); sc2[(k) & 1][1] = MFMA16(kf[u_][2 * b_ + 1], qf[u_], nm[u_]); } while (0)
    f32x4 oa[2] = {{0.f, 0.f, 0.f, 0.f}, {0.f, 0.f, 0.f, 0.f}}, obb[2] = {{0.f, 0.f, 0.f, 0.f}, {0.f, 0.f, 0.f, 0.f}};
    AT_VRD(0); AT_VRD(1); AT_QK(0);
    SBAR();
#pragma unroll
    for (int k = 0; k < 18; ++k) {
        const int b = k >> 1, u = k & 1;
        f32x4 s0 = sc2[k & 1][0], s1 = sc2[k & 1][1];
        if (k + 1 < 18) { const int b_ = (k + 1) >> 1, u_ = (k + 1) & 1; sc2[(k + 1) & 1][0] = MFMA16(kf[u_][2 * b_], qf[u_], nm[u_]); sc2[(k + 1) & 1][1] = MFMA16(kf[u_][2 * b_ + 1], qf[u_], nm[u_]); PIN(sc2[(k + 1) & 1][0]); PIN(sc2[(k + 1) & 1][1]); }
        if (k + 2 < 18) AT_VRD(k + 2);
        attn_mask<EARLY>(s0, 2 * b, dq, kb); attn_mask<EARLY>(s1, 2 * b + 1, dq, kb);
        u32x4 pw;
        pw[0] = cvtpk_c(__builtin_amdgcn_exp2f(s0[0]), __builtin_amdgcn_exp2f(s0[1])); pw[1] = cvtpk_c(__builtin_amdgcn_exp2f(s0[2]), __builtin_amdgcn_exp2f(s0[3]));
        pw[2] = cvtpk_c(__builtin_amdgcn_exp2f(s1[0]), __builtin_amdgcn_exp2f(s1[1])); pw[3] = cvtpk_c(__builtin_amdgcn_exp2f(s1[2]), __builtin_amdgcn_exp2f(s1[3]));
        const bf16x8 pf = __builtin_bit_cast(bf16x8, pw);
        oa[u] = MFMA16(__builtin_bit_cast(bf16x8, ((u32x4){vf[k % 3][0][0], vf[k % 3][0][1], vf[k % 3][1][0], vf[k % 3][1][1]})), pf, oa[u]);
        obb[u] = MFMA16(__builtin_bit_cast(bf16x8, ((u32x4){vf[k % 3][2][0], vf[k % 3][2][1], vf[k % 3][3][0], vf[k % 3][3][1]})), pf, obb[u]);
        PIN(oa[u]); PIN(obb[u]);
        SBAR();
    }
    STAMP(7);
    auto off = [](int c) { const int cc = c & 31; return (c >> 5) * 2048 + (2 * (cc >> 4) + ((cc >> 2) & 1)) * 512 + (((cc >> 3) & 1) * 4 + (cc & 3)) * 2; };
    const u32 base = (u32)(q >> 5) * 18 * 2048 + (q & 31) * 16;
    const float sc0 = gate * __builtin_amdgcn_rcpf(__shfl(obb[0][2], i16)), sc1 = gate * __builtin_amdgcn_rcpf(__shfl(obb[1][2], i16));
    const u32 p1 = cvtpk(oa[1][0] * sc1, oa[1][1] * sc1), r1 = cvtpk(oa[1][2] * sc1, oa[1][3] * sc1), t1 = cvtpk(obb[1][0] * sc1, obb[1][1] * sc1);
    const u32 nx = (u32)__shfl((int)(g == 0 ? t1 : p1), (lane + 16) & 63);
    const int col0 = 18 * head0;
    typedef u32 u32x2s __attribute__((ext_vector_type(2)));
    __builtin_amdgcn_raw_buffer_store_b64((u32x2s){cvtpk(oa[0][0] * sc0, oa[0][1] * sc0), cvtpk(oa[0][2] * sc0, oa[0][3] * sc0)}, ors, base + off(col0 + 4 * g), 0, 16);
    __builtin_amdgcn_raw_buffer_store_b64((u32x2s){r1, nx}, ors, base + off(col0 + 20 + 4 * g), 0, 16);
    if (g == 0) __builtin_amdgcn_raw_buffer_store_b64((u32x2s){cvtpk(obb[0][0] * sc0, obb[0][1] * sc0), p1}, ors, base + off(col0 + 16), 0, 16);
}

__device__ __forceinline__ int attn_wg(int qb, int hp) { return (hp & 7) + 8 * (2 * qb + (hp >> 3)); }
__device__ __forceinline__ void acquire_agent() { __builtin_amdgcn_fence(__ATOMIC_ACQUIRE, "agent"); WAIT_VM0(); }

__device__ __forceinline__ void attn_phase(const Args& A, char* smem, u64 ep) {
    STAMP_DECL STAMP(0);
    const int tid = threadIdx.x, lane = tid & 63, wid = RFL(tid >> 6);
    const int xcd = blockIdx.x & 7, idx = blockIdx.x >> 3;
    const int qb = idx >> 1, hp = xcd + 8 * (idx & 1);
    const int Q0 = qb * 128, cg0 = (36 * hp) >> 5, c0 = 36 * hp - 32 * cg0;
    const u32 lds0 = (u32)(uintptr_t)smem;
    gu64* fhp;
    { const int sp = blockIdx.x & 7, mt = (blockIdx.x >> 3) & 7; fhp = flag_ptr(A.ctrl, F_H, p1_wg(2 * mt + (lane & 1), 2 * sp + ((lane >> 1) & 1))); }
    const int q = Q0 + 16 * wid + (lane & 15);
    const float gate = __uint_as_float(__builtin_amdgcn_raw_buffer_load_b32(mkrsrc(A.rowrec, S_ * 32), q * 32 + 28, 0, 16));
    WAIT_VM0(); BARRIER();
    if (tid == 0) st_agent(flag_ptr(A.ctrl, F_H, blockIdx.x), ep);
    STAMP(2);
    const int cones = (c0 + 36) & 63;
    if (tid < 48) { const int kb = tid >> 2, pc = tid & 3; *(LAS u32x4*)((lptr)(size_t)lds0 + AT_V + kb * 4096 + (cones >> 5) * 2048 + pc * 512 + (cones & 31) * 16) = (u32x4){0x3f803f80u, 0x3f803f80u, 0x3f803f80u, 0x3f803f80u}; }
    BARRIER();
    STAMP(3);
    const __amdgpu_buffer_rsrc_t ors = mkrsrc(A.ob, S_ * D_ * 2);
    if (qb < 2) attn_units<true>((lcptr)(size_t)lds0, c0, cones, wid, lane, Q0, gate, ors, 2 * hp, fhp, ep ATS_PASS); else attn_units<false>((lcptr)(size_t)lds0, c0, cones, wid, lane, Q0, gate, ors, 2 * hp, fhp, ep ATS_PASS);
    STAMP(8);
    BARRIER();
    STAMP(4);
    STAMP_OUT(A.stamps + (2u << 17), 9);
}
constexpr int P3_A = 32768, P3_B = 64 * 288, P3_SLOT = P3_A + P3_B, P3_LDS = 3 * P3_SLOT;
struct P3Ctx {
    const char* pA; const char* pB;
    const char* oA; const char* oB;
    u32 aStride;
    int tl, nst; u32 sl;
    u32 dA, dB;
    u32 vA, vB0, vB1, vB2;
    lcptr ab, bb;
};
__device__ __forceinline__ void glds_c2(int tl, int nst, const void* sbase, u32 voff, u32 lds_base) {
    u32 sv;
    asm volatile("s_cmp_ge_i32 %4, %5\n\ts_cbranch_scc1 1f\n\ts_mov_b32 %0, m0\n\ts_mov_b32 m0, %3\n\ts_nop 0\n\tglobal_load_lds_dwordx4 %1, %2 sc1\n\tglobal_load_lds_dwordx4 %1, %2 offset:1024 sc1\n\ts_mov_b32 m0, %0\n1:"
                 : "=&s"(sv) : "v"(voff), "s"(sbase), "s"(lds_base), "s"(tl), "s"(nst) : "memory", "scc");
}
__device__ __forceinline__ void glds_c1(int tl, int nst, const void* sbase, u32 voff, u32 lds_base) {
    u32 sv;
    asm volatile("s_cmp_ge_i32 %4, %5\n\ts_cbranch_scc1 1f\n\ts_mov_b32 %0, m0\n\ts_mov_b32 m0, %3\n\ts_nop 0\n\tglobal_load_lds_dwordx4 %1, %2\n\ts_mov_b32 m0, %0\n1:"
                 : "=&s"(sv) : "v"(voff), "s"(sbase), "s"(lds_base), "s"(tl), "s"(nst) : "memory", "scc");
}
__device__ __forceinline__ void glds_cq(int tl, int nst, const void* sbase, u32 voff, u32 lds_base) {
    u32 sv;
    asm volatile("s_cmp_ge_i32 %4, %5\n\ts_cbranch_scc1 1f\n\ts_mov_b32 %0, m0\n\ts_mov_b32 m0, %3\n\ts_mov_b64 exec, 0xffff\n\tglobal_load_lds_dwordx4 %1, %2\n\ts_mov_b64 exec, -1\n\ts_mov_b32 m0, %0\n1:"
                 : "=&s"(sv) : "v"(voff), "s"(sbase), "s"(lds_base), "s"(tl), "s"(nst) : "memory", "scc");
}
__device__ __forceinline__ void p3_load_part(P3Ctx& X, int k) {
    if (k == 0) glds_c2(X.tl, X.nst, X.pA, X.vA, X.dA + X.sl);
    if (k == 1) glds_c2(X.tl, X.nst, X.pA + 2048, X.vA, X.dA + X.sl + 2048);
    if (k == 2) glds_c1(X.tl, X.nst, X.pB, X.vB0, X.dB + X.sl);
    if (k == 3) glds_c1(X.tl, X.nst, X.pB, X.vB1, X.dB + X.sl + 1024);
    if (k == 4) { glds_cq(X.tl, X.nst, X.pB, X.vB2, X.dB + X.sl + 2048);
        X.tl += 1; const bool sw = X.tl == 9; X.pA = sw ? X.oA : X.pA + 4096; X.pB = sw ? X.oB : X.pB + 64 * 1152; X.sl = X.sl == 2 * P3_SLOT ? 0 : X.sl + P3_SLOT; }
}
struct P3Frag { bf16x8 h[4]; bf16x8 w[5]; };
__device__ __forceinline__ void p3_waitbar(int rem) {
    asm volatile("s_cmp_lt_i32 %0, 1\n\ts_cbranch_scc1 2f\n1:\ts_waitcnt vmcnt(7) lgkmcnt(0)\n\ts_barrier\n\t.subsection 1\n2:\ts_waitcnt vmcnt(0)\n\ts_branch 1b\n\t.subsection 0" :: "s"(rem) : "memory", "scc");
}
template <int NT, int HF, bool RD, bool MM>
__device__ __forceinline__ void p3_half(P3Ctx& X, int t, u32 so, P3Frag& R, const P3Frag& P, f32x4 (&acc)[4][5]) {
    lcptr a = X.ab + so + HF * 2048, b = X.bb + so + HF * (32 * 288);
    if (RD) { if (HF == 0) p3_waitbar(X.nst - 1 - t); PIN(a); PIN(b); }
    s16x4 lo;
    SBAR();
#pragma unroll
    for (int i = 0; i < 4 * NT; ++i) {
        if (MM) acc[i & 3][i >> 2] = MFMA16(P.w[i >> 2], P.h[i & 3], acc[i & 3][i >> 2]);
        if (RD) {
            if (i < 4) R.h[i] = *(const LAS bf16x8*)(a + (i >> 1) * 4096 + (i & 1) * 256);
            else if (i < 4 + 2 * NT) { const int j = i - 4; if (j & 1) R.w[j >> 1] = cat8(lo, trd(b + (j >> 1) * 32 + 8 * 288)); else lo = trd(b + (j >> 1) * 32); }
            if (HF == 0) { if (i == (NT == 5 ? 2 : 6)) p3_load_part(X, 0); if (i == (NT == 5 ? 10 : 13)) p3_load_part(X, 1); }
            else { if (i == (NT == 5 ? 1 : 4)) p3_load_part(X, 2); if (i == (NT == 5 ? 8 : 10)) p3_load_part(X, 3); if (i == (NT == 5 ? 15 : 14)) p3_load_part(X, 4); }
        }
        SBAR();
    }
}
template <int NT>
__device__ __forceinline__ void p3_loop(P3Ctx& X, int nhalf, f32x4 (&acc)[4][5], bool w0, gu64* fo, bool fo_use, u64 ep, gu64* own_o) {
    u64 fv = 0;
    WAIT_VM0(); BARRIER();
    if (threadIdx.x == 0) st_agent(own_o, ep);
    P3Frag F0, F1;
    u32 so = 0;
    p3_half<NT, 0, true, false>(X, 0, so, F0, F1, acc);
    p3_half<NT, 1, true, true>(X, 0, so, F1, F0, acc);
    int t = 1;
    for (; 2 * t + 1 < nhalf; ++t) {
        so = so == 2 * P3_SLOT ? 0 : so + P3_SLOT;
        if (w0) { if (t == 2) poll_issue(fv, fo); else if (t == 4) poll_check(fv, fo, fo_use, ep); }
        p3_half<NT, 0, true, true>(X, t, so, F0, F1, acc);
        p3_half<NT, 1, true, true>(X, t, so, F1, F0, acc);
    }
    if (2 * t < nhalf) {
        so = so == 2 * P3_SLOT ? 0 : so + P3_SLOT;
        p3_half<NT, 0, true, true>(X, t, so, F0, F1, acc);
        p3_half<NT, 1, false, true>(X, t, so, F1, F0, acc);
    } else p3_half<NT, 0, false, true>(X, t, so, F0, F1, acc);
}
__device__ __forceinline__ void p3_phase(const Args& A, char* smem, u64 ep) {
    STAMP_DECL STAMP(0);
    const int tid = threadIdx.x, lane = tid & 63, wid = RFL(tid >> 6);
    const int sp = blockIdx.x & 7, idx = blockIdx.x >> 3;
    const int mt = idx & 7, nt = idx >> 3;
    const u32 lds0 = (u32)(uintptr_t)smem;
    const int mq = wid >> 1, nh = (wid & 1) ^ (wid >> 2);
    const int i16 = lane & 15, kq = lane >> 4;
    P3Ctx X;
    const int no = sp < 2 ? 1 : (sp < 4 ? 2 : 3), o0 = sp < 2 ? sp : (sp < 4 ? 2 * sp - 2 : 3 * sp - 6);
    STAMP(4);
    const int nhalf = 18 + no; X.nst = (nhalf + 1) >> 1; X.tl = 0; X.sl = 0;
    X.pA = A.hb + (size_t)((mt * 8 + wid) * 144 + 18 * sp) * 2048;
    X.oA = A.ob + (size_t)((mt * 8 + wid) * 18 + o0) * 2048;
    X.pB = (const char*)A.w2 + (size_t)(576 * sp) * 1152 + 288 * nt;
    X.oB = (const char*)A.wo + (size_t)(32 * o0) * 1152 + 288 * nt;
    X.dA = lds0 + wid * 4096; X.dB = lds0 + P3_A + wid * 144 * 16;
    X.vA = lane * 16;
    { const int e0 = wid * 144 + lane, e1 = e0 + 64, e2 = e0 + 128;
      X.vB0 = (e0 / 18) * 1152 + (e0 % 18) * 16; X.vB1 = (e1 / 18) * 1152 + (e1 % 18) * 16; X.vB2 = (e2 / 18) * 1152 + (e2 % 18) * 16; }
    X.ab = (lcptr)(size_t)lds0 + (mq * 2) * 4096 + kq * 512 + i16 * 16;
    X.bb = (lcptr)(size_t)lds0 + P3_A + (16 * (kq >> 1) + 4 * (kq & 1) + (i16 >> 2)) * 288 + (nh ? 5 * 32 : 0) + (i16 & 3) * 8;
#pragma unroll
    for (int t = 0; t < 2; ++t)
#pragma unroll
        for (int k = 0; k < 5; ++k) p3_load_part(X, k);
    f32x4 acc[4][5];
#pragma unroll
    for (int m = 0; m < 4; ++m)
#pragma unroll
        for (int n = 0; n < 5; ++n) acc[m][n] = (f32x4){0.f, 0.f, 0.f, 0.f};
    STAMP(1);
    const int hp0 = (32 * o0) / 36, hp1 = (32 * (o0 + no) - 1) / 36, hp = hp0 + (lane >> 1);
    gu64* fo = flag_ptr(A.ctrl, F_O, attn_wg(2 * mt + (lane & 1), hp <= hp1 ? hp : hp0));
    if (nh == 0) p3_loop<5>(X, nhalf, acc, wid == 0, fo, hp <= hp1, ep, flag_ptr(A.ctrl, F_O, blockIdx.x)); else p3_loop<4>(X, nhalf, acc, wid == 0, fo, hp <= hp1, ep, flag_ptr(A.ctrl, F_O, blockIdx.x));
    STAMP(2);
    const u32 tile = (u32)(mt * 4 + nt) * 8 * (72 * 1024);
    const __amdgpu_buffer_rsrc_t prs = mkrsrc(A.part + tile, 8 * 72 * 1024);
    const int NTN = nh ? 4 : 5;
#pragma unroll
    for (int pr = 0; pr < 2; ++pr)
#pragma unroll
        for (int n = 0; n < 5; ++n)
            if (n < NTN) st16_sc1((u32x4){cvtpk_c(acc[2 * pr][n][0], acc[2 * pr][n][1]), cvtpk_c(acc[2 * pr][n][2], acc[2 * pr][n][3]),
                                          cvtpk_c(acc[2 * pr + 1][n][0], acc[2 * pr + 1][n][1]), cvtpk_c(acc[2 * pr + 1][n][2], acc[2 * pr + 1][n][3])}, prs,
                                  sp * (72 * 1024) + ((mq * 2 + pr) * 9 + (nh ? 5 : 0) + n) * 1024 + lane * 16);
    WAIT_VM0(); BARRIER();
    if (tid == 0) st_agent(flag_ptr(A.ctrl, F_PART, blockIdx.x), ep);
    STAMP(3);
    if (tid < 320) {
        const bool act = tid < 288;
        const int ck0 = act ? tid : 0;
        u32x2 bb[2]; float og[2][2];
#pragma unroll
        for (int r = 0; r < 2; ++r) { const int ck = ck0 + 288 * r, n = ck >> 6, ln = ck & 63;
            bb[r] = *(const u32x2*)(A.b2 + nt * 144 + n * 16 + (ln >> 4) * 4);
#pragma unroll
            for (int e = 0; e < 2; ++e) og[r][e] = 1.f - __uint_as_float(__builtin_amdgcn_raw_buffer_load_b32(mkrsrc(A.rowrec, S_ * 32), (mt * 256 + 32 * sp + 16 * e + (ln & 15)) * 32 + 28, 0, 16)); }
        poll_flags(flag_ptr(A.ctrl, F_PART, (lane & 7) + 8 * idx), lane < 8, ep);
        u32x4 pv[2][8];
#pragma unroll
        for (int r = 0; r < 2; ++r)
#pragma unroll
            for (int s = 0; s < 8; ++s) { const int ck = ck0 + 288 * r; pv[r][s] = ld16_sc1(prs, s * (72 * 1024) + (sp * 9 + (ck >> 6)) * 1024 + (ck & 63) * 16); }
#pragma unroll
        for (int r = 0; r < 2; ++r) {
            float v[8] = {0.f, 0.f, 0.f, 0.f, 0.f, 0.f, 0.f, 0.f};
#pragma unroll
            for (int s = 0; s < 8; ++s)
#pragma unroll
                for (int j = 0; j < 4; ++j) { v[2 * j] += bflo(pv[r][s][j]); v[2 * j + 1] += bfhi(pv[r][s][j]); }
            const int ck = ck0 + 288 * r, n = ck >> 6, ln = ck & 63;
#pragma unroll
            for (int e = 0; e < 2; ++e) {
                const int row = mt * 256 + 32 * sp + 16 * e + (ln & 15), col = nt * 144 + n * 16 + (ln >> 4) * 4;
                u32x2 o; o[0] = cvtpk(v[4 * e] + og[r][e] * bflo(bb[r][0]), v[4 * e + 1] + og[r][e] * bfhi(bb[r][0])); o[1] = cvtpk(v[4 * e + 2] + og[r][e] * bflo(bb[r][1]), v[4 * e + 3] + og[r][e] * bfhi(bb[r][1]));
                if (act) *(u32x2*)(A.out + (size_t)row * D_ + col) = o;
            }
        }
    }
    STAMP(5);
    STAMP_OUT(A.stamps + (1u << 17), 6);
}
constexpr int MEGA_LDS = 163840;
static_assert(P1_LDS <= MEGA_LDS && AT_LDS <= MEGA_LDS && P3_LDS <= MEGA_LDS, "LDS");
__global__ __launch_bounds__(512, 2) void mega_kernel(Args A) {
    extern __shared__ __attribute__((aligned(16))) char smem[];
    u64 ep;
    typedef __attribute__((address_space(4))) const u64* ArgP;
#if defined(__HIP_DEVICE_COMPILE__)
    ArgP ap = (ArgP)__builtin_amdgcn_kernarg_segment_ptr();
#else
    ArgP ap = nullptr;
#endif
#define LOAD_ARGS(a) Args a; { asm volatile("" : "+s"(ap)); u64 w_[17]; _Pragma("unroll") for (int i_ = 0; i_ < 17; ++i_) w_[i_] = ap[i_]; __builtin_memcpy(&a, w_, sizeof(Args)); }
    static_assert(sizeof(Args) == 17 * 8, "Args: 17 pointers");
    { LOAD_ARGS(a) p1_phase(a, smem, ep); }
    { LOAD_ARGS(a) attn_phase(a, smem, ep); }
    { LOAD_ARGS(a) p3_phase(a, smem, ep); }
    if (blockIdx.x == 0 && threadIdx.x < 64) {
        LOAD_ARGS(a) u64* ctrl = a.ctrl;
#pragma unroll
        for (int i = 0; i < 4; ++i) poll_flags(flag_ptr(ctrl, F_STAT, threadIdx.x + 64 * i), true, ep);
        if (threadIdx.x == 0) st_agent(epoch_ptr(ctrl), ep);
    }
}
extern "C" void kernel_launch(void* const* d_in, const int* in_sizes, int n_in, void* d_out, int out_size, void* d_ws, size_t ws_size, hipStream_t stream) {
    char* ws = (char*)d_ws;
    Args A;
    A.x = (const u16*)d_in[0]; A.wqkv = (const u16*)d_in[1]; A.wo = (const u16*)d_in[2]; A.w1 = (const u16*)d_in[4]; A.b1 = (const u16*)d_in[5];
    A.w2 = (const u16*)d_in[6]; A.b2 = (const u16*)d_in[7]; A.gw = (const u16*)d_in[8];
    A.rowrec = (u32x4*)ws;
    A.wext = (u32x4*)(ws + 65536);
    char* big = ws + 147456;
    A.qkvb = big;
    A.hb = big + 7077888;
    A.ob = big + 7077888 + 18874368;
    A.part = big + 7077888 + 18874368 + 2359296;
    A.ctrl = (u64*)(big + 7077888 + 18874368 + 2359296 + 18874368);
    A.out = (u16*)d_out;
    A.stamps = (unsigned long long*)(ws + (200u << 20));
    (void)hipFuncSetAttribute((const void*)mega_kernel, hipFuncAttributeMaxDynamicSharedMemorySize, MEGA_LDS);
    mega_kernel<<<256, 512, MEGA_LDS, stream>>>(A);
}
```

```cpp
#include <hip/hip_runtime.h>
#include <hip/hip_bf16.h>
typedef unsigned short u16;
typedef unsigned int u32;
#define LAS __attribute__((address_space(3)))
typedef short bf16x8 __attribute__((ext_vector_type(8)));
typedef short s16x4 __attribute__((ext_vector_type(4)));
typedef float f32x16 __attribute__((ext_vector_type(16)));
typedef float f32x4 __attribute__((ext_vector_type(4)));
typedef u32 u32x4 __attribute__((ext_vector_type(4)));
typedef u32 u32x2 __attribute__((ext_vector_type(2)));
typedef LAS char* lptr;
typedef LAS const char* lcptr;

constexpr int S_ = 2048, D_ = 576, H_ = 32, DH_ = 18, F_ = 4608, NQKV_ = 1728;

__device__ __forceinline__ float bf2f(u16 b) { return __uint_as_float(((u32)b) << 16); }
__device__ __forceinline__ float bflo(u32 w) { return __uint_as_float(w << 16); }
__device__ __forceinline__ float bfhi(u32 w) { return __uint_as_float(w & 0xffff0000u); }
typedef __bf16 bf16x2_t __attribute__((ext_vector_type(2)));
typedef float f32x2 __attribute__((ext_vector_type(2)));
__device__ __forceinline__ u32 cvtpk_c(float lo, float hi) { return __builtin_bit_cast(u32, __builtin_convertvector((f32x2){lo, hi}, bf16x2_t)); }
__device__ __forceinline__ u32 cvtpk(float lo, float hi) { u32 r; asm("v_cvt_pk_bf16_f32 %0, %1, %2" : "=v"(r) : "v"(lo), "v"(hi)); return r; }
__device__ __forceinline__ u16 f2bf(float f) { return (u16)(cvtpk(f, f) & 0xffffu); }
__device__ __forceinline__ void glds16(const void* g, u32 lds_base) {
    u32 sv;
    asm volatile("s_mov_b32 %0, m0\n\ts_mov_b32 m0, %2\n\ts_nop 0\n\tglobal_load_lds_dwordx4 %1, off\n\ts_mov_b32 m0, %0" : "=&s"(sv) : "v"(g), "s"(lds_base) : "memory");
}
__device__ __forceinline__ void glds16s(const void* sbase, u32 voff, u32 lds_base) {
    u32 sv;
    asm volatile("s_mov_b32 %0, m0\n\ts_mov_b32 m0, %3\n\ts_nop 0\n\tglobal_load_lds_dwordx4 %1, %2\n\ts_mov_b32 m0, %0" : "=&s"(sv) : "v"(voff), "s"(sbase), "s"(lds_base) : "memory");
}
__device__ __forceinline__ void glds16s_off1k(const void* sbase, u32 voff, u32 lds_base) {
    u32 sv;
    asm volatile("s_mov_b32 %0, m0\n\ts_mov_b32 m0, %3\n\ts_nop 0\n\tglobal_load_lds_dwordx4 %1, %2\n\tglobal_load_lds_dwordx4 %1, %2 offset:1024\n\ts_mov_b32 m0, %0" : "=&s"(sv) : "v"(voff), "s"(sbase), "s"(lds_base) : "memory");
}
__device__ __forceinline__ void glds16s_off1k_sc1(const void* sbase, u32 voff, u32 lds_base) {
    u32 sv;
    asm volatile("s_mov_b32 %0, m0\n\ts_mov_b32 m0, %3\n\ts_nop 0\n\tglobal_load_lds_dwordx4 %1, %2 sc1\n\tglobal_load_lds_dwordx4 %1, %2 offset:1024 sc1\n\ts_mov_b32 m0, %0" : "=&s"(sv) : "v"(voff), "s"(sbase), "s"(lds_base) : "memory");
}
__device__ __forceinline__ s16x4 trd(lcptr p) {
    typedef short v4i16_t __attribute__((ext_vector_type(4)));
    return __builtin_bit_cast(s16x4, __builtin_amdgcn_ds_read_tr16_b64_v4i16((LAS v4i16_t*)p));
}
__device__ __forceinline__ bf16x8 cat8(s16x4 a, s16x4 b) { return (bf16x8){a[0], a[1], a[2], a[3], b[0], b[1], b[2], b[3]}; }
#define MFMA32(a, b, c) __builtin_amdgcn_mfma_f32_32x32x16_bf16(a, b, c, 0, 0, 0)
#define MFMA16(a, b, c) __builtin_amdgcn_mfma_f32_16x16x32_bf16(a, b, c, 0, 0, 0)
#define SBAR() __builtin_amdgcn_sched_barrier(0)
#define PIN(x) asm volatile("" : "+v"(x))
#define RFL(x) __builtin_amdgcn_readfirstlane(x)

#define STAMP_DECL
#define STAMP(i)
#define STAMP_OUT(buf, n)

typedef unsigned long long u64;
typedef __attribute__((address_space(1))) u64 gu64;
constexpr int FLAG_STRIDE = 16;
constexpr int F_STAT = 0, F_QKV = 1, F_H = 2, F_O = 3, F_PART = 4, F_KINDS = 5;
constexpr size_t CTRL_BYTES = (size_t)(F_KINDS * 256 + 1) * FLAG_STRIDE * 8;
__device__ __forceinline__ gu64* flag_ptr(u64* ctrl, int kind, int wg) { return (gu64*)(ctrl + (size_t)(kind * 256 + wg) * FLAG_STRIDE); }
__device__ __forceinline__ gu64* epoch_ptr(u64* ctrl) { return (gu64*)(ctrl + (size_t)(F_KINDS * 256) * FLAG_STRIDE); }
__device__ __forceinline__ u64 ld_agent(gu64* p) { return __hip_atomic_load(p, __ATOMIC_RELAXED, __HIP_MEMORY_SCOPE_AGENT); }
__device__ __forceinline__ void st_agent(gu64* p, u64 v) { __hip_atomic_store(p, v, __ATOMIC_RELAXED, __HIP_MEMORY_SCOPE_AGENT); }
__device__ __forceinline__ void poll_flags(gu64* mine, bool use, u64 ep) {
    const unsigned long long t0 = __builtin_amdgcn_s_memrealtime();
    for (;;) {
        const u64 v = use ? ld_agent(mine) : ep;
        if (__all(v == ep)) break;
        if (__builtin_amdgcn_s_memrealtime() - t0 > 5000000ull) break;
        __builtin_amdgcn_s_sleep(1);
    }
}
__device__ __forceinline__ void poll_issue(u64& v, gu64* p) { asm volatile("global_load_dwordx2 %0, %1, off sc1" : "=v"(v) : "v"(p) : "memory"); }
__device__ __forceinline__ void poll_check(u64& v, gu64* p, bool use, u64 ep) { asm volatile("" : "+v"(v)); if (!__all(!use || v == ep)) poll_flags(p, use, ep); }
__device__ __forceinline__ void st16_sc1(u32x4 v, __amdgpu_buffer_rsrc_t rs, u32 voff) { __builtin_amdgcn_raw_buffer_store_b128(v, rs, voff, 0, 16); }
__device__ __forceinline__ u32x4 ld16_sc1(__amdgpu_buffer_rsrc_t rs, u32 voff) { return __builtin_amdgcn_raw_buffer_load_b128(rs, voff, 0, 16); }
__device__ __forceinline__ __amdgpu_buffer_rsrc_t mkrsrc(const void* p, u32 bytes) { return __builtin_amdgcn_make_buffer_rsrc((void*)p, 0, bytes, 0x00020000); }
#define INV_AGENT() asm volatile("buffer_inv sc1" ::: "memory")
#define WAIT_VM0() asm volatile("s_waitcnt vmcnt(0)" ::: "memory")
#define BARRIER() asm volatile("s_waitcnt lgkmcnt(0)\n\ts_barrier" ::: "memory")
struct Args {
    const u16* x; const u16* wqkv; const u16* wo; const u16* w1; const u16* b1; const u16* w2; const u16* b2; const u16* gw;
    u64* ctrl; u32x4* rowrec; u32x4* wext; char* qkvb; char* hb; char* ob; char* part; u16* out; unsigned long long* stamps;
};
constexpr int P1_SLOT = 24576, P1_RING = 6 * P1_SLOT, P1_WEXT = P1_RING, P1_ROW = P1_WEXT + 288 * 16 + 64  , P1_CS = P1_ROW + 128 * 32  , P1_LDS = P1_CS + 1024 + 256  ;
constexpr float GC0 = -2.f * 0.7978845608028654f * 1.4426950408889634f, GC1 = GC0 * 0.044715f;
__device__ __forceinline__ u32 bfbits(float f) { return cvtpk(f, 0.f) & 0xffffu; }
constexpr float QSCALE = 0.23570226039551584f * 1.4426950408889634f;
#define P1_WAITBAR(N) asm volatile("s_waitcnt vmcnt(" #N ") lgkmcnt(0)\n\ts_barrier" ::: "memory")

struct P1Ctx {
    const char* w1; __amdgpu_buffer_rsrc_t hrs;
    int c, nch, g1first; u32 l16;
    const char* lb; const char* la;
    u32 ld, ldst0;
    u32 voffW[3];
    lcptr trbase, wexb; u32 wexs; u32 rs;
    lcptr xeb, rcb;
};
constexpr u32 P1_PART = 192u * F_ * 2u;
__device__ __forceinline__ int p1_w1g(const P1Ctx& X, int i) { return i < 6 ? X.g1first + 2 * (i - 2) : ((i == 6 && X.nch == 7) ? ((X.c & 1) ? 9 * X.c : 9 * X.c + 8) : X.g1first); }
__device__ __forceinline__ void p1_piece_a(const P1Ctx& X, int k) { asm volatile("s_add_u32 m0, %0, %1" :: "s"(X.ld), "n"(k * 8192) : "memory", "scc"); }
__device__ __forceinline__ void p1_piece_b(const P1Ctx& X, int k) {
    asm volatile("global_load_lds_dwordx4 %0, %1" :: "v"(X.voffW[k]), "s"(X.la) : "memory");
}
__device__ __forceinline__ void p1_piece(const P1Ctx& X, int k) { p1_piece_a(X, k); asm volatile("s_nop 0"); p1_piece_b(X, k); }
__device__ __forceinline__ void p1_top(P1Ctx& X, int i, int j) {
    if (j == 0) asm volatile("s_cmp_le_u32 %0, 2\n\ts_cbranch_scc1 3f\n\ts_waitcnt vmcnt(12) lgkmcnt(0)\n\ts_barrier\n3:" :: "s"(i) : "memory", "scc");
    else asm volatile("s_waitcnt vmcnt(12) lgkmcnt(0)\n\ts_barrier" ::: "memory");
    X.ld = X.ldst0 + X.rs;
    X.rs = X.rs == 5 * P1_SLOT ? 0 : X.rs + P1_SLOT;
    if (j == 0) X.la = X.lb + 2 * P1_PART;
    if (j == 1) { X.lb = X.w1 + p1_w1g(X, i + 2) * 64; X.la = X.lb; }
    if (j == 2) X.la = X.lb + P1_PART;
}
__device__ __forceinline__ void p1_top3(P1Ctx& X) {
    X.lb = X.w1 + p1_w1g(X, 2) * 64;
    X.la = X.lb; X.ld = X.ldst0; p1_piece(X, 0); p1_piece(X, 1); p1_piece(X, 2);
    X.la = X.lb + P1_PART; X.ld = X.ldst0 + P1_SLOT; p1_piece(X, 0); p1_piece(X, 1); p1_piece(X, 2);
    X.rs = 2 * P1_SLOT;
}
struct Epi { float a0, a1, u0, u1, c0, c1, sd; u32 pw[4]; };
__device__ __forceinline__ void p1_gelu_slot(int s, const P1Ctx& X, const f32x16& pa, Epi& E, u32 vo) {
    const int k = s >> 2, ph = s & 3;
    if (ph == 0) { E.a0 = pa[2 * k]; E.a1 = pa[2 * k + 1]; E.u0 = __builtin_fmaf(E.a0 * E.a0, E.c1, E.c0); E.u1 = __builtin_fmaf(E.a1 * E.a1, E.c1, E.c0); PIN(E.u0); PIN(E.u1); }
    if (ph == 1) { E.u0 = __builtin_amdgcn_exp2f(E.a0 * E.u0); E.u1 = __builtin_amdgcn_exp2f(E.a1 * E.u1); PIN(E.u0); PIN(E.u1); }
    if (ph == 2) { E.u0 = __builtin_amdgcn_rcpf(__builtin_fmaf(E.u0, E.sd, E.sd)); E.u1 = __builtin_amdgcn_rcpf(__builtin_fmaf(E.u1, E.sd, E.sd)); PIN(E.u0); PIN(E.u1); }
    if (ph == 3) { E.pw[k & 3] = cvtpk(E.a0 * E.u0, E.a1 * E.u1);
        if ((k & 3) == 3) __builtin_amdgcn_raw_buffer_store_b128((u32x4){E.pw[0], E.pw[1], E.pw[2], E.pw[3]}, X.hrs, vo + (k >> 2) * 1024, 0, 16); }
}
__device__ __forceinline__ void p1_consts(const P1Ctx& X, Epi& E) { const f32x4 k = *(const LAS f32x4*)X.rcb; E.c0 = GC0 * k[0]; E.c1 = GC1 * k[0] * k[0] * k[0]; E.sd = k[1]; }
struct P1Tab { __amdgpu_buffer_rsrc_t r0, r1; u32 o0, o1; u32x4 t0, t1; };
__device__ __forceinline__ void p1_tab_issue(P1Tab& T) {
    asm volatile("buffer_load_dwordx4 %0, %2, %3, 0 offen sc1\n\tbuffer_load_dwordx4 %1, %4, %5, 0 offen sc1" : "=&v"(T.t0), "=&v"(T.t1) : "v"(T.o0), "s"(T.r0), "v"(T.o1), "s"(T.r1) : "memory");
}
template <bool FIRST>
__device__ __forceinline__ void p1_w1_chunk(P1Ctx& X, int i, int pg, bool st, const bf16x8 (&xf)[36], bf16x8 (&wf)[6], f32x16& acc, f32x16& pacc, P1Tab& T) {
    const u32 vo = st ? X.l16 + (u32)pg * 2048 : 0x40000000u;
    Epi E; bf16x8 we, xe;
    lcptr p = X.trbase;
    SBAR();
#pragma unroll
    for (int s = 0; s < 36; ++s) {
        if (s == 0 || s == 12 || s == 24) { p1_top(X, i, s / 12); p = X.trbase + X.rs; PIN(p); }
        if (FIRST && s == 24) p1_tab_issue(T);
        if (s < 4) { pacc = MFMA32(wf[(s + 2) % 6], xf[32 + s], pacc); PIN(pacc); }
        if (s == 4) { pacc = MFMA32(we, xe, pacc); PIN(pacc); f32x16 z = {}; acc = MFMA32(wf[0], xf[0], z); PIN(acc); }
        if (s > 4) { acc = MFMA32(wf[(s - 4) % 6], xf[s - 4], acc); PIN(acc); }
        if (s % 4 == 2) p1_piece_a(X, (s % 12) >> 2);
        if (s == 1) { we = *(const LAS bf16x8*)(X.wexb + (st ? pg - 9 * X.c : 0) * X.wexs); xe = *(const LAS bf16x8*)X.xeb; }
        if (s == 3) p1_consts(X, E);
        if ((s & 1) == 0) {
            wf[(s + 1) % 6] = cat8(trd(p + ((s + 1) % 12) * 2048), trd(p + ((s + 1) % 12) * 2048 + 512));
            wf[s % 6] = cat8(trd(p + (s % 12) * 2048), trd(p + (s % 12) * 2048 + 512));
        }
        if (s % 4 == 2) p1_piece_b(X, (s % 12) >> 2);
        if (s >= 6) p1_gelu_slot(s - 6, X, pacc, E, vo);
        SBAR();
    }
    p1_gelu_slot(30, X, pacc, E, vo); p1_gelu_slot(31, X, pacc, E, vo);
}
template <bool SWAP, int J, bool LD>
__device__ __forceinline__ void p1_qkv_stage(P1Ctx& X, lcptr p, const bf16x8 (&xf)[36], f32x16& acc) {
    bf16x8 wf[4];
    PIN(p);
#pragma unroll
    for (int s = 0; s < 12 + 3; ++s) {
        if (s < 12) wf[s & 3] = cat8(trd(p + s * 2048), trd(p + s * 2048 + 512));
        if (s >= 3) { const int u = s - 3;
            if (J == 0 && u == 0) { f32x16 z = {}; acc = SWAP ? MFMA32(xf[0], wf[0], z) : MFMA32(wf[0], xf[0], z); }
            else acc = SWAP ? MFMA32(xf[J * 12 + u], wf[u & 3], acc) : MFMA32(wf[u & 3], xf[J * 12 + u], acc); }
        if (LD && (s == 4 || s == 8 || s == 12)) p1_piece(X, (s >> 2) - 1);
        SBAR();
    }
}
__device__ __forceinline__ void p1_qkv_store(__amdgpu_buffer_rsrc_t rs, u32 off, int lane, const f32x16& a, float sc) {
    u32 w[8];
#pragma unroll
    for (int k = 0; k < 8; ++k) w[k] = cvtpk(a[2 * k] * sc, a[2 * k + 1] * sc);
    st16_sc1((u32x4){w[0], w[1], w[2], w[3]}, rs, off + lane * 16);
    st16_sc1((u32x4){w[4], w[5], w[6], w[7]}, rs, off + 1024 + lane * 16);
}

__device__ __forceinline__ int p1_wg(int rb, int c) { return ((rb >> 3) | ((c >> 2) << 1)) + 8 * ((rb & 7) | ((c & 3) << 3)); }
__device__ __forceinline__ float dpp_ror4(float v) { return __builtin_bit_cast(float, __builtin_amdgcn_update_dpp(0, __builtin_bit_cast(int, v), 0x124, 0xf, 0xf, false)); }
__device__ __forceinline__ float dpp_ror8(float v) { return __builtin_bit_cast(float, __builtin_amdgcn_update_dpp(0, __builtin_bit_cast(int, v), 0x128, 0xf, 0xf, false)); }
__device__ __forceinline__ float xadd16(float v) { auto r = __builtin_amdgcn_permlane16_swap(__float_as_uint(v), __float_as_uint(v), false, false); return __uint_as_float(r[0]) + __uint_as_float(r[1]); }
__device__ __forceinline__ float xadd32(float v) { auto r = __builtin_amdgcn_permlane32_swap(__float_as_uint(v), __float_as_uint(v), false, false); return __uint_as_float(r[0]) + __uint_as_float(r[1]); }
template <int CTRL> __device__ __forceinline__ float dpp_mov(float v) { return __builtin_bit_cast(float, __builtin_amdgcn_update_dpp(0, __builtin_bit_cast(int, v), CTRL, 0xf, 0xf, false)); }
__device__ __forceinline__ float wave_sum(float v) {
    v += dpp_mov<0x121>(v); v += dpp_mov<0x122>(v); v += dpp_mov<0x124>(v); v += dpp_mov<0x128>(v);
    return xadd32(xadd16(v));
}
#define LD16A(dst, voff, sbase) asm volatile("global_load_dwordx4 %0, %1, %2" : "=v"(dst) : "v"(voff), "s"(sbase) : "memory")

__device__ __forceinline__ int p1_qkv_owner(int G) { return G < 24 ? G >> 2 : 6 + (G - 24) / 3; }
__device__ __forceinline__ gu64* attn_flag(const Args& A, int lane) {
    const int xcd = blockIdx.x & 7, idx = blockIdx.x >> 3, qb = idx >> 1, hp = xcd + 8 * (idx & 1), cg0 = (36 * hp) >> 5;
    const int gi = lane / 3, rr = lane - 3 * gi; int rbx = qb - rr; if (rbx < 0) rbx = 0;
    return flag_ptr(A.ctrl, F_QKV, p1_wg(rbx, p1_qkv_owner((lane < 18 ? (gi >> 1) * 18 + cg0 + (gi & 1) : 0))));
}
constexpr int AT_K = 0, AT_V = 12 * 4096, AT_Q = 24 * 4096, AT_LDS = AT_Q + 4 * 4096;
__device__ __forceinline__ void attn_stage(const Args& A, u32 lds0, int wid, int lane) {
    const int xcd = blockIdx.x & 7, idx = blockIdx.x >> 3, qb = idx >> 1, hp = xcd + 8 * (idx & 1), cg0 = (36 * hp) >> 5;
#pragma unroll
    for (int i = 0; i < 7; ++i) {
        const int p = wid + 8 * i;
        const int u = p >> 1, gsel = p & 1;
        const int sec = u < 12 ? 1 : (u < 24 ? 2 : 0), ub = u < 12 ? u : (u < 24 ? u - 12 : u - 24);
        int rb = qb * 4 + ub - (sec ? 8 : 0); if (rb < 0) rb = 0;
        glds16s_off1k_sc1(A.qkvb + (size_t)(rb * 54 + sec * 18 + cg0 + gsel) * 2048, lane * 16, lds0 + u * 4096 + gsel * 2048);
    }
}
__device__ __forceinline__ void p1_phase(const Args& A, char* smem, u64& ep) {
    STAMP_DECL STAMP(0);
    const int tid = threadIdx.x, lane = tid & 63, wid = RFL(tid >> 6);
    const int xcd = blockIdx.x & 7, idx = blockIdx.x >> 3;
    const int rb = (xcd & 1) * 8 + (idx & 7), c = (xcd >> 1) * 4 + (idx >> 3);
    const u32 lds0 = (u32)(uintptr_t)smem;
    const int r8 = lane >> 3, cp = lane & 7;
    const int csw = cp ^ (((r8 >> 1) & 1) << 2);
    int gq0, gq1, gs;
    if (c < 6) { gq0 = 4 * c; gq1 = 4 * c + 2; gs = 0; } else { const int q0 = 24 + 3 * (c - 6), odd = (c - 6) & 1; gq0 = q0 + odd; gq1 = 0; gs = odd ? q0 : q0 + 2; }
    const u32 voffQ = (u32)(wid * 8 + r8) * (NQKV_ * 2) + csw * 16;
    const char* wq0 = (const char*)A.wqkv + gq0 * 64;
    const char* xs = (const char*)A.x + (size_t)rb * 128 * (D_ * 2);
    u32 voffX[2];
#pragma unroll
    for (int i = 0; i < 2; ++i) { const int row = 16 * wid + 8 * i + r8; voffX[i] = (u32)row * (D_ * 2) + ((cp ^ ((row >> 1) & 7)) << 4); }
    const u32 XA = lds0 + P1_SLOT, XB = lds0 + 4 * P1_SLOT;
#define P1_LDQ(sb, j, slot) _Pragma("unroll") for (int k = 0; k < 3; ++k) glds16s((sb) + (size_t)((j) * 192 + k * 64) * (NQKV_ * 2), voffQ, lds0 + (slot) * P1_SLOT + wid * 1024 + k * 8192)
#define P1_LDX(piece, dst) _Pragma("unroll") for (int i = 0; i < 2; ++i) glds16s(xs + (piece) * 128, voffX[i], (dst) + (16 * wid + 8 * i) * 128)
    P1_LDQ(wq0, 0, 0);
    P1_LDX(0, XA); P1_LDX(1, XA + 16384); P1_LDX(2, XA + 32768);
    if (tid < 4) *(LAS u32x4*)((lptr)(size_t)lds0 + P1_WEXT + 288 * 16 + tid * 16) = (u32x4){0u, 0u, 0u, 0u};
    const int rg = wid & 3, ch = wid >> 2;
    P1Ctx X; X.w1 = (const char*)A.w1; X.hrs = mkrsrc(A.hb + (size_t)(rb * 4 + rg) * 144 * 2048, 144 * 2048); X.c = c; X.l16 = lane * 16;
    const int nch = c < 6 ? 7 : 6; X.nch = nch; X.g1first = 9 * c + (c & 1);
    const int m = lane & 31, h = lane >> 5;
#pragma unroll
    for (int k = 0; k < 3; ++k) X.voffW[k] = (u32)(wid * 8 + r8 + 64 * k) * (F_ * 2) + csw * 16;
    X.ldst0 = lds0 + wid * 1024; X.rs = 0; X.ld = X.ldst0; X.lb = X.w1; X.la = X.w1;
    { const int s16 = (lane >> 4) & 1, q = (lane & 15) >> 2, p = lane & 3;
      X.trbase = (lcptr)(size_t)lds0 + (8 * h + q) * 128 + (((4 * ch + 2 * s16 + (p >> 1)) ^ (((q >> 1) & 1) << 2)) * 16) + (p & 1) * 8;
      X.wexb = (lcptr)(size_t)lds0 + (h ? P1_WEXT + 288 * 16 : P1_WEXT + m * 16); X.wexs = h ? 0 : 512; }
    const int wsingle = (c & 1) ? 9 * c : 9 * c + 8;
    bf16x8 xf[36];
    f32x16 accA, accB;
    const bool swp = c >= 10;
    const int xrow = rg * 32 + m, xz = (xrow >> 1) & 7;
    X.rcb = (lcptr)(size_t)lds0 + P1_ROW + xrow * 32 + 16;
    X.xeb = h ? (lcptr)(size_t)lds0 + P1_WEXT + 288 * 16 : X.rcb - 16;
    lcptr xa = (lcptr)(size_t)XA + xrow * 128, xb = (lcptr)(size_t)XB + xrow * 128;
#define P1_XF(s0, base) _Pragma("unroll") for (int s = 0; s < 12; ++s) xf[s0 + s] = *(const LAS bf16x8*)((base) + (s >> 2) * 16384 + (((2 * (s & 3) + h) ^ xz) << 4))
#define P1_QS(J, LD, P) do { if (swp) p1_qkv_stage<true, J, LD>(X, P, xf, accA); else p1_qkv_stage<false, J, LD>(X, P, xf, accA); } while (0)
    STAMP(1);
    P1_WAITBAR(0);
    STAMP(2);
    P1_LDQ(wq0, 1, 3);
    P1_LDX(3, XB); P1_LDX(4, XB + 16384); P1_LDX(5, XB + 32768);
    P1_XF(0, xa);
    BARRIER();
    P1_LDX(6, XA); P1_LDX(7, XA + 16384); P1_LDX(8, XA + 32768);
    const int srow = rb * 128 + c * 8 + wid;
    u32x4 sx0, sx1, sg0, sg1, sw[5]; u64 e0; u32 sb1;
    { const u32 o0 = lane * 16, o1 = 1024 + (lane & 7) * 16;
      const char* xr = (const char*)A.x + (size_t)srow * (D_ * 2);
      LD16A(sx0, o0, xr); LD16A(sx1, o1, xr); LD16A(sg0, o0, (const char*)A.gw); LD16A(sg1, o1, (const char*)A.gw);
      const char* wc = (const char*)A.w1 + (size_t)(72 * wid) * (F_ * 2) + (9 * c + (rb < 9 ? rb : 0)) * 64;
#pragma unroll
      for (int i = 0; i < 5; ++i) { int r = (lane >> 2) + 16 * i; if (r > 71) r = 71; const u32 o = (u32)r * (F_ * 2) + (lane & 3) * 16; LD16A(sw[i], o, wc); }
      asm volatile("global_load_ushort %0, %1, %2" : "=v"(sb1) : "v"((u32)(lane & 31) * 2), "s"((const char*)A.b1 + (9 * c + (rb < 9 ? rb : 0)) * 64) : "memory");
      asm volatile("global_load_dwordx2 %0, %1, %2 sc1" : "=v"(e0) : "v"(0u), "s"(epoch_ptr(A.ctrl)) : "memory"); }
    {
        const char* wa = c < 6 ? (const char*)A.wqkv + gq1 * 64 : (const char*)A.wqkv + gs * 64; const char* wb = (const char*)A.w1 + ((c & 1) ? 9 * c : 9 * c + 8) * 64;
        const char* pb = xs; u32 po = 0; bool pu = false;
        if (wid < 2) { const int k = wid * 64 + lane, l = (c & 3) * 96 + k; pu = k < 96; po = (u32)(l / 3) * (D_ * 2) + (6 + l % 3) * 128; }
        else if (wid == 2) { pb = wq0; pu = lane < 24; po = (u32)(384 + (rb & 7) * 24 + lane) * (NQKV_ * 2); }
        else if (wid < 5) { const int k = (wid - 3) * 64 + lane; pb = wa; pu = k < 72; po = (u32)((rb & 7) * 72 + k) * (NQKV_ * 2); }
        else if (wid < 7) { const int k = (wid - 5) * 64 + lane; pb = wb; pu = k < 72 && c >= 6; po = (u32)((rb & 7) * 72 + k) * (F_ * 2); }
        asm volatile("s_mov_b32 m0, %0" :: "s"(lds0 + P1_CS + 1024) : "memory");
        if (pu) asm volatile("s_nop 0\n\tglobal_load_lds_dword %0, %1" :: "v"(po), "s"(pb) : "memory");
    }
    P1_QS(0, false, X.trbase);
    STAMP(3);
    P1_WAITBAR(17);
    STAMP(4);
    P1_LDQ(wq0, 2, 0);
    P1_XF(12, xb);
    P1_QS(1, false, X.trbase + 3 * P1_SLOT);
    STAMP(5);
    P1_WAITBAR(3);
    STAMP(6);
    if (c < 6) { const char* wq1 = (const char*)A.wqkv + gq1 * 64; P1_LDQ(wq1, 0, 3); P1_LDQ(wq1, 1, 4); P1_LDQ(wq1, 2, 5); }
    else {
        const bool hi = csw >= 4; const u32 pitch = hi ? F_ * 2 : NQKV_ * 2;
        const char* lp = (hi ? (const char*)A.w1 + wsingle * 64 : (const char*)A.wqkv + gs * 64) + (size_t)((u32)(wid * 8 + r8) * pitch) + (csw & 3) * 16;
#pragma unroll
        for (int k = 0; k < 9; ++k) { glds16(lp, lds0 + 3 * P1_SLOT + wid * 1024 + k * 8192); lp += pitch * 64; }
    }
    asm volatile("" : "+v"(sx0), "+v"(sx1), "+v"(sg0), "+v"(sg1), "+v"(sw[0]), "+v"(sw[1]), "+v"(sw[2]), "+v"(sw[3]), "+v"(sw[4]), "+v"(e0), "+v"(sb1) :: "memory");
    ep = ((u64)(u32)RFL((u32)(e0 >> 32)) << 32 | (u32)RFL((u32)e0)) + 1;
    {
        typedef __bf16 bf2 __attribute__((ext_vector_type(2)));
        const bool two = lane < 8;
        const bf2 one2 = __builtin_bit_cast(bf2, 0x3f803f80u);
        float s = 0.f, gsum = 0.f, q2 = 0.f;
#pragma unroll
        for (int j = 0; j < 8; ++j) { const u32 xw = j < 4 ? sx0[j] : (two ? sx1[j - 4] : 0u), gw = j < 4 ? sg0[j] : sg1[j - 4]; const bf2 xv = __builtin_bit_cast(bf2, xw);
            s = __builtin_amdgcn_fdot2_f32_bf16(xv, one2, s, false); gsum = __builtin_amdgcn_fdot2_f32_bf16(xv, __builtin_bit_cast(bf2, gw), gsum, false); q2 = __builtin_amdgcn_fdot2_f32_bf16(xv, xv, q2, false); }
        s = wave_sum(s); gsum = wave_sum(gsum); q2 = wave_sum(q2);
        const float m = s * (1.f / 576.f);
        const float q = fmaxf(q2 - 576.f * m * m, 0.f);
        const float sd = __builtin_amdgcn_sqrtf(q * (1.f / 576.f) + 1e-5f), r = __builtin_amdgcn_rcpf(sd);
        const float eg = __builtin_amdgcn_exp2f(-1.4426950408889634f * gsum), gate = __builtin_amdgcn_rcpf(1.f + eg);
        const float sdg = sd * (1.f + __builtin_amdgcn_exp2f(1.4426950408889634f * gsum));
        const u32 mh = bfbits(-m), ml = bfbits(-m - __uint_as_float(mh << 16)), sh = bfbits(sd), sl = bfbits(sd - __uint_as_float(sh << 16));
        const u32x4 rec = lane == 0 ? (u32x4){mh | (mh << 16), ml | (sh << 16), sl, (u32)ep} : (u32x4){__float_as_uint(r), __float_as_uint(sdg), __float_as_uint(gate), (u32)ep};
        if (lane < 2) st16_sc1(rec, mkrsrc(A.rowrec, S_ * 32), srow * 32 + lane * 16);
        if (rb < 9) {
        float cs[8];
#pragma unroll
        for (int j = 0; j < 8; ++j) cs[j] = 0.f;
#pragma unroll
        for (int i = 0; i < 5; ++i)
#pragma unroll
            for (int j = 0; j < 4; ++j) { const u32 wv = (i < 4 || lane < 32) ? sw[i][j] : 0u; cs[2 * j] += bflo(wv); cs[2 * j + 1] += bfhi(wv); }
#pragma unroll
        for (int j = 0; j < 8; ++j) { float v = cs[j]; v += dpp_ror4(v); v += dpp_ror8(v); cs[j] = xadd32(xadd16(v)); }
        if (lane < 4) { LAS f32x4* d = (LAS f32x4*)((lptr)(size_t)lds0 + P1_CS + wid * 128 + lane * 32); d[0] = (f32x4){cs[0], cs[1], cs[2], cs[3]}; d[1] = (f32x4){cs[4], cs[5], cs[6], cs[7]}; }
        }
    }
    P1_XF(24, xa);
    P1_WAITBAR(9);
    if (wid == 0 && lane < 32 && rb < 9) {
        float t = 0.f;
#pragma unroll
        for (int w = 0; w < 8; ++w) t += *(const LAS float*)((lcptr)(size_t)lds0 + P1_CS + w * 128 + lane * 4);
        const int col = (9 * c + rb) * 32 + lane;
        const u32 chh = bfbits(t), cl = bfbits(t - __uint_as_float(chh << 16)), bb = sb1;
        st16_sc1((u32x4){chh | (cl << 16), chh | (bb << 16), bb, (u32)ep}, mkrsrc(A.wext, F_ * 16), col * 16);
    }
    P1_QS(2, false, X.trbase);
    const __amdgpu_buffer_rsrc_t qrs = mkrsrc(A.qkvb + (size_t)(rb * 4 + rg) * 54 * 2048, 54 * 2048);
    p1_qkv_store(qrs, (gq0 + ch) * 2048, lane, accA, gq0 + ch < 18 ? QSCALE : 1.f);
    STAMP(7);
    P1_WAITBAR(0);
    if (tid == 0) st_agent(flag_ptr(A.ctrl, F_STAT, blockIdx.x), ep);
    p1_top3(X);
    {
        const char* w2s = (const char*)A.w2 + (size_t)(576 * xcd) * 1152 + (size_t)idx * 20736;
        const int l = wid * 64 + lane;
        u64 pc; asm volatile("s_getpc_b64 %0" : "=s"(pc));
        asm volatile("s_mov_b32 m0, %0" :: "s"(lds0 + P1_CS) : "memory");
        if (l < 162) asm volatile("s_nop 0\n\tglobal_load_lds_dword %0, %1" :: "v"((u32)l * 128), "s"(w2s) : "memory");
        if (l < 21) asm volatile("s_nop 0\n\tglobal_load_lds_dword %0, %1" :: "v"((u32)l * 128), "s"((const char*)A.wo + (size_t)blockIdx.x * 2592) : "memory");
        if (wid == 7 && lane < 8) asm volatile("s_nop 0\n\tglobal_load_lds_dword %0, %1" :: "v"((u32)(idx * 8 + lane) * 128), "s"((const char*)(pc & ~(u64)127)) : "memory");
    }
    const bool c1q = c < 6 || ch == 0;
    P1Tab T; T.r0 = mkrsrc(A.rowrec, S_ * 32); T.r1 = mkrsrc(A.wext, F_ * 16); T.o0 = (rb * 256 + (tid & 255)) * 16; T.o1 = (288 * c + (tid < 288 ? tid : 0)) * 16;
    bf16x8 wf[6];
#pragma unroll
    for (int k = 0; k < 6; ++k) wf[k] = xf[k];
    if (c1q) {
        p1_top(X, 1, 0); P1_QS(0, true, X.trbase + X.rs);
        p1_top(X, 1, 1); P1_QS(1, true, X.trbase + X.rs);
        p1_top(X, 1, 2); p1_tab_issue(T); P1_QS(2, true, X.trbase + X.rs);
        const int g = c < 6 ? gq1 + ch : gs;
        p1_qkv_store(qrs, g * 2048, lane, accA, g < 18 ? QSCALE : 1.f);
        asm volatile("s_waitcnt vmcnt(0)" : "+v"(T.t0), "+v"(T.t1) :: "memory");
    } else { p1_w1_chunk<true>(X, 1, 0, false, xf, wf, accB, accA, T); asm volatile("s_waitcnt vmcnt(0)" : "+v"(T.t0), "+v"(T.t1) :: "memory"); }
    STAMP(8);
    { const unsigned long long tm0 = __builtin_amdgcn_s_memrealtime();
      while (!__all((tid >= 256 || T.t0[3] == (u32)ep) && (tid >= 288 || T.t1[3] == (u32)ep))) {
          T.t0 = ld16_sc1(T.r0, T.o0); T.t1 = ld16_sc1(T.r1, T.o1);
          if (__builtin_amdgcn_s_memrealtime() - tm0 > 5000000ull) break; }
      if ((tid & 1) == 0) T.t0[3] = 0u;
      T.t1[3] = 0u;
      if (tid < 256) *(LAS u32x4*)((lptr)(size_t)lds0 + P1_ROW + tid * 16) = T.t0;
      if (tid < 288) *(LAS u32x4*)((lptr)(size_t)lds0 + P1_WEXT + tid * 16) = T.t1; }
    P1_WAITBAR(0);
    if (tid == 0) st_agent(flag_ptr(A.ctrl, F_QKV, blockIdx.x), ep);
    const int iend = (nch == 7 && ch == 1) ? 6 : nch;
#define P1_G(i) ((i) == 1 ? wsingle : p1_w1g(X, i) + ((i) < 6 ? ch : 0))
#define P1_ST1
#define P1_ST2
    u64 fq = 0;
    for (int i = 2; i < iend; i += 2) {
        p1_w1_chunk<false>(X, i, P1_G(i - 1), i > 2 || !c1q, xf, wf, accA, accB, T); P1_ST1
        if (wid == 0) { if (i == 2) poll_issue(fq, attn_flag(A, lane)); else if (i == 4) { poll_check(fq, attn_flag(A, lane), lane < 18, ep); } }
        if (i + 1 < iend) p1_w1_chunk<false>(X, i + 1, P1_G(i), true, xf, wf, accB, accA, T); P1_ST2
    }
    if (iend < nch) { P1_WAITBAR(12); P1_WAITBAR(9); P1_WAITBAR(6); }
    BARRIER();
    attn_stage(A, lds0, wid, lane);
    int ie = iend; asm volatile("" : "+s"(ie));
    if ((ie & 1) != 0) accB = accA;
    { Epi E; const int lg = P1_G(ie - 1); const u32 vo = X.l16 + (u32)lg * 2048;
      const bf16x8 we = *(const LAS bf16x8*)(X.wexb + (lg - 9 * X.c) * X.wexs), xe = *(const LAS bf16x8*)X.xeb;
      p1_consts(X, E);
#pragma unroll
      for (int s = 0; s < 4; ++s) accB = MFMA32(wf[(s + 2) % 6], xf[32 + s], accB);
      accB = MFMA32(we, xe, accB);
#pragma unroll
      for (int s = 0; s < 32; ++s) p1_gelu_slot(s, X, accB, E, vo); }
    STAMP(15);
    STAMP_OUT(A.stamps, 16);
}
__device__ __forceinline__ int qk_off(int c) { const int cc = c & 31; return (c >> 5) * 2048 + (2 * (cc >> 4) + ((cc >> 2) & 1)) * 512 + (((cc >> 3) & 1) * 4 + (cc & 3)) * 2; }
__device__ __forceinline__ float xmax16(float v) { auto r = __builtin_amdgcn_permlane16_swap(__float_as_uint(v), __float_as_uint(v), false, false); return fmaxf(__uint_as_float(r[0]), __uint_as_float(r[1])); }
__device__ __forceinline__ float xmax32(float v) { auto r = __builtin_amdgcn_permlane32_swap(__float_as_uint(v), __float_as_uint(v), false, false); return fmaxf(__uint_as_float(r[0]), __uint_as_float(r[1])); }
__device__ __forceinline__ float max3f(float a, float b, float c) { return __builtin_fmaxf(__builtin_fmaxf(a, b), c); }

template <bool EARLY>
__device__ __forceinline__ void attn_mask(f32x4& s, int t, int dq, int kb) {
    if (EARLY) {
#pragma unroll
        for (int r = 0; r < 4; ++r) { const int d = dq - 16 * t - r; const bool ok = (u32)d <= 255u && (kb + 16 * t + r) >= 0; s[r] = ok ? s[r] : -INFINITY; }
    } else if (t < 2) {
#pragma unroll
        for (int r = 0; r < 4; ++r) s[r] = (dq - 16 * t - r <= 255) ? s[r] : -INFINITY;
    } else if (t >= 16) {
#pragma unroll
        for (int r = 0; r < 4; ++r) s[r] = (dq - 16 * t - r >= 0) ? s[r] : -INFINITY;
    }
}
#define ATS_ARG
#define ATS_PASS
template <bool EARLY>
__device__ __forceinline__ void attn_units(lcptr L, int c00  , int cones, int w, int lane, int Q0, float gate, __amdgpu_buffer_rsrc_t ors, int head0, gu64* fhp, u64 ep ATS_ARG) {
    u64 fh = 0; if (w == 0) poll_issue(fh, fhp);
    const int i16 = lane & 15, g = lane >> 4;
    const int lb0 = w >> 1;
    const int q = Q0 + 16 * w + i16, kb = Q0 - 256 + lb0 * 32 + 4 * g, dq = q - kb;
    bf16x8 kf[2][18], qf[2];
    f32x4 nm[2];
#pragma unroll
    for (int u = 0; u < 2; ++u) {
        const int c0 = c00 + 18 * u;
        const int p2 = c0 & 2, op = qk_off(c0 + p2 + 4 * g), os = qk_off(p2 ? c0 - 2 : c0 + 16);
        lcptr qp = L + AT_Q + (w >> 1) * 4096 + ((w & 1) * 16 + i16) * 16;
        const u32x2 qa = *(const LAS u32x2*)(qp + op), qc = *(const LAS u32x2*)(qp + os);
        qf[u] = __builtin_bit_cast(bf16x8, (u32x4){qa[0], qa[1], (g == 0 && !p2) ? qc[0] : 0u, (g == 0 && p2) ? qc[1] : 0u});
        lcptr kb0 = L + AT_K + lb0 * 4096 + i16 * 16;
        lcptr k0 = kb0 + op, k1 = kb0 + os;
        PIN(k0); PIN(k1);
#pragma unroll
        for (int t = 0; t < 18; ++t) { const int o = (t >> 1) * 4096 + (t & 1) * 256;
            const u32x2 ka = *(const volatile LAS u32x2*)(k0 + o), kc = *(const volatile LAS u32x2*)(k1 + o);
            kf[u][t] = __builtin_bit_cast(bf16x8, (u32x4){ka[0], ka[1], kc[0], kc[1]}); }
    }
    lcptr va[2], vb[2];
#pragma unroll
    for (int u = 0; u < 2; ++u) {
        const int c0 = c00 + 18 * u, ca = c0 + i16, cb = (i16 < 2) ? c0 + 16 + i16 : (i16 == 2 ? cones : c0);
        va[u] = L + AT_V + lb0 * 4096 + (ca >> 5) * 2048 + (g & 1) * 512 + (ca & 31) * 16 + (g >> 1) * 8;
        vb[u] = L + AT_V + lb0 * 4096 + (cb >> 5) * 2048 + (g & 1) * 512 + (cb & 31) * 16 + (g >> 1) * 8;
        PIN(va[u]); PIN(vb[u]);
    }
    SBAR();
    STAMP(5);
#pragma unroll
    for (int u = 0; u < 2; ++u) {
        f32x4 s[18];
#pragma unroll
        for (int t = 0; t < 18; ++t) s[t] = MFMA16(kf[u][t], qf[u], ((f32x4){0.f, 0.f, 0.f, 0.f}));
#pragma unroll
        for (int t = 0; t < 18; ++t) attn_mask<EARLY>(s[t], t, dq, kb);
        float m0 = max3f(s[0][0], s[0][1], s[0][2]), m1 = max3f(s[0][3], s[1][0], s[1][1]);
        m0 = max3f(m0, s[1][2], s[1][3]);
#pragma unroll
        for (int t = 2; t < 18; t += 2) { m0 = max3f(m0, s[t][0], s[t][1]); m1 = max3f(m1, s[t][2], s[t][3]); m0 = max3f(m0, s[t + 1][0], s[t + 1][1]); m1 = max3f(m1, s[t + 1][2], s[t + 1][3]); }
        const float m = -xmax32(xmax16(fmaxf(m0, m1)));
        nm[u] = (f32x4){m, m, m, m};
    }
    if (w == 0) { WAIT_VM0(); poll_check(fh, fhp, lane < 4, ep); }
    STAMP(6);
    u32x2 vf[3][4]; f32x4 sc2[2][2];
#define AT_VRD(k) do { const int b_ = (k) >> 1, u_ = (k) & 1; vf[(k) % 3][0] = *(const volatile LAS u32x2*)(va[u_] + b_ * 4096); vf[(k) % 3][1] = *(const volatile LAS u32x2*)(va[u_] + b_ * 4096 + 1024); \
        vf[(k) % 3][2] = *(const volatile LAS u32x2*)(vb[u_] + b_ * 4096); vf[(k) % 3][3] = *(const volatile LAS u32x2*)(vb[u_] + b_ * 4096 + 1024); } while (0)
#define AT_QK(k) do { const int b_ = (k) >> 1, u_ = (k) & 1; sc2[(k) & 1][0] = MFMA16(kf[u_][2 * b_], qf[u_], nm[u_]); sc2[(k) & 1][1] = MFMA16(kf[u_][2 * b_ + 1], qf[u_], nm[u_]); } while (0)
    f32x4 oa[2] = {{0.f, 0.f, 0.f, 0.f}, {0.f, 0.f, 0.f, 0.f}}, obb[2] = {{0.f, 0.f, 0.f, 0.f}, {0.f, 0.f, 0.f, 0.f}};
    AT_VRD(0); AT_VRD(1); AT_QK(0);
    SBAR();
#pragma unroll
    for (int k = 0; k < 18; ++k) {
        const int b = k >> 1, u = k & 1;
        f32x4 s0 = sc2[k & 1][0], s1 = sc2[k & 1][1];
        if (k + 1 < 18) { const int b_ = (k + 1) >> 1, u_ = (k + 1) & 1; sc2[(k + 1) & 1][0] = MFMA16(kf[u_][2 * b_], qf[u_], nm[u_]); sc2[(k + 1) & 1][1] = MFMA16(kf[u_][2 * b_ + 1], qf[u_], nm[u_]); PIN(sc2[(k + 1) & 1][0]); PIN(sc2[(k + 1) & 1][1]); }
        if (k + 2 < 18) AT_VRD(k + 2);
        attn_mask<EARLY>(s0, 2 * b, dq, kb); attn_mask<EARLY>(s1, 2 * b + 1, dq, kb);
        u32x4 pw;
        pw[0] = cvtpk_c(__builtin_amdgcn_exp2f(s0[0]), __builtin_amdgcn_exp2f(s0[1])); pw[1] = cvtpk_c(__builtin_amdgcn_exp2f(s0[2]), __builtin_amdgcn_exp2f(s0[3]));
        pw[2] = cvtpk_c(__builtin_amdgcn_exp2f(s1[0]), __builtin_amdgcn_exp2f(s1[1])); pw[3] = cvtpk_c(__builtin_amdgcn_exp2f(s1[2]), __builtin_amdgcn_exp2f(s1[3]));
        const bf16x8 pf = __builtin_bit_cast(bf16x8, pw);
        oa[u] = MFMA16(__builtin_bit_cast(bf16x8, ((u32x4){vf[k % 3][0][0], vf[k % 3][0][1], vf[k % 3][1][0], vf[k % 3][1][1]})), pf, oa[u]);
        obb[u] = MFMA16(__builtin_bit_cast(bf16x8, ((u32x4){vf[k % 3][2][0], vf[k % 3][2][1], vf[k % 3][3][0], vf[k % 3][3][1]})), pf, obb[u]);
        PIN(oa[u]); PIN(obb[u]);
        SBAR();
    }
    STAMP(7);
    auto off = [](int c) { const int cc = c & 31; return (c >> 5) * 2048 + (2 * (cc >> 4) + ((cc >> 2) & 1)) * 512 + (((cc >> 3) & 1) * 4 + (cc & 3)) * 2; };
    const u32 base = (u32)(q >> 5) * 18 * 2048 + (q & 31) * 16;
    const float sc0 = gate * __builtin_amdgcn_rcpf(__shfl(obb[0][2], i16)), sc1 = gate * __builtin_amdgcn_rcpf(__shfl(obb[1][2], i16));
    const u32 p1 = cvtpk(oa[1][0] * sc1, oa[1][1] * sc1), r1 = cvtpk(oa[1][2] * sc1, oa[1][3] * sc1), t1 = cvtpk(obb[1][0] * sc1, obb[1][1] * sc1);
    const u32 nx = (u32)__shfl((int)(g == 0 ? t1 : p1), (lane + 16) & 63);
    const int col0 = 18 * head0;
    typedef u32 u32x2s __attribute__((ext_vector_type(2)));
    __builtin_amdgcn_raw_buffer_store_b64((u32x2s){cvtpk(oa[0][0] * sc0, oa[0][1] * sc0), cvtpk(oa[0][2] * sc0, oa[0][3] * sc0)}, ors, base + off(col0 + 4 * g), 0, 16);
    __builtin_amdgcn_raw_buffer_store_b64((u32x2s){r1, nx}, ors, base + off(col0 + 20 + 4 * g), 0, 16);
    if (g == 0) __builtin_amdgcn_raw_buffer_store_b64((u32x2s){cvtpk(obb[0][0] * sc0, obb[0][1] * sc0), p1}, ors, base + off(col0 + 16), 0, 16);
}

__device__ __forceinline__ int attn_wg(int qb, int hp) { return (hp & 7) + 8 * (2 * qb + (hp >> 3)); }
__device__ __forceinline__ void acquire_agent() { __builtin_amdgcn_fence(__ATOMIC_ACQUIRE, "agent"); WAIT_VM0(); }

__device__ __forceinline__ void attn_phase(const Args& A, char* smem, u64 ep) {
    STAMP_DECL STAMP(0);
    const int tid = threadIdx.x, lane = tid & 63, wid = RFL(tid >> 6);
    const int xcd = blockIdx.x & 7, idx = blockIdx.x >> 3;
    const int qb = idx >> 1, hp = xcd + 8 * (idx & 1);
    const int Q0 = qb * 128, cg0 = (36 * hp) >> 5, c0 = 36 * hp - 32 * cg0;
    const u32 lds0 = (u32)(uintptr_t)smem;
    gu64* fhp;
    { const int sp = blockIdx.x & 7, mt = (blockIdx.x >> 3) & 7; fhp = flag_ptr(A.ctrl, F_H, p1_wg(2 * mt + (lane & 1), 2 * sp + ((lane >> 1) & 1))); }
    const int q = Q0 + 16 * wid + (lane & 15);
    const float gate = __uint_as_float(__builtin_amdgcn_raw_buffer_load_b32(mkrsrc(A.rowrec, S_ * 32), q * 32 + 24, 0, 16));
    WAIT_VM0(); BARRIER();
    if (tid == 0) st_agent(flag_ptr(A.ctrl, F_H, blockIdx.x), ep);
    STAMP(2);
    const int cones = (c0 + 36) & 63;
    if (tid < 48) { const int kb = tid >> 2, pc = tid & 3; *(LAS u32x4*)((lptr)(size_t)lds0 + AT_V + kb * 4096 + (cones >> 5) * 2048 + pc * 512 + (cones & 31) * 16) = (u32x4){0x3f803f80u, 0x3f803f80u, 0x3f803f80u, 0x3f803f80u}; }
    BARRIER();
    STAMP(3);
    const __amdgpu_buffer_rsrc_t ors = mkrsrc(A.ob, S_ * D_ * 2);
    if (qb < 2) attn_units<true>((lcptr)(size_t)lds0, c0, cones, wid, lane, Q0, gate, ors, 2 * hp, fhp, ep ATS_PASS); else attn_units<false>((lcptr)(size_t)lds0, c0, cones, wid, lane, Q0, gate, ors, 2 * hp, fhp, ep ATS_PASS);
    STAMP(8);
    BARRIER();
    STAMP(4);
    STAMP_OUT(A.stamps + (2u << 17), 9);
}
constexpr int P3_A = 32768, P3_B = 64 * 288, P3_SLOT = P3_A + P3_B, P3_LDS = 3 * P3_SLOT;
struct P3Ctx {
    const char* pA; const char* pB;
    const char* oA; const char* oB;
    u32 aStride;
    int tl, nst; u32 sl;
    u32 dA, dB;
    u32 vA, vA2, vB0, vB1, vB2;
    lcptr ab, bb;
};
__device__ __forceinline__ void p3_part_a(const P3Ctx& X, int k) {
    const u32 base = k < 2 ? X.dA : X.dB; const int add = k == 1 ? 2048 : (k == 3 ? 1024 : (k == 4 ? 2048 : 0));
    asm volatile("s_add_u32 m0, %0, %1" :: "s"(base + X.sl), "n"(add) : "memory", "scc");
}
__device__ __forceinline__ void p3_part_b(P3Ctx& X, int k) {
    if (k == 0) asm volatile("global_load_lds_dwordx4 %0, %1 sc1\n\tglobal_load_lds_dwordx4 %0, %1 offset:1024 sc1" :: "v"(X.vA), "s"(X.pA) : "memory");
    if (k == 1) asm volatile("global_load_lds_dwordx4 %0, %1 sc1\n\tglobal_load_lds_dwordx4 %0, %1 offset:1024 sc1" :: "v"(X.vA2), "s"(X.pA) : "memory");
    if (k == 2) asm volatile("global_load_lds_dwordx4 %0, %1" :: "v"(X.vB0), "s"(X.pB) : "memory");
    if (k == 3) asm volatile("global_load_lds_dwordx4 %0, %1" :: "v"(X.vB1), "s"(X.pB) : "memory");
    if (k == 4) { asm volatile("s_mov_b64 exec, 0xffff\n\tglobal_load_lds_dwordx4 %0, %1\n\ts_mov_b64 exec, -1" :: "v"(X.vB2), "s"(X.pB) : "memory");
        X.tl += 1; const bool sw = X.tl == 9, adv = X.tl < X.nst;
        X.pA = sw ? X.oA : X.pA + (adv ? 4096 : 0); X.pB = sw ? X.oB : X.pB + (adv ? 64 * 1152 : 0); X.sl = X.sl == 2 * P3_SLOT ? 0 : X.sl + P3_SLOT; }
}
__device__ __forceinline__ void p3_load_part(P3Ctx& X, int k) { p3_part_a(X, k); asm volatile("s_nop 0"); p3_part_b(X, k); }
struct P3Frag { bf16x8 h[4]; bf16x8 w[5]; };
__device__ __forceinline__ void p3_waitbar() { asm volatile("s_waitcnt vmcnt(7) lgkmcnt(0)\n\ts_barrier" ::: "memory"); }
template <int NT, int HF, bool RD, bool MM>
__device__ __forceinline__ void p3_half(P3Ctx& X, int t, u32 so, P3Frag& R, const P3Frag& P, f32x4 (&acc)[4][5]) {
    lcptr a = X.ab + so + HF * 2048, b = X.bb + so + HF * (32 * 288);
    if (RD) { if (HF == 0) p3_waitbar(); PIN(a); PIN(b); }
    s16x4 lo;
    SBAR();
#pragma unroll
    for (int i = 0; i < 4 * NT; ++i) {
        if (MM) acc[i & 3][i >> 2] = MFMA16(P.w[i >> 2], P.h[i & 3], acc[i & 3][i >> 2]);
        constexpr int S0 = HF == 0 ? 2 : 1, S1 = HF == 0 ? 8 : 6, S2 = HF == 0 ? -1 : 10;
        const int part = i == S0 ? (HF == 0 ? 0 : 2) : (i == S1 ? (HF == 0 ? 1 : 3) : (i == S2 ? 4 : -1));
        if (RD) {
            if (part >= 0) p3_part_a(X, part);
            if (i < 4) R.h[i] = *(const LAS bf16x8*)(a + (i >> 1) * 4096 + (i & 1) * 256);
            else if (i < 4 + 2 * NT) { const int j = i - 4; if (j & 1) R.w[j >> 1] = cat8(lo, trd(b + (j >> 1) * 32 + 8 * 288)); else lo = trd(b + (j >> 1) * 32); }
            if (part >= 0) p3_part_b(X, part);
        }
        SBAR();
    }
}
template <int NT>
__device__ __forceinline__ void p3_loop(P3Ctx& X, int nhalf, f32x4 (&acc)[4][5], bool w0, gu64* fo, bool fo_use, u64 ep, gu64* own_o) {
    u64 fv = 0;
    WAIT_VM0(); BARRIER();
    if (threadIdx.x == 0) st_agent(own_o, ep);
    P3Frag F0, F1;
    u32 so = 0;
    p3_half<NT, 0, true, false>(X, 0, so, F0, F1, acc);
    p3_half<NT, 1, true, true>(X, 0, so, F1, F0, acc);
    int t = 1;
    for (; 2 * t + 1 < nhalf; ++t) {
        so = so == 2 * P3_SLOT ? 0 : so + P3_SLOT;
        if (w0) { if (t == 2) poll_issue(fv, fo); else if (t == 4) poll_check(fv, fo, fo_use, ep); }
        p3_half<NT, 0, true, true>(X, t, so, F0, F1, acc);
        p3_half<NT, 1, true, true>(X, t, so, F1, F0, acc);
    }
    if (2 * t < nhalf) {
        so = so == 2 * P3_SLOT ? 0 : so + P3_SLOT;
        p3_half<NT, 0, true, true>(X, t, so, F0, F1, acc);
        p3_half<NT, 1, false, true>(X, t, so, F1, F0, acc);
    } else p3_half<NT, 0, false, true>(X, t, so, F0, F1, acc);
}
__device__ __forceinline__ void p3_phase(const Args& A, char* smem, u64 ep) {
    STAMP_DECL STAMP(0);
    const int tid = threadIdx.x, lane = tid & 63, wid = RFL(tid >> 6);
    const int sp = blockIdx.x & 7, idx = blockIdx.x >> 3;
    const int mt = idx & 7, nt = idx >> 3;
    const u32 lds0 = (u32)(uintptr_t)smem;
    const int mq = wid >> 1, nh = (wid & 1) ^ (wid >> 2);
    const int i16 = lane & 15, kq = lane >> 4;
    P3Ctx X;
    const int no = sp < 2 ? 1 : (sp < 4 ? 2 : 3), o0 = sp < 2 ? sp : (sp < 4 ? 2 * sp - 2 : 3 * sp - 6);
    STAMP(4);
    const int nhalf = 18 + no; X.nst = (nhalf + 1) >> 1; X.tl = 0; X.sl = 0;
    X.pA = A.hb + (size_t)((mt * 8 + wid) * 144 + 18 * sp) * 2048;
    X.oA = A.ob + (size_t)((mt * 8 + wid) * 18 + o0) * 2048;
    X.pB = (const char*)A.w2 + (size_t)(576 * sp) * 1152 + 288 * nt;
    X.oB = (const char*)A.wo + (size_t)(32 * o0) * 1152 + 288 * nt;
    X.dA = lds0 + wid * 4096; X.dB = lds0 + P3_A + wid * 144 * 16;
    X.vA = lane * 16; X.vA2 = lane * 16 + 2048;
    { const int e0 = wid * 144 + lane, e1 = e0 + 64, e2 = e0 + 128;
      X.vB0 = (e0 / 18) * 1152 + (e0 % 18) * 16; X.vB1 = (e1 / 18) * 1152 + (e1 % 18) * 16; X.vB2 = (e2 / 18) * 1152 + (e2 % 18) * 16; }
    X.ab = (lcptr)(size_t)lds0 + (mq * 2) * 4096 + kq * 512 + i16 * 16;
    X.bb = (lcptr)(size_t)lds0 + P3_A + (16 * (kq >> 1) + 4 * (kq & 1) + (i16 >> 2)) * 288 + (nh ? 5 * 32 : 0) + (i16 & 3) * 8;
#pragma unroll
    for (int t = 0; t < 2; ++t)
#pragma unroll
        for (int k = 0; k < 5; ++k) p3_load_part(X, k);
    f32x4 acc[4][5];
#pragma unroll
    for (int m = 0; m < 4; ++m)
#pragma unroll
        for (int n = 0; n < 5; ++n) acc[m][n] = (f32x4){0.f, 0.f, 0.f, 0.f};
    STAMP(1);
    const int hp0 = (32 * o0) / 36, hp1 = (32 * (o0 + no) - 1) / 36, hp = hp0 + (lane >> 1);
    gu64* fo = flag_ptr(A.ctrl, F_O, attn_wg(2 * mt + (lane & 1), hp <= hp1 ? hp : hp0));
    if (nh == 0) p3_loop<5>(X, nhalf, acc, wid == 0, fo, hp <= hp1, ep, flag_ptr(A.ctrl, F_O, blockIdx.x)); else p3_loop<4>(X, nhalf, acc, wid == 0, fo, hp <= hp1, ep, flag_ptr(A.ctrl, F_O, blockIdx.x));
    STAMP(2);
    WAIT_VM0(); BARRIER();
    const u32 tile = (u32)(mt * 4 + nt) * 8 * (72 * 1024);
    const __amdgpu_buffer_rsrc_t prs = mkrsrc(A.part + tile, 8 * 72 * 1024);
    const int NTN = nh ? 4 : 5;
#pragma unroll
    for (int pr = 0; pr < 2; ++pr)
#pragma unroll
        for (int n = 0; n < 5; ++n)
            if (n < NTN) { const u32x4 v = {cvtpk_c(acc[2 * pr][n][0], acc[2 * pr][n][1]), cvtpk_c(acc[2 * pr][n][2], acc[2 * pr][n][3]),
                                            cvtpk_c(acc[2 * pr + 1][n][0], acc[2 * pr + 1][n][1]), cvtpk_c(acc[2 * pr + 1][n][2], acc[2 * pr + 1][n][3])};
                if (mq * 2 + pr == sp) *(LAS u32x4*)((lptr)(size_t)lds0 + ((nh ? 5 : 0) + n) * 1024 + lane * 16) = v;
                else st16_sc1(v, prs, sp * (72 * 1024) + ((mq * 2 + pr) * 9 + (nh ? 5 : 0) + n) * 1024 + lane * 16); }
    WAIT_VM0(); BARRIER();
    if (tid == 0) st_agent(flag_ptr(A.ctrl, F_PART, blockIdx.x), ep);
    STAMP(3);
    if (tid < 320) {
        const bool act = tid < 288;
        const int ck0 = act ? tid : 0;
        u32x2 bb[2]; float og[2][2];
#pragma unroll
        for (int r = 0; r < 2; ++r) { const int ck = ck0 + 288 * r, n = ck >> 6, ln = ck & 63;
            bb[r] = *(const u32x2*)(A.b2 + nt * 144 + n * 16 + (ln >> 4) * 4);
#pragma unroll
            for (int e = 0; e < 2; ++e) og[r][e] = 1.f - __uint_as_float(__builtin_amdgcn_raw_buffer_load_b32(mkrsrc(A.rowrec, S_ * 32), (mt * 256 + 32 * sp + 16 * e + (ln & 15)) * 32 + 24, 0, 16)); }
        poll_flags(flag_ptr(A.ctrl, F_PART, (lane & 7) + 8 * idx), lane < 8, ep);
        u32x4 pv[2][8];
#pragma unroll
        for (int r = 0; r < 2; ++r)
#pragma unroll
            for (int s = 0; s < 8; ++s) { const int ck = ck0 + 288 * r; pv[r][s] = ld16_sc1(prs, s == sp ? 0x40000000u : (u32)(s * (72 * 1024) + (sp * 9 + (ck >> 6)) * 1024 + (ck & 63) * 16)); }
#pragma unroll
        for (int r = 0; r < 2; ++r) {
            const u32x4 own = *(const LAS u32x4*)((lcptr)(size_t)lds0 + (ck0 + 288 * r) * 16);
            float v[8];
#pragma unroll
            for (int j = 0; j < 4; ++j) { v[2 * j] = bflo(own[j]); v[2 * j + 1] = bfhi(own[j]); }
#pragma unroll
            for (int s = 0; s < 8; ++s)
#pragma unroll
                for (int j = 0; j < 4; ++j) { v[2 * j] += bflo(pv[r][s][j]); v[2 * j + 1] += bfhi(pv[r][s][j]); }
            const int ck = ck0 + 288 * r, n = ck >> 6, ln = ck & 63;
#pragma unroll
            for (int e = 0; e < 2; ++e) {
                const int row = mt * 256 + 32 * sp + 16 * e + (ln & 15), col = nt * 144 + n * 16 + (ln >> 4) * 4;
                u32x2 o; o[0] = cvtpk(v[4 * e] + og[r][e] * bflo(bb[r][0]), v[4 * e + 1] + og[r][e] * bfhi(bb[r][0])); o[1] = cvtpk(v[4 * e + 2] + og[r][e] * bflo(bb[r][1]), v[4 * e + 3] + og[r][e] * bfhi(bb[r][1]));
                if (act) *(u32x2*)(A.out + (size_t)row * D_ + col) = o;
            }
        }
    }
    STAMP(5);
    STAMP_OUT(A.stamps + (1u << 17), 6);
}
constexpr int MEGA_LDS = 163840;
static_assert(P1_LDS <= MEGA_LDS && AT_LDS <= MEGA_LDS && P3_LDS <= MEGA_LDS, "LDS");
__global__ __launch_bounds__(512, 2) void mega_kernel(Args A) {
    extern __shared__ __attribute__((aligned(16))) char smem[];
    u64 ep;
    typedef __attribute__((address_space(4))) const u64* ArgP;
#if defined(__HIP_DEVICE_COMPILE__)
    ArgP ap = (ArgP)__builtin_amdgcn_kernarg_segment_ptr();
#else
    ArgP ap = nullptr;
#endif
#define LOAD_ARGS(a) Args a; { asm volatile("" : "+s"(ap)); u64 w_[17]; _Pragma("unroll") for (int i_ = 0; i_ < 17; ++i_) w_[i_] = ap[i_]; __builtin_memcpy(&a, w_, sizeof(Args)); }
    static_assert(sizeof(Args) == 17 * 8, "Args: 17 pointers");
    { LOAD_ARGS(a) p1_phase(a, smem, ep); }
    { LOAD_ARGS(a) attn_phase(a, smem, ep); }
    { LOAD_ARGS(a) p3_phase(a, smem, ep); }
    if (blockIdx.x == 0 && threadIdx.x < 64) {
        LOAD_ARGS(a) u64* ctrl = a.ctrl;
#pragma unroll
        for (int i = 0; i < 4; ++i) poll_flags(flag_ptr(ctrl, F_STAT, threadIdx.x + 64 * i), true, ep);
        if (threadIdx.x == 0) st_agent(epoch_ptr(ctrl), ep);
    }
}
extern "C" void kernel_launch(void* const* d_in, const int* in_sizes, int n_in, void* d_out, int out_size, void* d_ws, size_t ws_size, hipStream_t stream) {
    char* ws = (char*)d_ws;
    Args A;
    A.x = (const u16*)d_in[0]; A.wqkv = (const u16*)d_in[1]; A.wo = (const u16*)d_in[2]; A.w1 = (const u16*)d_in[4]; A.b1 = (const u16*)d_in[5];
    A.w2 = (const u16*)d_in[6]; A.b2 = (const u16*)d_in[7]; A.gw = (const u16*)d_in[8];
    A.rowrec = (u32x4*)ws;
    A.wext = (u32x4*)(ws + 65536);
    char* big = ws + 147456;
    A.qkvb = big;
    A.hb = big + 7077888;
    A.ob = big + 7077888 + 18874368;
    A.part = big + 7077888 + 18874368 + 2359296;
    A.ctrl = (u64*)(big + 7077888 + 18874368 + 2359296 + 18874368);
    A.out = (u16*)d_out;
    A.stamps = (unsigned long long*)(ws + (200u << 20));
    (void)hipFuncSetAttribute((const void*)mega_kernel, hipFuncAttributeMaxDynamicSharedMemorySize, MEGA_LDS);
    mega_kernel<<<256, 512, MEGA_LDS, stream>>>(A);
}
```

```cpp
#include <hip/hip_runtime.h>
#include <hip/hip_bf16.h>
typedef unsigned short u16;
typedef unsigned int u32;
#define LAS __attribute__((address_space(3)))
typedef short bf16x8 __attribute__((ext_vector_type(8)));
typedef short s16x4 __attribute__((ext_vector_type(4)));
typedef float f32x16 __attribute__((ext_vector_type(16)));
typedef float f32x4 __attribute__((ext_vector_type(4)));
typedef u32 u32x4 __attribute__((ext_vector_type(4)));
typedef u32 u32x2 __attribute__((ext_vector_type(2)));
typedef LAS char* lptr;
typedef LAS const char* lcptr;

constexpr int S_ = 2048, D_ = 576, H_ = 32, DH_ = 18, F_ = 4608, NQKV_ = 1728;

__device__ __forceinline__ float bf2f(u16 b) { return __uint_as_float(((u32)b) << 16); }
__device__ __forceinline__ float bflo(u32 w) { return __uint_as_float(w << 16); }
__device__ __forceinline__ float bfhi(u32 w) { return __uint_as_float(w & 0xffff0000u); }
typedef __bf16 bf16x2_t __attribute__((ext_vector_type(2)));
typedef float f32x2 __attribute__((ext_vector_type(2)));
__device__ __forceinline__ u32 cvtpk_c(float lo, float hi) { return __builtin_bit_cast(u32, __builtin_convertvector((f32x2){lo, hi}, bf16x2_t)); }
__device__ __forceinline__ u32 cvtpk(float lo, float hi) { u32 r; asm("v_cvt_pk_bf16_f32 %0, %1, %2" : "=v"(r) : "v"(lo), "v"(hi)); return r; }
__device__ __forceinline__ u16 f2bf(float f) { return (u16)(cvtpk(f, f) & 0xffffu); }
__device__ __forceinline__ void glds16(const void* g, u32 lds_base) {
    u32 sv;
    asm volatile("s_mov_b32 %0, m0\n\ts_mov_b32 m0, %2\n\ts_nop 0\n\tglobal_load_lds_dwordx4 %1, off\n\ts_mov_b32 m0, %0" : "=&s"(sv) : "v"(g), "s"(lds_base) : "memory");
}
__device__ __forceinline__ void glds16s(const void* sbase, u32 voff, u32 lds_base) {
    u32 sv;
    asm volatile("s_mov_b32 %0, m0\n\ts_mov_b32 m0, %3\n\ts_nop 0\n\tglobal_load_lds_dwordx4 %1, %2\n\ts_mov_b32 m0, %0" : "=&s"(sv) : "v"(voff), "s"(sbase), "s"(lds_base) : "memory");
}
__device__ __forceinline__ void glds16s_off1k(const void* sbase, u32 voff, u32 lds_base) {
    u32 sv;
    asm volatile("s_mov_b32 %0, m0\n\ts_mov_b32 m0, %3\n\ts_nop 0\n\tglobal_load_lds_dwordx4 %1, %2\n\tglobal_load_lds_dwordx4 %1, %2 offset:1024\n\ts_mov_b32 m0, %0" : "=&s"(sv) : "v"(voff), "s"(sbase), "s"(lds_base) : "memory");
}
__device__ __forceinline__ void glds16s_off1k_sc1(const void* sbase, u32 voff, u32 lds_base) {
    u32 sv;
    asm volatile("s_mov_b32 %0, m0\n\ts_mov_b32 m0, %3\n\ts_nop 0\n\tglobal_load_lds_dwordx4 %1, %2 sc1\n\tglobal_load_lds_dwordx4 %1, %2 offset:1024 sc1\n\ts_mov_b32 m0, %0" : "=&s"(sv) : "v"(voff), "s"(sbase), "s"(lds_base) : "memory");
}
__device__ __forceinline__ s16x4 trd(lcptr p) {
    typedef short v4i16_t __attribute__((ext_vector_type(4)));
    return __builtin_bit_cast(s16x4, __builtin_amdgcn_ds_read_tr16_b64_v4i16((LAS v4i16_t*)p));
}
__device__ __forceinline__ bf16x8 cat8(s16x4 a, s16x4 b) { return (bf16x8){a[0], a[1], a[2], a[3], b[0], b[1], b[2], b[3]}; }
#define MFMA32(a, b, c) __builtin_amdgcn_mfma_f32_32x32x16_bf16(a, b, c, 0, 0, 0)
#define MFMA16(a, b, c) __builtin_amdgcn_mfma_f32_16x16x32_bf16(a, b, c, 0, 0, 0)
#define SBAR() __builtin_amdgcn_sched_barrier(0)
#define PIN(x) asm volatile("" : "+v"(x))
#define RFL(x) __builtin_amdgcn_readfirstlane(x)

#define STAMP_DECL
#define STAMP(i)
#define STAMP_OUT(buf, n)

typedef unsigned long long u64;
typedef __attribute__((address_space(1))) u64 gu64;
constexpr int FLAG_STRIDE = 16;
constexpr int F_STAT = 0, F_QKV = 1, F_H = 2, F_O = 3, F_PART = 4, F_KINDS = 5;
constexpr size_t CTRL_BYTES = (size_t)(F_KINDS * 256 + 1) * FLAG_STRIDE * 8;
__device__ __forceinline__ gu64* flag_ptr(u64* ctrl, int kind, int wg) { return (gu64*)(ctrl + (size_t)(kind * 256 + wg) * FLAG_STRIDE); }
__device__ __forceinline__ gu64* epoch_ptr(u64* ctrl) { return (gu64*)(ctrl + (size_t)(F_KINDS * 256) * FLAG_STRIDE); }
__device__ __forceinline__ u64 ld_agent(gu64* p) { return __hip_atomic_load(p, __ATOMIC_RELAXED, __HIP_MEMORY_SCOPE_AGENT); }
__device__ __forceinline__ void st_agent(gu64* p, u64 v) { __hip_atomic_store(p, v, __ATOMIC_RELAXED, __HIP_MEMORY_SCOPE_AGENT); }
__device__ __forceinline__ void poll_flags(gu64* mine, bool use, u64 ep) {
    const unsigned long long t0 = __builtin_amdgcn_s_memrealtime();
    for (;;) {
        const u64 v = use ? ld_agent(mine) : ep;
        if (__all(v == ep)) break;
        if (__builtin_amdgcn_s_memrealtime() - t0 > 5000000ull) break;
        __builtin_amdgcn_s_sleep(1);
    }
}
__device__ __forceinline__ void poll_issue(u64& v, gu64* p) { asm volatile("global_load_dwordx2 %0, %1, off sc1" : "=v"(v) : "v"(p) : "memory"); }
__device__ __forceinline__ void poll_check(u64& v, gu64* p, bool use, u64 ep) { asm volatile("" : "+v"(v)); if (!__all(!use || v == ep)) poll_flags(p, use, ep); }
__device__ __forceinline__ void st16_sc1(u32x4 v, __amdgpu_buffer_rsrc_t rs, u32 voff) { __builtin_amdgcn_raw_buffer_store_b128(v, rs, voff, 0, 16); }
__device__ __forceinline__ u32x4 ld16_sc1(__amdgpu_buffer_rsrc_t rs, u32 voff) { return __builtin_amdgcn_raw_buffer_load_b128(rs, voff, 0, 16); }
__device__ __forceinline__ __amdgpu_buffer_rsrc_t mkrsrc(const void* p, u32 bytes) { return __builtin_amdgcn_make_buffer_rsrc((void*)p, 0, bytes, 0x00020000); }
#define INV_AGENT() asm volatile("buffer_inv sc1" ::: "memory")
#define WAIT_VM0() asm volatile("s_waitcnt vmcnt(0)" ::: "memory")
#define BARRIER() asm volatile("s_waitcnt lgkmcnt(0)\n\ts_barrier" ::: "memory")
struct Args {
    const u16* x; const u16* wqkv; const u16* wo; const u16* w1; const u16* b1; const u16* w2; const u16* b2; const u16* gw;
    u64* ctrl; u32x4* rowrec; u32x4* wext; char* qkvb; char* hb; char* ob; char* part; u16* out; unsigned long long* stamps;
};
constexpr int P1_SLOT = 24576, P1_RING = 6 * P1_SLOT, P1_WEXT = P1_RING, P1_ROW = P1_WEXT + 288 * 16 + 64  , P1_CS = P1_ROW + 128 * 32  , P1_LDS = P1_CS + 1024 + 256  ;
constexpr float GC0 = -2.f * 0.7978845608028654f * 1.4426950408889634f, GC1 = GC0 * 0.044715f;
__device__ __forceinline__ u32 bfbits(float f) { return cvtpk(f, 0.f) & 0xffffu; }
constexpr float QSCALE = 0.23570226039551584f * 1.4426950408889634f;
#define P1_WAITBAR(N) asm volatile("s_waitcnt vmcnt(" #N ") lgkmcnt(0)\n\ts_barrier" ::: "memory")

struct P1Ctx {
    const char* w1; __amdgpu_buffer_rsrc_t hrs;
    int c, nch, g1first; u32 l16;
    const char* lb; const char* la;
    u32 ld, ldst0;
    u32 voffW[3];
    lcptr trbase, wexb; u32 wexs; u32 rs;
    lcptr xeb, rcb;
};
constexpr u32 P1_PART = 192u * F_ * 2u;
__device__ __forceinline__ int p1_w1g(const P1Ctx& X, int i) { return i < 6 ? X.g1first + 2 * (i - 2) : ((i == 6 && X.nch == 7) ? ((X.c & 1) ? 9 * X.c : 9 * X.c + 8) : X.g1first); }
__device__ __forceinline__ void p1_piece_a(const P1Ctx& X, int k) { asm volatile("s_add_u32 m0, %0, %1" :: "s"(X.ld), "n"(k * 8192) : "memory", "scc"); }
__device__ __forceinline__ void p1_piece_b(const P1Ctx& X, int k) {
    asm volatile("global_load_lds_dwordx4 %0, %1" :: "v"(X.voffW[k]), "s"(X.la) : "memory");
}
__device__ __forceinline__ void p1_piece(const P1Ctx& X, int k) { p1_piece_a(X, k); asm volatile("s_nop 0"); p1_piece_b(X, k); }
__device__ __forceinline__ void p1_top(P1Ctx& X, int i, int j) {
    if (j == 0) asm volatile("s_cmp_le_u32 %0, 2\n\ts_cbranch_scc1 3f\n\ts_waitcnt vmcnt(12) lgkmcnt(0)\n\ts_barrier\n3:" :: "s"(i) : "memory", "scc");
    else asm volatile("s_waitcnt vmcnt(12) lgkmcnt(0)\n\ts_barrier" ::: "memory");
    X.ld = X.ldst0 + X.rs;
    X.rs = X.rs == 5 * P1_SLOT ? 0 : X.rs + P1_SLOT;
    if (j == 0) X.la = X.lb + 2 * P1_PART;
    if (j == 1) { X.lb = X.w1 + p1_w1g(X, i + 2) * 64; X.la = X.lb; }
    if (j == 2) X.la = X.lb + P1_PART;
}
__device__ __forceinline__ void p1_top3(P1Ctx& X) {
    X.lb = X.w1 + p1_w1g(X, 2) * 64;
    X.la = X.lb; X.ld = X.ldst0; p1_piece(X, 0); p1_piece(X, 1); p1_piece(X, 2);
    X.la = X.lb + P1_PART; X.ld = X.ldst0 + P1_SLOT; p1_piece(X, 0); p1_piece(X, 1); p1_piece(X, 2);
    X.rs = 2 * P1_SLOT;
}
struct Epi { float a0, a1, u0, u1, c0, c1, sd; u32 pw[4]; };
__device__ __forceinline__ void p1_gelu_slot(int s, const P1Ctx& X, const f32x16& pa, Epi& E, u32 vo) {
    const int k = s >> 2, ph = s & 3;
    if (ph == 0) { E.a0 = pa[2 * k]; E.a1 = pa[2 * k + 1]; E.u0 = __builtin_fmaf(E.a0 * E.a0, E.c1, E.c0); E.u1 = __builtin_fmaf(E.a1 * E.a1, E.c1, E.c0); PIN(E.u0); PIN(E.u1); }
    if (ph == 1) { E.u0 = __builtin_amdgcn_exp2f(E.a0 * E.u0); E.u1 = __builtin_amdgcn_exp2f(E.a1 * E.u1); PIN(E.u0); PIN(E.u1); }
    if (ph == 2) { E.u0 = __builtin_amdgcn_rcpf(__builtin_fmaf(E.u0, E.sd, E.sd)); E.u1 = __builtin_amdgcn_rcpf(__builtin_fmaf(E.u1, E.sd, E.sd)); PIN(E.u0); PIN(E.u1); }
    if (ph == 3) { E.pw[k & 3] = cvtpk(E.a0 * E.u0, E.a1 * E.u1);
        if ((k & 3) == 3) __builtin_amdgcn_raw_buffer_store_b128((u32x4){E.pw[0], E.pw[1], E.pw[2], E.pw[3]}, X.hrs, vo + (k >> 2) * 1024, 0, 16); }
}
__device__ __forceinline__ void p1_consts(const P1Ctx& X, Epi& E) { const f32x4 k = *(const LAS f32x4*)X.rcb; E.c0 = GC0 * k[0]; E.c1 = GC1 * k[0] * k[0] * k[0]; E.sd = k[1]; }
struct P1Tab { __amdgpu_buffer_rsrc_t r0, r1; u32 o0, o1; u32x4 t0, t1; };
__device__ __forceinline__ void p1_tab_issue(P1Tab& T) {
    asm volatile("buffer_load_dwordx4 %0, %2, %3, 0 offen sc1\n\tbuffer_load_dwordx4 %1, %4, %5, 0 offen sc1" : "=&v"(T.t0), "=&v"(T.t1) : "v"(T.o0), "s"(T.r0), "v"(T.o1), "s"(T.r1) : "memory");
}
template <bool FIRST>
__device__ __forceinline__ void p1_w1_chunk(P1Ctx& X, int i, int pg, bool st, const bf16x8 (&xf)[36], bf16x8 (&wf)[6], f32x16& acc, f32x16& pacc, P1Tab& T) {
    const u32 vo = st ? X.l16 + (u32)pg * 2048 : 0x40000000u;
    Epi E; bf16x8 we, xe;
    lcptr p = X.trbase;
    SBAR();
#pragma unroll
    for (int s = 0; s < 36; ++s) {
        if (s == 0 || s == 12 || s == 24) { p1_top(X, i, s / 12); p = X.trbase + X.rs; PIN(p); }
        if (FIRST && s == 12) p1_tab_issue(T);
        if (s < 4) { pacc = MFMA32(wf[(s + 2) % 6], xf[32 + s], pacc); PIN(pacc); }
        if (s == 4) { pacc = MFMA32(we, xe, pacc); PIN(pacc); f32x16 z = {}; acc = MFMA32(wf[0], xf[0], z); PIN(acc); }
        if (s > 4) { acc = MFMA32(wf[(s - 4) % 6], xf[s - 4], acc); PIN(acc); }
        if (s % 4 == 2) p1_piece_a(X, (s % 12) >> 2);
        if (s == 1) { we = *(const LAS bf16x8*)(X.wexb + (st ? pg - 9 * X.c : 0) * X.wexs); xe = *(const LAS bf16x8*)X.xeb; }
        if (s == 3) p1_consts(X, E);
        if ((s & 1) == 0) {
            wf[(s + 1) % 6] = cat8(trd(p + ((s + 1) % 12) * 2048), trd(p + ((s + 1) % 12) * 2048 + 512));
            wf[s % 6] = cat8(trd(p + (s % 12) * 2048), trd(p + (s % 12) * 2048 + 512));
        }
        if (s % 4 == 2) p1_piece_b(X, (s % 12) >> 2);
        if (s >= 6) p1_gelu_slot(s - 6, X, pacc, E, vo);
        SBAR();
    }
    p1_gelu_slot(30, X, pacc, E, vo); p1_gelu_slot(31, X, pacc, E, vo);
}
template <bool SWAP>
__device__ __forceinline__ void p1_qkv_chunk(P1Ctx& X, const bf16x8 (&xf)[36], f32x16& acc, P1Tab& T) {
    bf16x8 wf[6];
    lcptr p = X.trbase;
    SBAR();
#pragma unroll
    for (int s = 0; s < 40; ++s) {
        if (s == 0 || s == 12 || s == 24) { p1_top(X, 1, s / 12); p = X.trbase + X.rs; PIN(p); }
        if (s == 12) p1_tab_issue(T);
        if (s == 4) { f32x16 z = {}; acc = SWAP ? MFMA32(xf[0], wf[0], z) : MFMA32(wf[0], xf[0], z); PIN(acc); }
        if (s > 4) { acc = SWAP ? MFMA32(xf[s - 4], wf[(s - 4) % 6], acc) : MFMA32(wf[(s - 4) % 6], xf[s - 4], acc); PIN(acc); }
        if (s < 36) {
            if (s % 4 == 2) p1_piece_a(X, (s % 12) >> 2);
            if ((s & 1) == 0) {
                wf[(s + 1) % 6] = cat8(trd(p + ((s + 1) % 12) * 2048), trd(p + ((s + 1) % 12) * 2048 + 512));
                wf[s % 6] = cat8(trd(p + (s % 12) * 2048), trd(p + (s % 12) * 2048 + 512));
            }
            if (s % 4 == 2) p1_piece_b(X, (s % 12) >> 2);
        }
        SBAR();
    }
}
template <bool SWAP, int J, bool LD>
__device__ __forceinline__ void p1_qkv_stage(P1Ctx& X, lcptr p, const bf16x8 (&xf)[36], f32x16& acc) {
    bf16x8 wf[4];
    PIN(p);
#pragma unroll
    for (int s = 0; s < 12 + 3; ++s) {
        if (s < 12) wf[s & 3] = cat8(trd(p + s * 2048), trd(p + s * 2048 + 512));
        if (s >= 3) { const int u = s - 3;
            if (J == 0 && u == 0) { f32x16 z = {}; acc = SWAP ? MFMA32(xf[0], wf[0], z) : MFMA32(wf[0], xf[0], z); }
            else acc = SWAP ? MFMA32(xf[J * 12 + u], wf[u & 3], acc) : MFMA32(wf[u & 3], xf[J * 12 + u], acc); }
        if (LD && (s == 4 || s == 8 || s == 12)) p1_piece(X, (s >> 2) - 1);
        SBAR();
    }
}
__device__ __forceinline__ void p1_qkv_store(__amdgpu_buffer_rsrc_t rs, u32 off, int lane, const f32x16& a, float sc) {
    u32 w[8];
#pragma unroll
    for (int k = 0; k < 8; ++k) w[k] = cvtpk(a[2 * k] * sc, a[2 * k + 1] * sc);
    st16_sc1((u32x4){w[0], w[1], w[2], w[3]}, rs, off + lane * 16);
    st16_sc1((u32x4){w[4], w[5], w[6], w[7]}, rs, off + 1024 + lane * 16);
}

__device__ __forceinline__ int p1_wg(int rb, int c) { return ((rb >> 3) | ((c >> 2) << 1)) + 8 * ((rb & 7) | ((c & 3) << 3)); }
__device__ __forceinline__ float dpp_ror4(float v) { return __builtin_bit_cast(float, __builtin_amdgcn_update_dpp(0, __builtin_bit_cast(int, v), 0x124, 0xf, 0xf, false)); }
__device__ __forceinline__ float dpp_ror8(float v) { return __builtin_bit_cast(float, __builtin_amdgcn_update_dpp(0, __builtin_bit_cast(int, v), 0x128, 0xf, 0xf, false)); }
__device__ __forceinline__ float xadd16(float v) { auto r = __builtin_amdgcn_permlane16_swap(__float_as_uint(v), __float_as_uint(v), false, false); return __uint_as_float(r[0]) + __uint_as_float(r[1]); }
__device__ __forceinline__ float xadd32(float v) { auto r = __builtin_amdgcn_permlane32_swap(__float_as_uint(v), __float_as_uint(v), false, false); return __uint_as_float(r[0]) + __uint_as_float(r[1]); }
template <int CTRL> __device__ __forceinline__ float dpp_mov(float v) { return __builtin_bit_cast(float, __builtin_amdgcn_update_dpp(0, __builtin_bit_cast(int, v), CTRL, 0xf, 0xf, false)); }
__device__ __forceinline__ float wave_sum(float v) {
    v += dpp_mov<0x121>(v); v += dpp_mov<0x122>(v); v += dpp_mov<0x124>(v); v += dpp_mov<0x128>(v);
    return xadd32(xadd16(v));
}
#define LD16A(dst, voff, sbase) asm volatile("global_load_dwordx4 %0, %1, %2" : "=v"(dst) : "v"(voff), "s"(sbase) : "memory")

__device__ __forceinline__ int p1_qkv_owner(int G) { return G < 24 ? G >> 2 : 6 + (G - 24) / 3; }
__device__ __forceinline__ gu64* attn_flag(const Args& A, int lane) {
    const int xcd = blockIdx.x & 7, idx = blockIdx.x >> 3, qb = idx >> 1, hp = xcd + 8 * (idx & 1), cg0 = (36 * hp) >> 5;
    const int gi = lane / 3, rr = lane - 3 * gi; int rbx = qb - rr; if (rbx < 0) rbx = 0;
    return flag_ptr(A.ctrl, F_QKV, p1_wg(rbx, p1_qkv_owner((lane < 18 ? (gi >> 1) * 18 + cg0 + (gi & 1) : 0))));
}
constexpr int AT_K = 0, AT_V = 12 * 4096, AT_Q = 24 * 4096, AT_LDS = AT_Q + 4 * 4096;
__device__ __forceinline__ void attn_stage(const Args& A, u32 lds0, int wid, int lane) {
    const int xcd = blockIdx.x & 7, idx = blockIdx.x >> 3, qb = idx >> 1, hp = xcd + 8 * (idx & 1), cg0 = (36 * hp) >> 5;
#pragma unroll
    for (int i = 0; i < 7; ++i) {
        const int p = wid + 8 * i;
        const int u = p >> 1, gsel = p & 1;
        const int sec = u < 12 ? 1 : (u < 24 ? 2 : 0), ub = u < 12 ? u : (u < 24 ? u - 12 : u - 24);
        int rb = qb * 4 + ub - (sec ? 8 : 0); if (rb < 0) rb = 0;
        glds16s_off1k_sc1(A.qkvb + (size_t)(rb * 54 + sec * 18 + cg0 + gsel) * 2048, lane * 16, lds0 + u * 4096 + gsel * 2048);
    }
}
__device__ __forceinline__ void p1_phase(const Args& A, char* smem, u64& ep) {
    STAMP_DECL STAMP(0);
    const int tid = threadIdx.x, lane = tid & 63, wid = RFL(tid >> 6);
    const int xcd = blockIdx.x & 7, idx = blockIdx.x >> 3;
    const int rb = (xcd & 1) * 8 + (idx & 7), c = (xcd >> 1) * 4 + (idx >> 3);
    const u32 lds0 = (u32)(uintptr_t)smem;
    const int r8 = lane >> 3, cp = lane & 7;
    const int csw = cp ^ (((r8 >> 1) & 1) << 2);
    int gq0, gq1, gs;
    if (c < 6) { gq0 = 4 * c; gq1 = 4 * c + 2; gs = 0; } else { const int q0 = 24 + 3 * (c - 6), odd = (c - 6) & 1; gq0 = q0 + odd; gq1 = 0; gs = odd ? q0 : q0 + 2; }
    const u32 voffQ = (u32)(wid * 8 + r8) * (NQKV_ * 2) + csw * 16;
    const char* wq0 = (const char*)A.wqkv + gq0 * 64;
    const char* xs = (const char*)A.x + (size_t)rb * 128 * (D_ * 2);
    u32 voffX[2];
#pragma unroll
    for (int i = 0; i < 2; ++i) { const int row = 16 * wid + 8 * i + r8; voffX[i] = (u32)row * (D_ * 2) + ((cp ^ ((row >> 1) & 7)) << 4); }
    const u32 XA = lds0 + P1_SLOT, XB = lds0 + 4 * P1_SLOT;
#define P1_LDQ(sb, j, slot) _Pragma("unroll") for (int k = 0; k < 3; ++k) glds16s((sb) + (size_t)((j) * 192 + k * 64) * (NQKV_ * 2), voffQ, lds0 + (slot) * P1_SLOT + wid * 1024 + k * 8192)
#define P1_LDX(piece, dst) _Pragma("unroll") for (int i = 0; i < 2; ++i) glds16s(xs + (piece) * 128, voffX[i], (dst) + (16 * wid + 8 * i) * 128)
    P1_LDQ(wq0, 0, 0);
    P1_LDX(0, XA); P1_LDX(1, XA + 16384); P1_LDX(2, XA + 32768);
    if (tid < 4) *(LAS u32x4*)((lptr)(size_t)lds0 + P1_WEXT + 288 * 16 + tid * 16) = (u32x4){0u, 0u, 0u, 0u};
    const int rg = wid & 3, ch = wid >> 2;
    P1Ctx X; X.w1 = (const char*)A.w1; X.hrs = mkrsrc(A.hb + (size_t)(rb * 4 + rg) * 144 * 2048, 144 * 2048); X.c = c; X.l16 = lane * 16;
    const int nch = c < 6 ? 7 : 6; X.nch = nch; X.g1first = 9 * c + (c & 1);
    const int m = lane & 31, h = lane >> 5;
#pragma unroll
    for (int k = 0; k < 3; ++k) X.voffW[k] = (u32)(wid * 8 + r8 + 64 * k) * (F_ * 2) + csw * 16;
    X.ldst0 = lds0 + wid * 1024; X.rs = 0; X.ld = X.ldst0; X.lb = X.w1; X.la = X.w1;
    { const int s16 = (lane >> 4) & 1, q = (lane & 15) >> 2, p = lane & 3;
      X.trbase = (lcptr)(size_t)lds0 + (8 * h + q) * 128 + (((4 * ch + 2 * s16 + (p >> 1)) ^ (((q >> 1) & 1) << 2)) * 16) + (p & 1) * 8;
      X.wexb = (lcptr)(size_t)lds0 + (h ? P1_WEXT + 288 * 16 : P1_WEXT + m * 16); X.wexs = h ? 0 : 512; }
    const int wsingle = (c & 1) ? 9 * c : 9 * c + 8;
    asm volatile("s_barrier" ::: "memory");
    P1_LDQ(wq0, 1, 3);
    P1_LDX(3, XB); P1_LDX(4, XB + 16384); P1_LDX(5, XB + 32768);
    {
        const char* wa = c < 6 ? (const char*)A.wqkv + gq1 * 64 : (const char*)A.wqkv + gs * 64; const char* wb = (const char*)A.w1 + ((c & 1) ? 9 * c : 9 * c + 8) * 64;
        const char* pb = xs; u32 po = 0; bool pu = true;
        if (wid < 2) { const int k = wid * 64 + lane, l = (c & 3) * 96 + k; pu = k < 96; po = (u32)(l / 3) * (D_ * 2) + (6 + l % 3) * 128; }
        else if (wid == 2) pu = false;
        else if (wid == 3) { pb = wq0; pu = lane < 24; po = (u32)(384 + (rb & 7) * 24 + lane) * (NQKV_ * 2); }
        else if (wid < 6) { const int k = (wid - 4) * 64 + lane; pb = wa; pu = k < 72; po = (u32)((rb & 7) * 72 + k) * (NQKV_ * 2); }
        else { const int k = (wid - 6) * 64 + lane; pb = wb; pu = k < 72 && c >= 6; po = (u32)((rb & 7) * 72 + k) * (F_ * 2); }
        asm volatile("s_mov_b32 m0, %0" :: "s"(lds0 + P1_CS + 1024) : "memory");
        asm volatile("s_nop 0\n\tglobal_load_lds_dword %0, %1" :: "v"(pu ? po : 0u), "s"(pb) : "memory");
    }
    bf16x8 xf[36];
    f32x16 accA, accB;
    const bool swp = c >= 10;
    const int xrow = rg * 32 + m, xz = (xrow >> 1) & 7;
    X.rcb = (lcptr)(size_t)lds0 + P1_ROW + xrow * 32 + 16;
    X.xeb = h ? (lcptr)(size_t)lds0 + P1_WEXT + 288 * 16 : X.rcb - 16;
    lcptr xa = (lcptr)(size_t)XA + xrow * 128, xb = (lcptr)(size_t)XB + xrow * 128;
#define P1_XF(s0, base) _Pragma("unroll") for (int s = 0; s < 12; ++s) xf[s0 + s] = *(const LAS bf16x8*)((base) + (s >> 2) * 16384 + (((2 * (s & 3) + h) ^ xz) << 4))
#define P1_QS(J, LD, P) do { if (swp) p1_qkv_stage<true, J, LD>(X, P, xf, accA); else p1_qkv_stage<false, J, LD>(X, P, xf, accA); } while (0)
    STAMP(1);
    P1_WAITBAR(10);
    STAMP(2);
    P1_XF(0, xa);
    BARRIER();
    P1_LDX(6, XA); P1_LDX(7, XA + 16384); P1_LDX(8, XA + 32768);
    P1_QS(0, false, X.trbase);
    STAMP(3);
    P1_WAITBAR(7);
    STAMP(4);
    P1_LDQ(wq0, 2, 0);
    const int srow = rb * 128 + c * 8 + wid;
    u32x4 sx0, sx1, sg0, sg1, sw[5]; u64 e0; u32 sb1;
    { const u32 o0 = lane * 16, o1 = 1024 + (lane & 7) * 16;
      const char* xr = (const char*)A.x + (size_t)srow * (D_ * 2);
      LD16A(sx0, o0, xr); LD16A(sx1, o1, xr); LD16A(sg0, o0, (const char*)A.gw); LD16A(sg1, o1, (const char*)A.gw);
      const char* wc = (const char*)A.w1 + (size_t)(72 * wid) * (F_ * 2) + (9 * c + (rb < 9 ? rb : 0)) * 64;
#pragma unroll
      for (int i = 0; i < 5; ++i) { int r = (lane >> 2) + 16 * i; if (r > 71) r = 71; const u32 o = (u32)r * (F_ * 2) + (lane & 3) * 16; LD16A(sw[i], o, wc); }
      asm volatile("global_load_ushort %0, %1, %2" : "=v"(sb1) : "v"((u32)(lane & 31) * 2), "s"((const char*)A.b1 + (9 * c + (rb < 9 ? rb : 0)) * 64) : "memory");
      asm volatile("global_load_dwordx2 %0, %1, %2 sc1" : "=v"(e0) : "v"(0u), "s"(epoch_ptr(A.ctrl)) : "memory"); }
    P1_XF(12, xb);
    P1_QS(1, false, X.trbase + 3 * P1_SLOT);
    STAMP(5);
    P1_WAITBAR(14);
    STAMP(6);
    if (c < 6) { const char* wq1 = (const char*)A.wqkv + gq1 * 64; P1_LDQ(wq1, 0, 3); P1_LDQ(wq1, 1, 4); P1_LDQ(wq1, 2, 5); }
    else {
        const bool hi = csw >= 4; const u32 pitch = hi ? F_ * 2 : NQKV_ * 2;
        const char* lp = (hi ? (const char*)A.w1 + wsingle * 64 : (const char*)A.wqkv + gs * 64) + (size_t)((u32)(wid * 8 + r8) * pitch) + (csw & 3) * 16;
#pragma unroll
        for (int k = 0; k < 9; ++k) { glds16(lp, lds0 + 3 * P1_SLOT + wid * 1024 + k * 8192); lp += pitch * 64; }
    }
    P1_XF(24, xa);
    P1_WAITBAR(20);
    P1_QS(2, false, X.trbase);
    const __amdgpu_buffer_rsrc_t qrs = mkrsrc(A.qkvb + (size_t)(rb * 4 + rg) * 54 * 2048, 54 * 2048);
    p1_qkv_store(qrs, (gq0 + ch) * 2048, lane, accA, gq0 + ch < 18 ? QSCALE : 1.f);
    asm volatile("s_waitcnt vmcnt(11)" : "+v"(sx0), "+v"(sx1), "+v"(sg0), "+v"(sg1), "+v"(sw[0]), "+v"(sw[1]), "+v"(sw[2]), "+v"(sw[3]), "+v"(sw[4]), "+v"(e0), "+v"(sb1) :: "memory");
    ep = ((u64)(u32)RFL((u32)(e0 >> 32)) << 32 | (u32)RFL((u32)e0)) + 1;
    {
        typedef __bf16 bf2 __attribute__((ext_vector_type(2)));
        const bool two = lane < 8;
        const bf2 one2 = __builtin_bit_cast(bf2, 0x3f803f80u);
        float s = 0.f, gsum = 0.f, q2 = 0.f;
#pragma unroll
        for (int j = 0; j < 8; ++j) { const u32 xw = j < 4 ? sx0[j] : (two ? sx1[j - 4] : 0u), gw = j < 4 ? sg0[j] : sg1[j - 4]; const bf2 xv = __builtin_bit_cast(bf2, xw);
            s = __builtin_amdgcn_fdot2_f32_bf16(xv, one2, s, false); gsum = __builtin_amdgcn_fdot2_f32_bf16(xv, __builtin_bit_cast(bf2, gw), gsum, false); q2 = __builtin_amdgcn_fdot2_f32_bf16(xv, xv, q2, false); }
        s = wave_sum(s); gsum = wave_sum(gsum); q2 = wave_sum(q2);
        const float m = s * (1.f / 576.f);
        const float q = fmaxf(q2 - 576.f * m * m, 0.f);
        const float sd = __builtin_amdgcn_sqrtf(q * (1.f / 576.f) + 1e-5f), r = __builtin_amdgcn_rcpf(sd);
        const float eg = __builtin_amdgcn_exp2f(-1.4426950408889634f * gsum), gate = __builtin_amdgcn_rcpf(1.f + eg);
        const float sdg = sd * (1.f + __builtin_amdgcn_exp2f(1.4426950408889634f * gsum));
        const u32 mh = bfbits(-m), ml = bfbits(-m - __uint_as_float(mh << 16)), sh = bfbits(sd), sl = bfbits(sd - __uint_as_float(sh << 16));
        const u32x4 rec = lane == 0 ? (u32x4){mh | (mh << 16), ml | (sh << 16), sl, (u32)ep} : (u32x4){__float_as_uint(r), __float_as_uint(sdg), __float_as_uint(gate), (u32)ep};
        if (lane < 2) st16_sc1(rec, mkrsrc(A.rowrec, S_ * 32), srow * 32 + lane * 16);
        if (rb < 9) {
        float cs[8];
#pragma unroll
        for (int j = 0; j < 8; ++j) cs[j] = 0.f;
#pragma unroll
        for (int i = 0; i < 5; ++i)
#pragma unroll
            for (int j = 0; j < 4; ++j) { const u32 wv = (i < 4 || lane < 32) ? sw[i][j] : 0u; cs[2 * j] += bflo(wv); cs[2 * j + 1] += bfhi(wv); }
#pragma unroll
        for (int j = 0; j < 8; ++j) { float v = cs[j]; v += dpp_ror4(v); v += dpp_ror8(v); cs[j] = xadd32(xadd16(v)); }
        if (lane < 4) { LAS f32x4* d = (LAS f32x4*)((lptr)(size_t)lds0 + P1_CS + wid * 128 + lane * 32); d[0] = (f32x4){cs[0], cs[1], cs[2], cs[3]}; d[1] = (f32x4){cs[4], cs[5], cs[6], cs[7]}; }
        }
    }
    STAMP(7);
    P1_WAITBAR(6);
    if (tid == 0) st_agent(flag_ptr(A.ctrl, F_STAT, blockIdx.x), ep);
    if (wid == 0 && lane < 32 && rb < 9) {
        float t = 0.f;
#pragma unroll
        for (int w = 0; w < 8; ++w) t += *(const LAS float*)((lcptr)(size_t)lds0 + P1_CS + w * 128 + lane * 4);
        const int col = (9 * c + rb) * 32 + lane;
        const u32 chh = bfbits(t), cl = bfbits(t - __uint_as_float(chh << 16)), bb = sb1;
        st16_sc1((u32x4){chh | (cl << 16), chh | (bb << 16), bb, (u32)ep}, mkrsrc(A.wext, F_ * 16), col * 16);
    }
    p1_top3(X);
    {
        const char* w2s = (const char*)A.w2 + (size_t)(576 * xcd) * 1152 + (size_t)idx * 20736;
        const int l = wid * 64 + lane;
        asm volatile("s_mov_b32 m0, %0" :: "s"(lds0 + P1_CS) : "memory");
        if (l < 162) asm volatile("s_nop 0\n\tglobal_load_lds_dword %0, %1" :: "v"((u32)l * 128), "s"(w2s) : "memory");
        if (l < 21) asm volatile("s_nop 0\n\tglobal_load_lds_dword %0, %1" :: "v"((u32)l * 128), "s"((const char*)A.wo + (size_t)blockIdx.x * 2592) : "memory");
    }
    const bool c1q = c < 6 || ch == 0;
    P1Tab T; T.r0 = mkrsrc(A.rowrec, S_ * 32); T.r1 = mkrsrc(A.wext, F_ * 16); T.o0 = (rb * 256 + (tid & 255)) * 16; T.o1 = (288 * c + (tid < 288 ? tid : 0)) * 16;
    bf16x8 wf[6];
#pragma unroll
    for (int k = 0; k < 6; ++k) wf[k] = xf[k];
    if (c1q) {
        if (swp) p1_qkv_chunk<true>(X, xf, accA, T); else p1_qkv_chunk<false>(X, xf, accA, T);
        const int g = c < 6 ? gq1 + ch : gs;
        p1_qkv_store(qrs, g * 2048, lane, accA, g < 18 ? QSCALE : 1.f);
        asm volatile("s_waitcnt vmcnt(8)" : "+v"(T.t0), "+v"(T.t1) :: "memory");
    } else { p1_w1_chunk<true>(X, 1, 0, false, xf, wf, accB, accA, T); asm volatile("s_waitcnt vmcnt(8)" : "+v"(T.t0), "+v"(T.t1) :: "memory"); }
    STAMP(8);
    { const unsigned long long tm0 = __builtin_amdgcn_s_memrealtime();
      while (!__all((tid >= 256 || T.t0[3] == (u32)ep) && (tid >= 288 || T.t1[3] == (u32)ep))) {
          T.t0 = ld16_sc1(T.r0, T.o0); T.t1 = ld16_sc1(T.r1, T.o1);
          if (__builtin_amdgcn_s_memrealtime() - tm0 > 5000000ull) break; }
      if ((tid & 1) == 0) T.t0[3] = 0u;
      T.t1[3] = 0u;
      if (tid < 256) *(LAS u32x4*)((lptr)(size_t)lds0 + P1_ROW + tid * 16) = T.t0;
      if (tid < 288) *(LAS u32x4*)((lptr)(size_t)lds0 + P1_WEXT + tid * 16) = T.t1; }
    P1_WAITBAR(0);
    if (tid == 0) st_agent(flag_ptr(A.ctrl, F_QKV, blockIdx.x), ep);
    const int iend = (nch == 7 && ch == 1) ? 6 : nch;
#define P1_G(i) ((i) == 1 ? wsingle : p1_w1g(X, i) + ((i) < 6 ? ch : 0))
#define P1_ST1
#define P1_ST2
    u64 fq = 0;
    for (int i = 2; i < iend; i += 2) {
        p1_w1_chunk<false>(X, i, P1_G(i - 1), i > 2 || !c1q, xf, wf, accA, accB, T); P1_ST1
        if (wid == 0) { if (i == 2) poll_issue(fq, attn_flag(A, lane)); else if (i == 4) { poll_check(fq, attn_flag(A, lane), lane < 18, ep); } }
        if (i + 1 < iend) p1_w1_chunk<false>(X, i + 1, P1_G(i), true, xf, wf, accB, accA, T); P1_ST2
    }
    if (iend < nch) { P1_WAITBAR(12); P1_WAITBAR(9); P1_WAITBAR(6); }
    BARRIER();
    attn_stage(A, lds0, wid, lane);
    int ie = iend; asm volatile("" : "+s"(ie));
    if ((ie & 1) != 0) accB = accA;
    { Epi E; const int lg = P1_G(ie - 1); const u32 vo = X.l16 + (u32)lg * 2048;
      const bf16x8 we = *(const LAS bf16x8*)(X.wexb + (lg - 9 * X.c) * X.wexs), xe = *(const LAS bf16x8*)X.xeb;
      p1_consts(X, E);
      Epi E2 = E;
#pragma unroll
      for (int s = 0; s < 4; ++s) accB = MFMA32(wf[(s + 2) % 6], xf[32 + s], accB);
      accB = MFMA32(we, xe, accB);
#pragma unroll
      for (int s = 0; s < 16; ++s) { p1_gelu_slot(s, X, accB, E, vo); p1_gelu_slot(s + 16, X, accB, E2, vo); } }
    STAMP(15);
    STAMP_OUT(A.stamps, 16);
}
__device__ __forceinline__ int qk_off(int c) { const int cc = c & 31; return (c >> 5) * 2048 + (2 * (cc >> 4) + ((cc >> 2) & 1)) * 512 + (((cc >> 3) & 1) * 4 + (cc & 3)) * 2; }
__device__ __forceinline__ float xmax16(float v) { auto r = __builtin_amdgcn_permlane16_swap(__float_as_uint(v), __float_as_uint(v), false, false); return fmaxf(__uint_as_float(r[0]), __uint_as_float(r[1])); }
__device__ __forceinline__ float xmax32(float v) { auto r = __builtin_amdgcn_permlane32_swap(__float_as_uint(v), __float_as_uint(v), false, false); return fmaxf(__uint_as_float(r[0]), __uint_as_float(r[1])); }
__device__ __forceinline__ float max3f(float a, float b, float c) { return __builtin_fmaxf(__builtin_fmaxf(a, b), c); }

template <bool EARLY>
__device__ __forceinline__ void attn_mask(f32x4& s, int t, int dq, int tneg) {
    if (t < 2) {
#pragma unroll
        for (int r = 0; r < 4; ++r) s[r] = (dq - 16 * t - r <= 255) ? s[r] : -INFINITY;
    } else if (t >= 16) {
#pragma unroll
        for (int r = 0; r < 4; ++r) s[r] = (dq - 16 * t - r >= 0) ? s[r] : -INFINITY;
    }
    if (EARLY && t < 16) {
        const bool dead = t < tneg;
#pragma unroll
        for (int r = 0; r < 4; ++r) s[r] = dead ? -INFINITY : s[r];
    }
}
#define ATS_ARG
#define ATS_PASS
template <bool EARLY>
__device__ __forceinline__ void attn_units(lcptr L, int c00  , int cones, int w, int lane, int Q0, float gate, __amdgpu_buffer_rsrc_t ors, int head0, gu64* fhp, u64 ep ATS_ARG) {
    u64 fh = 0; if (w == 0) poll_issue(fh, fhp);
    const int i16 = lane & 15, g = lane >> 4;
    const int lb0 = w >> 1;
    const int q = Q0 + 16 * w + i16, kb = Q0 - 256 + lb0 * 32 + 4 * g, dq = q - kb, tneg = (256 - Q0 - lb0 * 32) >> 4;
    bf16x8 kf[2][18], qf[2];
    f32x4 nm[2];
#pragma unroll
    for (int u = 0; u < 2; ++u) {
        const int c0 = c00 + 18 * u;
        const int p2 = c0 & 2, op = qk_off(c0 + p2 + 4 * g), os = qk_off(p2 ? c0 - 2 : c0 + 16);
        lcptr qp = L + AT_Q + (w >> 1) * 4096 + ((w & 1) * 16 + i16) * 16;
        const u32x2 qa = *(const LAS u32x2*)(qp + op), qc = *(const LAS u32x2*)(qp + os);
        qf[u] = __builtin_bit_cast(bf16x8, (u32x4){qa[0], qa[1], (g == 0 && !p2) ? qc[0] : 0u, (g == 0 && p2) ? qc[1] : 0u});
        lcptr kb0 = L + AT_K + lb0 * 4096 + i16 * 16;
        lcptr k0 = kb0 + op, k1 = kb0 + os;
        PIN(k0); PIN(k1);
#pragma unroll
        for (int t = 0; t < 18; ++t) { const int o = (t >> 1) * 4096 + (t & 1) * 256;
            const u32x2 ka = *(const volatile LAS u32x2*)(k0 + o), kc = *(const volatile LAS u32x2*)(k1 + o);
            kf[u][t] = __builtin_bit_cast(bf16x8, (u32x4){ka[0], ka[1], kc[0], kc[1]}); }
    }
    lcptr va[2], vb[2];
#pragma unroll
    for (int u = 0; u < 2; ++u) {
        const int c0 = c00 + 18 * u, ca = c0 + i16, cb = (i16 < 2) ? c0 + 16 + i16 : (i16 == 2 ? cones : c0);
        va[u] = L + AT_V + lb0 * 4096 + (ca >> 5) * 2048 + (g & 1) * 512 + (ca & 31) * 16 + (g >> 1) * 8;
        vb[u] = L + AT_V + lb0 * 4096 + (cb >> 5) * 2048 + (g & 1) * 512 + (cb & 31) * 16 + (g >> 1) * 8;
        PIN(va[u]); PIN(vb[u]);
    }
    SBAR();
    STAMP(5);
#pragma unroll
    for (int u = 0; u < 2; ++u) {
        f32x4 s[18];
#pragma unroll
        for (int t = 0; t < 18; ++t) s[t] = MFMA16(kf[u][t], qf[u], ((f32x4){0.f, 0.f, 0.f, 0.f}));
#pragma unroll
        for (int t = 0; t < 18; ++t) attn_mask<EARLY>(s[t], t, dq, tneg);
        float m0 = max3f(s[0][0], s[0][1], s[0][2]), m1 = max3f(s[0][3], s[1][0], s[1][1]);
        m0 = max3f(m0, s[1][2], s[1][3]);
#pragma unroll
        for (int t = 2; t < 18; t += 2) { m0 = max3f(m0, s[t][0], s[t][1]); m1 = max3f(m1, s[t][2], s[t][3]); m0 = max3f(m0, s[t + 1][0], s[t + 1][1]); m1 = max3f(m1, s[t + 1][2], s[t + 1][3]); }
        const float m = -xmax32(xmax16(fmaxf(m0, m1)));
        nm[u] = (f32x4){m, m, m, m};
    }
    if (w == 0) { WAIT_VM0(); poll_check(fh, fhp, lane < 4, ep);
    }
    STAMP(6);
    u32x2 vf[3][4]; f32x4 sc2[2][2];
#define AT_VRD(k) do { const int b_ = (k) >> 1, u_ = (k) & 1; vf[(k) % 3][0] = *(const volatile LAS u32x2*)(va[u_] + b_ * 4096); vf[(k) % 3][1] = *(const volatile LAS u32x2*)(va[u_] + b_ * 4096 + 1024); \
        vf[(k) % 3][2] = *(const volatile LAS u32x2*)(vb[u_] + b_ * 4096); vf[(k) % 3][3] = *(const volatile LAS u32x2*)(vb[u_] + b_ * 4096 + 1024); } while (0)
#define AT_QK(k) do { const int b_ = (k) >> 1, u_ = (k) & 1; sc2[(k) & 1][0] = MFMA16(kf[u_][2 * b_], qf[u_], nm[u_]); sc2[(k) & 1][1] = MFMA16(kf[u_][2 * b_ + 1], qf[u_], nm[u_]); } while (0)
    f32x4 oa[2] = {{0.f, 0.f, 0.f, 0.f}, {0.f, 0.f, 0.f, 0.f}}, obb[2] = {{0.f, 0.f, 0.f, 0.f}, {0.f, 0.f, 0.f, 0.f}};
    AT_VRD(0); AT_VRD(1); AT_QK(0);
    SBAR();
#pragma unroll
    for (int k = 0; k < 18; ++k) {
        const int b = k >> 1, u = k & 1;
        f32x4 s0 = sc2[k & 1][0], s1 = sc2[k & 1][1];
        if (k + 1 < 18) { const int b_ = (k + 1) >> 1, u_ = (k + 1) & 1; sc2[(k + 1) & 1][0] = MFMA16(kf[u_][2 * b_], qf[u_], nm[u_]); sc2[(k + 1) & 1][1] = MFMA16(kf[u_][2 * b_ + 1], qf[u_], nm[u_]); PIN(sc2[(k + 1) & 1][0]); PIN(sc2[(k + 1) & 1][1]); }
        if (k + 2 < 18) AT_VRD(k + 2);
        attn_mask<EARLY>(s0, 2 * b, dq, tneg); attn_mask<EARLY>(s1, 2 * b + 1, dq, tneg);
        u32x4 pw;
        pw[0] = cvtpk_c(__builtin_amdgcn_exp2f(s0[0]), __builtin_amdgcn_exp2f(s0[1])); pw[1] = cvtpk_c(__builtin_amdgcn_exp2f(s0[2]), __builtin_amdgcn_exp2f(s0[3]));
        pw[2] = cvtpk_c(__builtin_amdgcn_exp2f(s1[0]), __builtin_amdgcn_exp2f(s1[1])); pw[3] = cvtpk_c(__builtin_amdgcn_exp2f(s1[2]), __builtin_amdgcn_exp2f(s1[3]));
        const bf16x8 pf = __builtin_bit_cast(bf16x8, pw);
        oa[u] = MFMA16(__builtin_bit_cast(bf16x8, ((u32x4){vf[k % 3][0][0], vf[k % 3][0][1], vf[k % 3][1][0], vf[k % 3][1][1]})), pf, oa[u]);
        obb[u] = MFMA16(__builtin_bit_cast(bf16x8, ((u32x4){vf[k % 3][2][0], vf[k % 3][2][1], vf[k % 3][3][0], vf[k % 3][3][1]})), pf, obb[u]);
        PIN(oa[u]); PIN(obb[u]);
        SBAR();
    }
    STAMP(7);
    auto off = [](int c) { const int cc = c & 31; return (c >> 5) * 2048 + (2 * (cc >> 4) + ((cc >> 2) & 1)) * 512 + (((cc >> 3) & 1) * 4 + (cc & 3)) * 2; };
    const u32 base = (u32)(q >> 5) * 18 * 2048 + (q & 31) * 16;
    const float sc0 = gate * __builtin_amdgcn_rcpf(__shfl(obb[0][2], i16)), sc1 = gate * __builtin_amdgcn_rcpf(__shfl(obb[1][2], i16));
    const u32 p1 = cvtpk(oa[1][0] * sc1, oa[1][1] * sc1), r1 = cvtpk(oa[1][2] * sc1, oa[1][3] * sc1), t1 = cvtpk(obb[1][0] * sc1, obb[1][1] * sc1);
    const u32 nx = (u32)__shfl((int)(g == 0 ? t1 : p1), (lane + 16) & 63);
    const int col0 = 18 * head0;
    typedef u32 u32x2s __attribute__((ext_vector_type(2)));
    __builtin_amdgcn_raw_buffer_store_b64((u32x2s){cvtpk(oa[0][0] * sc0, oa[0][1] * sc0), cvtpk(oa[0][2] * sc0, oa[0][3] * sc0)}, ors, base + off(col0 + 4 * g), 0, 16);
    __builtin_amdgcn_raw_buffer_store_b64((u32x2s){r1, nx}, ors, base + off(col0 + 20 + 4 * g), 0, 16);
    if (g == 0) __builtin_amdgcn_raw_buffer_store_b64((u32x2s){cvtpk(obb[0][0] * sc0, obb[0][1] * sc0), p1}, ors, base + off(col0 + 16), 0, 16);
}

__device__ __forceinline__ int attn_wg(int qb, int hp) { return (hp & 7) + 8 * (2 * qb + (hp >> 3)); }
__device__ __forceinline__ void acquire_agent() { __builtin_amdgcn_fence(__ATOMIC_ACQUIRE, "agent"); WAIT_VM0(); }

__device__ __forceinline__ void attn_phase(const Args& A, char* smem, u64 ep) {
    STAMP_DECL STAMP(0);
    const int tid = threadIdx.x, lane = tid & 63, wid = RFL(tid >> 6);
    const int xcd = blockIdx.x & 7, idx = blockIdx.x >> 3;
    const int qb = idx >> 1, hp = xcd + 8 * (idx & 1);
    const int Q0 = qb * 128, cg0 = (36 * hp) >> 5, c0 = 36 * hp - 32 * cg0;
    const u32 lds0 = (u32)(uintptr_t)smem;
    gu64* fhp;
    { const int sp = blockIdx.x & 7, mt = (blockIdx.x >> 3) & 7; fhp = flag_ptr(A.ctrl, F_H, p1_wg(2 * mt + (lane & 1), 2 * sp + ((lane >> 1) & 1))); }
    const int q = Q0 + 16 * wid + (lane & 15);
    const float gate = __uint_as_float(__builtin_amdgcn_raw_buffer_load_b32(mkrsrc(A.rowrec, S_ * 32), q * 32 + 24, 0, 16));
    WAIT_VM0(); BARRIER();
    if (tid == 0) st_agent(flag_ptr(A.ctrl, F_H, blockIdx.x), ep);
    STAMP(2);
    const int cones = (c0 + 36) & 63;
    if (tid < 48) { const int kb = tid >> 2, pc = tid & 3; *(LAS u32x4*)((lptr)(size_t)lds0 + AT_V + kb * 4096 + (cones >> 5) * 2048 + pc * 512 + (cones & 31) * 16) = (u32x4){0x3f803f80u, 0x3f803f80u, 0x3f803f80u, 0x3f803f80u}; }
    BARRIER();
    STAMP(3);
    const __amdgpu_buffer_rsrc_t ors = mkrsrc(A.ob, S_ * D_ * 2);
    if (qb < 2) attn_units<true>((lcptr)(size_t)lds0, c0, cones, wid, lane, Q0, gate, ors, 2 * hp, fhp, ep ATS_PASS); else attn_units<false>((lcptr)(size_t)lds0, c0, cones, wid, lane, Q0, gate, ors, 2 * hp, fhp, ep ATS_PASS);
    STAMP(8);
    STAMP(4);
    STAMP_OUT(A.stamps + (2u << 17), 9);
}
constexpr int P3_A = 32768, P3_B = 64 * 288, P3_SLOT = P3_A + P3_B, P3_LDS = 3 * P3_SLOT;
struct P3Ctx {
    const char* pA; const char* pB;
    const char* oA; const char* oB;
    u32 aStride;
    int tl, nst; u32 sl;
    u32 dA, dB;
    u32 vA, vA2, vB0, vB1, vB2;
    lcptr ab, bb;
};
__device__ __forceinline__ void p3_part_a(const P3Ctx& X, int k) {
    const u32 base = k < 2 ? X.dA : X.dB; const int add = k == 1 ? 2048 : (k == 3 ? 1024 : (k == 4 ? 2048 : 0));
    asm volatile("s_add_u32 m0, %0, %1" :: "s"(base + X.sl), "n"(add) : "memory", "scc");
}
__device__ __forceinline__ void p3_part_b(P3Ctx& X, int k) {
    if (k == 0) asm volatile("global_load_lds_dwordx4 %0, %1 sc1\n\tglobal_load_lds_dwordx4 %0, %1 offset:1024 sc1" :: "v"(X.vA), "s"(X.pA) : "memory");
    if (k == 1) asm volatile("global_load_lds_dwordx4 %0, %1 sc1\n\tglobal_load_lds_dwordx4 %0, %1 offset:1024 sc1" :: "v"(X.vA2), "s"(X.pA) : "memory");
    if (k == 2) asm volatile("global_load_lds_dwordx4 %0, %1" :: "v"(X.vB0), "s"(X.pB) : "memory");
    if (k == 3) asm volatile("global_load_lds_dwordx4 %0, %1" :: "v"(X.vB1), "s"(X.pB) : "memory");
    if (k == 4) { asm volatile("s_mov_b64 exec, 0xffff\n\tglobal_load_lds_dwordx4 %0, %1\n\ts_mov_b64 exec, -1" :: "v"(X.vB2), "s"(X.pB) : "memory");
        X.tl += 1; const bool sw = X.tl == 9, adv = X.tl < X.nst;
        X.pA = sw ? X.oA : X.pA + (adv ? 4096 : 0); X.pB = sw ? X.oB : X.pB + (adv ? 64 * 1152 : 0); X.sl = X.sl == 2 * P3_SLOT ? 0 : X.sl + P3_SLOT; }
}
__device__ __forceinline__ void p3_load_part(P3Ctx& X, int k) { p3_part_a(X, k); asm volatile("s_nop 0"); p3_part_b(X, k); }
struct P3Frag { bf16x8 h[4]; bf16x8 w[5]; };
__device__ __forceinline__ void p3_waitbar() { asm volatile("s_waitcnt vmcnt(7) lgkmcnt(0)\n\ts_barrier" ::: "memory"); }
template <int NT, int HF, bool RD, bool MM>
__device__ __forceinline__ void p3_half(P3Ctx& X, int t, u32 so, P3Frag& R, const P3Frag& P, f32x4 (&acc)[4][5]) {
    lcptr a = X.ab + so + HF * 2048, b = X.bb + so + HF * (32 * 288);
    if (RD) { if (HF == 0) p3_waitbar(); PIN(a); PIN(b); }
    s16x4 lo;
    SBAR();
#pragma unroll
    for (int i = 0; i < 4 * NT; ++i) {
        if (MM) acc[i & 3][i >> 2] = MFMA16(P.w[i >> 2], P.h[i & 3], acc[i & 3][i >> 2]);
        constexpr int S0 = HF == 0 ? 2 : 1, S1 = HF == 0 ? 8 : 6, S2 = HF == 0 ? -1 : 10;
        const int part = i == S0 ? (HF == 0 ? 0 : 2) : (i == S1 ? (HF == 0 ? 1 : 3) : (i == S2 ? 4 : -1));
        if (RD) {
            if (part >= 0) p3_part_a(X, part);
            if (i < 4) R.h[i] = *(const LAS bf16x8*)(a + (i >> 1) * 4096 + (i & 1) * 256);
            else if (i < 4 + 2 * NT) { const int j = i - 4; if (j & 1) R.w[j >> 1] = cat8(lo, trd(b + (j >> 1) * 32 + 8 * 288)); else lo = trd(b + (j >> 1) * 32); }
            if (part >= 0) p3_part_b(X, part);
        }
        SBAR();
    }
}
template <int NT>
__device__ __forceinline__ void p3_loop(P3Ctx& X, int nhalf, f32x4 (&acc)[4][5], bool w0, gu64* fo, bool fo_use, u64 ep, gu64* own_o) {
    u64 fv = 0;
    asm volatile("s_waitcnt vmcnt(14)\n\ts_barrier" ::: "memory");
    if (threadIdx.x == 0) st_agent(own_o, ep);
    P3Frag F0, F1;
    u32 so = 0;
    p3_half<NT, 0, true, false>(X, 0, so, F0, F1, acc);
    p3_half<NT, 1, true, true>(X, 0, so, F1, F0, acc);
    int t = 1;
    for (; 2 * t + 1 < nhalf; ++t) {
        so = so == 2 * P3_SLOT ? 0 : so + P3_SLOT;
        if (w0) { if (t == 2) poll_issue(fv, fo); else if (t == 4) poll_check(fv, fo, fo_use, ep); }
        p3_half<NT, 0, true, true>(X, t, so, F0, F1, acc);
        p3_half<NT, 1, true, true>(X, t, so, F1, F0, acc);
    }
    if (2 * t < nhalf) {
        so = so == 2 * P3_SLOT ? 0 : so + P3_SLOT;
        p3_half<NT, 0, true, true>(X, t, so, F0, F1, acc);
        p3_half<NT, 1, false, true>(X, t, so, F1, F0, acc);
    } else p3_half<NT, 0, false, true>(X, t, so, F0, F1, acc);
}
__device__ __forceinline__ void p3_phase(const Args& A, char* smem, u64 ep) {
    STAMP_DECL STAMP(0);
    const int tid = threadIdx.x, lane = tid & 63, wid = RFL(tid >> 6);
    const int sp = blockIdx.x & 7, idx = blockIdx.x >> 3;
    const int mt = idx & 7, nt = idx >> 3;
    const u32 lds0 = (u32)(uintptr_t)smem;
    const int mq = wid >> 1, nh = (wid & 1) ^ (wid >> 2);
    const int i16 = lane & 15, kq = lane >> 4;
    P3Ctx X;
    const int no = sp < 2 ? 1 : (sp < 4 ? 2 : 3), o0 = sp < 2 ? sp : (sp < 4 ? 2 * sp - 2 : 3 * sp - 6);
    STAMP(4);
    const int nhalf = 18 + no; X.nst = (nhalf + 1) >> 1; X.tl = 0; X.sl = 0;
    X.pA = A.hb + (size_t)((mt * 8 + wid) * 144 + 18 * sp) * 2048;
    X.oA = A.ob + (size_t)((mt * 8 + wid) * 18 + o0) * 2048;
    X.pB = (const char*)A.w2 + (size_t)(576 * sp) * 1152 + 288 * nt;
    X.oB = (const char*)A.wo + (size_t)(32 * o0) * 1152 + 288 * nt;
    X.dA = lds0 + wid * 4096; X.dB = lds0 + P3_A + wid * 144 * 16;
    X.vA = lane * 16; X.vA2 = lane * 16 + 2048;
    { const int e0 = wid * 144 + lane, e1 = e0 + 64, e2 = e0 + 128;
      X.vB0 = (e0 / 18) * 1152 + (e0 % 18) * 16; X.vB1 = (e1 / 18) * 1152 + (e1 % 18) * 16; X.vB2 = (e2 / 18) * 1152 + (e2 % 18) * 16; }
    X.ab = (lcptr)(size_t)lds0 + (mq * 2) * 4096 + kq * 512 + i16 * 16;
    X.bb = (lcptr)(size_t)lds0 + P3_A + (16 * (kq >> 1) + 4 * (kq & 1) + (i16 >> 2)) * 288 + (nh ? 5 * 32 : 0) + (i16 & 3) * 8;
    PIN(X.vA); PIN(X.vA2); PIN(X.vB0); PIN(X.vB1); PIN(X.vB2); PIN(X.ab); PIN(X.bb);
    asm volatile("" : "+s"(X.pA), "+s"(X.pB), "+s"(X.oA), "+s"(X.oB), "+s"(X.dA), "+s"(X.dB));
    BARRIER();
#pragma unroll
    for (int t = 0; t < 2; ++t)
#pragma unroll
        for (int k = 0; k < 5; ++k) p3_load_part(X, k);
    f32x4 acc[4][5];
#pragma unroll
    for (int m = 0; m < 4; ++m)
#pragma unroll
        for (int n = 0; n < 5; ++n) acc[m][n] = (f32x4){0.f, 0.f, 0.f, 0.f};
    STAMP(1);
    const int hp0 = (32 * o0) / 36, hp1 = (32 * (o0 + no) - 1) / 36, hp = hp0 + (lane >> 1);
    gu64* fo = flag_ptr(A.ctrl, F_O, attn_wg(2 * mt + (lane & 1), hp <= hp1 ? hp : hp0));
    if (nh == 0) p3_loop<5>(X, nhalf, acc, wid == 0, fo, hp <= hp1, ep, flag_ptr(A.ctrl, F_O, blockIdx.x)); else p3_loop<4>(X, nhalf, acc, wid == 0, fo, hp <= hp1, ep, flag_ptr(A.ctrl, F_O, blockIdx.x));
    STAMP(2);
    const u32 tile = (u32)(mt * 4 + nt) * 8 * (72 * 1024);
    const __amdgpu_buffer_rsrc_t prs = mkrsrc(A.part + tile, 8 * 72 * 1024);
    const int NTN = nh ? 4 : 5;
    u32x4 pk[2][5];
#pragma unroll
    for (int pr = 0; pr < 2; ++pr)
#pragma unroll
        for (int n = 0; n < 5; ++n)
            if (n < NTN) { pk[pr][n] = (u32x4){cvtpk_c(acc[2 * pr][n][0], acc[2 * pr][n][1]), cvtpk_c(acc[2 * pr][n][2], acc[2 * pr][n][3]),
                                               cvtpk_c(acc[2 * pr + 1][n][0], acc[2 * pr + 1][n][1]), cvtpk_c(acc[2 * pr + 1][n][2], acc[2 * pr + 1][n][3])};
                if (mq * 2 + pr != sp) st16_sc1(pk[pr][n], prs, sp * (72 * 1024) + ((mq * 2 + pr) * 9 + (nh ? 5 : 0) + n) * 1024 + lane * 16); }
    WAIT_VM0(); BARRIER();
    if (tid == 0) st_agent(flag_ptr(A.ctrl, F_PART, idx) + sp, ep);
#pragma unroll
    for (int pr = 0; pr < 2; ++pr)
#pragma unroll
        for (int n = 0; n < 5; ++n)
            if (n < NTN && mq * 2 + pr == sp) *(LAS u32x4*)((lptr)(size_t)lds0 + ((nh ? 5 : 0) + n) * 1024 + lane * 16) = pk[pr][n];
    BARRIER();
    STAMP(3);
    if (tid < 320) {
        const bool act = tid < 288;
        const int ck0 = act ? tid : 0;
        u32x2 bb[2]; float og[2][2];
#pragma unroll
        for (int r = 0; r < 2; ++r) { const int ck = ck0 + 288 * r, n = ck >> 6, ln = ck & 63;
            bb[r] = *(const u32x2*)(A.b2 + nt * 144 + n * 16 + (ln >> 4) * 4);
#pragma unroll
            for (int e = 0; e < 2; ++e) og[r][e] = 1.f - __uint_as_float(__builtin_amdgcn_raw_buffer_load_b32(mkrsrc(A.rowrec, S_ * 32), (mt * 256 + 32 * sp + 16 * e + (ln & 15)) * 32 + 24, 0, 16)); }
        poll_flags(flag_ptr(A.ctrl, F_PART, idx) + (lane & 7), lane < 8, ep);
        u32x4 pv[2][8];
#pragma unroll
        for (int r = 0; r < 2; ++r)
#pragma unroll
            for (int s = 0; s < 8; ++s) { const int ck = ck0 + 288 * r; pv[r][s] = ld16_sc1(prs, s == sp ? 0x40000000u : (u32)(s * (72 * 1024) + (sp * 9 + (ck >> 6)) * 1024 + (ck & 63) * 16)); }
#pragma unroll
        for (int r = 0; r < 2; ++r) {
            const u32x4 own = *(const LAS u32x4*)((lcptr)(size_t)lds0 + (ck0 + 288 * r) * 16);
            float v[8];
#pragma unroll
            for (int j = 0; j < 4; ++j) { v[2 * j] = bflo(own[j]); v[2 * j + 1] = bfhi(own[j]); }
#pragma unroll
            for (int s = 0; s < 8; ++s)
#pragma unroll
                for (int j = 0; j < 4; ++j) { v[2 * j] += bflo(pv[r][s][j]); v[2 * j + 1] += bfhi(pv[r][s][j]); }
            const int ck = ck0 + 288 * r, n = ck >> 6, ln = ck & 63;
#pragma unroll
            for (int e = 0; e < 2; ++e) {
                const int row = mt * 256 + 32 * sp + 16 * e + (ln & 15), col = nt * 144 + n * 16 + (ln >> 4) * 4;
                u32x2 o; o[0] = cvtpk(v[4 * e] + og[r][e] * bflo(bb[r][0]), v[4 * e + 1] + og[r][e] * bfhi(bb[r][0])); o[1] = cvtpk(v[4 * e + 2] + og[r][e] * bflo(bb[r][1]), v[4 * e + 3] + og[r][e] * bfhi(bb[r][1]));
                if (act) *(u32x2*)(A.out + (size_t)row * D_ + col) = o;
            }
        }
    } else if (blockIdx.x == 0 && wid == 7) {
        const unsigned long long t0 = __builtin_amdgcn_s_memrealtime();
        for (;;) {
            u64 v[4];
#pragma unroll
            for (int i = 0; i < 4; ++i) v[i] = ld_agent(flag_ptr(A.ctrl, F_STAT, lane + 64 * i));
            if (__all(v[0] == ep && v[1] == ep && v[2] == ep && v[3] == ep)) break;
            if (__builtin_amdgcn_s_memrealtime() - t0 > 5000000ull) break;
            __builtin_amdgcn_s_sleep(1);
        }
        if (lane == 0) st_agent(epoch_ptr(A.ctrl), ep);
    }
    STAMP(5);
    STAMP_OUT(A.stamps + (1u << 17), 6);
}
constexpr int MEGA_LDS = 163840;
static_assert(P1_LDS <= MEGA_LDS && AT_LDS <= MEGA_LDS && P3_LDS <= MEGA_LDS, "LDS");
__global__ __launch_bounds__(512, 2) void mega_kernel(Args A) {
    extern __shared__ __attribute__((aligned(16))) char smem[];
    u64 ep;
    typedef __attribute__((address_space(4))) const u64* ArgP;
#if defined(__HIP_DEVICE_COMPILE__)
    ArgP ap = (ArgP)__builtin_amdgcn_kernarg_segment_ptr();
#else
    ArgP ap = nullptr;
#endif
#define LOAD_ARGS(a) Args a; { asm volatile("" : "+s"(ap)); u64 w_[17]; _Pragma("unroll") for (int i_ = 0; i_ < 17; ++i_) w_[i_] = ap[i_]; __builtin_memcpy(&a, w_, sizeof(Args)); }
    static_assert(sizeof(Args) == 17 * 8, "Args: 17 pointers");
    { LOAD_ARGS(a) p1_phase(a, smem, ep); }
    { LOAD_ARGS(a) attn_phase(a, smem, ep); }
    { LOAD_ARGS(a) p3_phase(a, smem, ep); }
}
extern "C" void kernel_launch(void* const* d_in, const int* in_sizes, int n_in, void* d_out, int out_size, void* d_ws, size_t ws_size, hipStream_t stream) {
    char* ws = (char*)d_ws;
    Args A;
    A.x = (const u16*)d_in[0]; A.wqkv = (const u16*)d_in[1]; A.wo = (const u16*)d_in[2]; A.w1 = (const u16*)d_in[4]; A.b1 = (const u16*)d_in[5];
    A.w2 = (const u16*)d_in[6]; A.b2 = (const u16*)d_in[7]; A.gw = (const u16*)d_in[8];
    A.rowrec = (u32x4*)ws;
    A.wext = (u32x4*)(ws + 65536);
    char* big = ws + 147456;
    A.qkvb = big;
    A.hb = big + 7077888;
    A.ob = big + 7077888 + 18874368;
    A.part = big + 7077888 + 18874368 + 2359296;
    A.ctrl = (u64*)(big + 7077888 + 18874368 + 2359296 + 18874368);
    A.out = (u16*)d_out;
    A.stamps = (unsigned long long*)(ws + (200u << 20));
    (void)hipFuncSetAttribute((const void*)mega_kernel, hipFuncAttributeMaxDynamicSharedMemorySize, MEGA_LDS);
    mega_kernel<<<256, 512, MEGA_LDS, stream>>>(A);
}
```
